```python
import torch

def _softmax(x, axis=-1):
    return torch.softmax(x, dim=axis)
def _gelu(x):
    return torch.nn.functional.gelu(x, approximate="tanh")
def _silu(x):
    return torch.nn.functional.silu(x)
def _sigmoid(x):
    return torch.sigmoid(x)
def _layernorm(x, axis=-1, eps=1e-5):
    m = x.mean(dim=axis, keepdim=True)
    v = x.var(dim=axis, keepdim=True, unbiased=False)
    return (x - m) / torch.sqrt(v + eps)
def _scatter_add(tgt, idx, src, axis=0):
    return tgt.clone().index_add_(axis, idx, src)
def _segment_sum(src, seg, n):
    out = torch.zeros((n,) + src.shape[1:], dtype=src.dtype)
    return out.index_add_(0, seg, src)
def _pad(x, pw):
    flat = tuple(v for pair in reversed(pw) for v in pair)
    return torch.nn.functional.pad(x, flat)

kernel_name = 'concat_heads__linear_attn_block__ssm_conv_s4d__prompt'

def setup_inputs():

    _w = (lambda a: (lambda t: t.to(torch.bfloat16) if t.is_floating_point() else t)(torch.as_tensor(a)))
    def _A_setup():
        B, S, D = 1, 2048, 640
        return {
            "x": _w((lambda *s: torch.randn(*s, dtype=torch.bfloat16) * 0.05)(B, S, D)),
            "wqkv": _w((lambda *s: torch.randn(*s, dtype=torch.bfloat16) * 0.4)(D, 3 * D)),
            "wo": _w((lambda *s: torch.randn(*s, dtype=torch.bfloat16) * 0.05)(D, D)),
        }
    _A_in = _A_setup()

    def _B_setup():
        Bsz, L, D, N = 1, 2048, 640, 192
        return {
            "u": _w((lambda *s: torch.randn(*s, dtype=torch.bfloat16) * 0.05)(Bsz, L, D)),
            "log_A_real": _w((lambda *s: torch.randn(*s, dtype=torch.bfloat16) * 6.4)(D, N) * 0.5),
            "A_imag": _w((lambda *s: torch.randn(*s, dtype=torch.bfloat16) * 0.05)(D, N) * 3.14),
            "C_real": _w((lambda *s: torch.randn(*s, dtype=torch.bfloat16) * 0.1)(D, N) / N ** 0.5),
            "C_imag": _w((lambda *s: torch.randn(*s, dtype=torch.bfloat16) * 0.1)(D, N) / N ** 0.5),
            "log_dt": _w((lambda *s: torch.randn(*s, dtype=torch.bfloat16) * 0.05)(D) * 0.5 - 4.0),
            "D_skip": _w((lambda *s: torch.randn(*s, dtype=torch.bfloat16) * 0.05)(D)),
        }
    _B_in = _B_setup()

    return {
        "A_x": _A_in["x"],
        "A_wqkv": _A_in["wqkv"],
        "A_wo": _A_in["wo"],
        "B_u": _B_in["u"],
        "B_log_A_real": _B_in["log_A_real"],
        "B_A_imag": _B_in["A_imag"],
        "B_C_real": _B_in["C_real"],
        "B_C_imag": _B_in["C_imag"],
        "B_log_dt": _B_in["log_dt"],
        "B_D_skip": _B_in["D_skip"],
    }

def reference(A_x, A_wqkv, A_wo, B_u, B_log_A_real, B_A_imag, B_C_real, B_C_imag, B_log_dt, B_D_skip):
    def _A_ref(x, wqkv, wo):
        B, S, D = x.shape
        H = 32; Dh = D // H
        qkv = (lambda x, *d: x.permute(*d))((x @ wqkv).reshape(B, S, 3, H, Dh), 2, 0, 3, 1, 4)
        q, k, v = qkv[0], qkv[1], qkv[2]
        phi = lambda t: torch.where(t > 0, t + 1, torch.exp(t)) + 1e-6
        pq, pk = phi(q), phi(k)
        kv = torch.einsum("bhsd,bhse->bhde", pk, v)
        z = torch.einsum("bhsd->bhd", pk)
        num = torch.einsum("bhqd,bhde->bhqe", pq, kv)
        den = torch.einsum("bhqd,bhd->bhq", pq, z)[..., None] + 1e-6
        o = num / den
        return (lambda x, *d: x.permute(*d))(o, 0, 2, 1, 3).reshape(B, S, D) @ wo

    def _B_ref(u, log_A_real, A_imag, C_real, C_imag, log_dt, D_skip):
        L = u.shape[1]
        dt = torch.exp(log_dt)
        A = -torch.exp(log_A_real) + 1j * A_imag
        C = (C_real + 1j * C_imag) * dt[:, None]
        pos = (lambda x, dt: x.to(dt))(torch.arange(L), log_dt.dtype)
        P = torch.exp((A * dt[:, None])[:, :, None] * pos[None, None, :])
        K = 2.0 * torch.einsum("dn,dnl->dl", C, P).real
        L2 = 2 * L
        u_t = (lambda x, *d: x.permute(*d))(u, 0, 2, 1)
        Kf = (lambda x, *a, **k: torch.fft.rfft(x.float(), *a, **k))(K, L2)
        Uf = (lambda x, *a, **k: torch.fft.rfft(x.float(), *a, **k))(u_t, L2)
        y = (lambda x, *a, **k: torch.fft.irfft(x, *a, **k).to(torch.bfloat16))(Uf * Kf[None, :, :], L2)[:, :, :L]
        y = (lambda x, *d: x.permute(*d))(y, 0, 2, 1)
        return y + u * D_skip[None, None, :]

    a = _A_ref(A_x, A_wqkv, A_wo)
    b = _B_ref(B_u, B_log_A_real, B_A_imag, B_C_real, B_C_imag, B_log_dt, B_D_skip)
    return torch.concatenate([a, b], axis=-1)
```

```cpp
#include <hip/hip_runtime.h>
#include <hip/hip_bf16.h>
#include <stdint.h>
typedef unsigned short u16;
__device__ __forceinline__ float bf2f(u16 x) { return __uint_as_float(((unsigned)x) << 16); }
__device__ __forceinline__ u16 f2bf(float f) { unsigned u = __float_as_uint(f); u += 0x7fffu + ((u >> 16) & 1u); return (u16)(u >> 16); }
__device__ __forceinline__ float rbf(float f) { return bf2f(f2bf(f)); }

constexpr int S = 2048, D = 640, H = 32, DH = 20, NS = 192;

__global__ void k_qkv(const u16* x, const u16* w, float* qkv) {
  int n = blockIdx.x * 64 + (threadIdx.x & 63);
  int s = blockIdx.y * 4 + (threadIdx.x >> 6);
  float acc = 0.f;
  for (int k = 0; k < D; ++k) acc = fmaf(bf2f(x[s * D + k]), bf2f(w[k * 3 * D + n]), acc);
  float t = rbf(acc);
  if (n < 2 * D) { float p = t > 0.f ? rbf(t + 1.f) : rbf(expf(t)); t = rbf(p + 1e-6f); }
  qkv[s * 3 * D + n] = t;
}
__global__ void k_kv(const float* qkv, float* kv, float* z) {
  int h = blockIdx.x; int t = threadIdx.x;
  if (t < 400) { int d = t / 20, e = t % 20; float acc = 0.f;
    for (int s = 0; s < S; ++s) acc = fmaf(qkv[s * 1920 + D + h * DH + d], qkv[s * 1920 + 2 * D + h * DH + e], acc);
    kv[h * 400 + t] = rbf(acc);
  } else if (t < 420) { int d = t - 400; float acc = 0.f;
    for (int s = 0; s < S; ++s) acc += qkv[s * 1920 + D + h * DH + d];
    z[h * 20 + d] = rbf(acc);
  }
}
__global__ void k_o(const float* qkv, const float* kv, const float* z, float* o) {
  int idx = blockIdx.x * 256 + threadIdx.x; int s = idx / D, c = idx % D, h = c / DH, e = c % DH;
  float num = 0.f, den = 0.f;
  for (int d = 0; d < DH; ++d) { float pq = qkv[s * 1920 + h * DH + d]; num = fmaf(pq, kv[h * 400 + d * 20 + e], num); den = fmaf(pq, z[h * 20 + d], den); }
  num = rbf(num); den = rbf(rbf(den) + 1e-6f);
  o[idx] = rbf(num / den);
}
__global__ void k_out(const float* o, const u16* wo, u16* out) {
  int n = blockIdx.x * 64 + (threadIdx.x & 63);
  int s = blockIdx.y * 4 + (threadIdx.x >> 6);
  float acc = 0.f;
  for (int k = 0; k < D; ++k) acc = fmaf(o[s * D + k], bf2f(wo[k * D + n]), acc);
  out[s * 1280 + n] = f2bf(acc);
}
__global__ void k_K(const u16* lar, const u16* aim, const u16* cr, const u16* ci, const u16* ldt, float* K) {
  int d = blockIdx.x;
  float dt = rbf(expf(bf2f(ldt[d])));
  for (int l = threadIdx.x; l < S; l += 256) {
    float pos = rbf((float)l);
    float acc = 0.f;
    for (int n = 0; n < NS; ++n) {
      float ar = -rbf(expf(bf2f(lar[d * NS + n]))) * dt;
      float ai = bf2f(aim[d * NS + n]) * dt;
      float Cr = bf2f(cr[d * NS + n]) * dt, Ci = bf2f(ci[d * NS + n]) * dt;
      float e = expf(ar * pos); float sn, cs; sincosf(ai * pos, &sn, &cs);
      acc += Cr * e * cs - Ci * e * sn;
    }
    K[d * S + l] = 2.f * acc;
  }
}
__global__ void k_conv(const u16* u, const float* K, const u16* dsk, u16* out) {
  int d = blockIdx.x % D; int l = (blockIdx.x / D) * 256 + threadIdx.x;
  float acc = 0.f;
  for (int j = 0; j <= l; ++j) acc = fmaf(K[d * S + j], bf2f(u[(l - j) * D + d]), acc);
  float y = rbf(acc);
  float sk = rbf(bf2f(u[l * D + d]) * bf2f(dsk[d]));
  out[l * 1280 + D + d] = f2bf(y + sk);
}
extern "C" void kernel_launch(void* const* d_in, const int* in_sizes, int n_in, void* d_out, int out_size, void* d_ws, size_t ws_size, hipStream_t stream) {
  const u16* x = (const u16*)d_in[0]; const u16* wqkv = (const u16*)d_in[1]; const u16* wo = (const u16*)d_in[2];
  const u16* u = (const u16*)d_in[3]; const u16* lar = (const u16*)d_in[4]; const u16* aim = (const u16*)d_in[5];
  const u16* cr = (const u16*)d_in[6]; const u16* ci = (const u16*)d_in[7]; const u16* ldt = (const u16*)d_in[8]; const u16* dsk = (const u16*)d_in[9];
  u16* out = (u16*)d_out;
  float* qkv = (float*)d_ws; float* kv = qkv + S * 1920; float* z = kv + 32 * 400; float* o = z + 640; float* K = o + S * D;
  k_qkv<<<dim3(30, S / 4), 256, 0, stream>>>(x, wqkv, qkv);
  k_kv<<<32, 448, 0, stream>>>(qkv, kv, z);
  k_o<<<S * D / 256, 256, 0, stream>>>(qkv, kv, z, o);
  k_out<<<dim3(10, S / 4), 256, 0, stream>>>(o, wo, out);
  k_K<<<640, 256, 0, stream>>>(lar, aim, cr, ci, ldt, K);
  k_conv<<<640 * 8, 256, 0, stream>>>(u, K, dsk, out);
}
```

```cpp
#include <hip/hip_runtime.h>
#include <hip/hip_bf16.h>
#include <stdint.h>
typedef unsigned short u16;
typedef unsigned int u32;
typedef float f32x2 __attribute__((ext_vector_type(2)));
typedef float f32x4 __attribute__((ext_vector_type(4)));
typedef float f32x16 __attribute__((ext_vector_type(16)));
typedef short s16x4 __attribute__((ext_vector_type(4)));
typedef short s16x8 __attribute__((ext_vector_type(8)));
typedef _Float16 h8 __attribute__((ext_vector_type(8)));
typedef _Float16 h2 __attribute__((ext_vector_type(2)));
typedef __bf16 bf16x2 __attribute__((ext_vector_type(2)));
typedef u32 u32x2 __attribute__((ext_vector_type(2)));
typedef u32 u32x4 __attribute__((ext_vector_type(4)));
#define DI __device__ __forceinline__
#define LAS __attribute__((address_space(3)))
DI float bf2f(u16 x) { return __uint_as_float(((u32)x) << 16); }
DI u32 pk_bf16(float a, float b) { bf16x2 r = __builtin_convertvector((f32x2){a, b}, bf16x2); return __builtin_bit_cast(u32, r); }
DI u32 pk_f16(float a, float b) { h2 r = __builtin_convertvector((f32x2){a, b}, h2); return __builtin_bit_cast(u32, r); }
DI float lo16(u32 x) { return __uint_as_float(x << 16); }
DI float hi16(u32 x) { return __uint_as_float(x & 0xffff0000u); }
DI float rbf(float f) { return lo16(pk_bf16(f, 0.f)); }
DI u32 phi_pair(float a, float b) {
  u32 t = pk_bf16(a, b);
  float ta = lo16(t), tb = hi16(t);
  float ea = __builtin_amdgcn_fmed3f(__builtin_amdgcn_exp2f(ta * 1.4426950408889634f), 0.f, 1.f);
  float eb = __builtin_amdgcn_fmed3f(__builtin_amdgcn_exp2f(tb * 1.4426950408889634f), 0.f, 1.f);
  return pk_bf16(ea + fmaxf(ta, 1e-6f), eb + fmaxf(tb, 1e-6f));
}
DI f32x2 cmul(f32x2 x, f32x2 y) {
  f32x2 t, z;
  asm("v_pk_mul_f32 %0, %1, %2 op_sel_hi:[0,1]" : "=v"(t) : "v"(x), "v"(y));
  asm("v_pk_fma_f32 %0, %1, %2, %3 op_sel:[1,1,0] op_sel_hi:[1,0,1] neg_lo:[0,1,0]" : "=v"(z) : "v"(x), "v"(y), "v"(t));
  return z;
}
DI f32x2 cexpP(float ar2, float airev, float P) {
  float e = __builtin_amdgcn_exp2f(ar2 * P);
  float t = __builtin_amdgcn_fractf(airev * P);
  float s = __builtin_amdgcn_sinf(t), c = __builtin_amdgcn_cosf(t);
  f32x2 r = {e * c, e * s};
  return r;
}
constexpr int S = 2048, D = 640, NS = 192;
constexpr size_t WS_PQ = 0;
constexpr size_t WS_SLAB = 4u << 20;
constexpr size_t WS_KVF = 8u << 20;
constexpr size_t WS_WOP = 9u << 20;
constexpr size_t WS_UT = 10u << 20;
constexpr size_t WS_KC = 13u << 20;
constexpr size_t WS_KS = 14u << 20;

constexpr int QSTAGE = 36864, QNBUF = 4;
constexpr int SLAB_BLK = 2 * 10 * 256;
constexpr int TROW = 272;

template <bool KV>
DI void q_block(const u16* __restrict__ x, const u16* __restrict__ w, u16* __restrict__ pq, float* __restrict__ slab, int rt, int ct, char* smem) {
  const int tid = threadIdx.x, lane = tid & 63, wv = __builtin_amdgcn_readfirstlane(tid >> 6);
  const int row0 = rt * 128;
  if (wv >= 8) return;
  if (wv >= 4) {
    const int lw = wv - 4;
    const u16* xs[4]; const u16* ws_[5];
#pragma unroll
    for (int i = 0; i < 4; ++i) { int pc = lw * 4 + i; int row = 8 * pc + (lane >> 3); int ch = (lane & 7) ^ ((row >> 1) & 7); xs[i] = x + (size_t)(row0 + row) * D + 8 * ch; }
#pragma unroll
    for (int i = 0; i < 5; ++i) {
      int pc = lw * 5 + i; int sb = 4 * pc + (lane >> 4); int kb = sb / 10, nb = sb % 10; int row = ((lane >> 1) & 7) ^ (4 * (kb & 1)); int c = nb * 16 + 8 * (lane & 1);
      int gc;
      if (KV) { int wn2 = c / 80, cc = c % 80; gc = cc < 40 ? 640 + 80 * ct + 40 * wn2 + cc : 1280 + 80 * ct + 40 * wn2 + (cc - 40); }
      else gc = 160 * ct + c;
      ws_[i] = w + (size_t)(8 * kb + row) * 1920 + gc;
    }
    auto stage = [&](int kt) {
      char* b = smem + (kt % QNBUF) * QSTAGE;
#pragma unroll
      for (int i = 0; i < 4; ++i) __builtin_amdgcn_global_load_lds((const u32*)(xs[i] + kt * 64), (LAS u32*)(b + (lw * 4 + i) * 1024), 16, 0, 0);
#pragma unroll
      for (int i = 0; i < 5; ++i) __builtin_amdgcn_global_load_lds((const u32*)(ws_[i] + (size_t)kt * 64 * 1920), (LAS u32*)(b + 16384 + (lw * 5 + i) * 1024), 16, 0, 0);
    };
#pragma unroll
    for (int i = 0; i < QNBUF; ++i) stage(i);
    asm volatile("s_waitcnt vmcnt(%0)" :: "n"((QNBUF - 1) * 9) : "memory");
    __builtin_amdgcn_s_barrier();
#pragma unroll
    for (int kt = 0; kt < 9; ++kt) {
      const int issued = (kt + QNBUF < 10) ? kt + QNBUF : 10;
      asm volatile("s_waitcnt vmcnt(%0)" :: "n"((issued - (kt + 2)) * 9) : "memory");
      __builtin_amdgcn_s_barrier();
      if (kt + QNBUF < 10) stage(kt + QNBUF);
    }
    if (KV) { __builtin_amdgcn_s_barrier(); __builtin_amdgcn_s_barrier(); }
    return;
  }
  const int wm = wv >> 1, wn = wv & 1;
  f32x4 acc[4][5];
#pragma unroll
  for (int m = 0; m < 4; ++m)
#pragma unroll
    for (int n = 0; n < 5; ++n) acc[m][n] = (f32x4){0, 0, 0, 0};
  const int fr = lane & 15, g = lane >> 4;
  const u32 aoff0 = (wm * 64 + fr) * 128 + (((0 + g) ^ (fr >> 1)) * 16);
  const u32 aoff1 = (wm * 64 + fr) * 128 + (((4 + g) ^ (fr >> 1)) * 16);
  const u32 boff0 = 16384 + g * 2560 + wn * 1280 + ((0 ^ (4 * (g & 1))) + (fr >> 2)) * 32 + (lane & 3) * 8;
  const u32 boff1 = 16384 + g * 2560 + wn * 1280 + ((4 ^ (4 * (g & 1))) + (fr >> 2)) * 32 + (lane & 3) * 8;
  const u32 sbase = (u32)(size_t)smem;
  s16x8 af[2][4]; s16x4 bl[2][5], bh[2][5];
  u32 aad[QNBUF][2], bad[QNBUF][2];
#pragma unroll
  for (int b = 0; b < QNBUF; ++b) { aad[b][0] = sbase + b * QSTAGE + aoff0; aad[b][1] = sbase + b * QSTAGE + aoff1; bad[b][0] = sbase + b * QSTAGE + boff0; bad[b][1] = sbase + b * QSTAGE + boff1;
    asm volatile("" : "+v"(aad[b][0]), "+v"(aad[b][1]), "+v"(bad[b][0]), "+v"(bad[b][1])); }
  auto ldop = [&](int kt, int ks, int i) {
    const int b = kt % QNBUF;
    if (i < 10) {
      const int n = i >> 1;
      if (i & 1) bh[ks][n] = __builtin_amdgcn_ds_read_tr16_b64_v4i16((LAS s16x4*)(bad[b][1] + ks * 10240 + n * 256));
      else       bl[ks][n] = __builtin_amdgcn_ds_read_tr16_b64_v4i16((LAS s16x4*)(bad[b][0] + ks * 10240 + n * 256));
    } else af[ks][i - 10] = *(const LAS s16x8*)(aad[b][ks] + (i - 10) * 2048);
  };
  auto mma1 = [&](int sl, int i) {
    const int m = i / 5, n = i % 5;
    s16x8 bb = __builtin_shufflevector(bl[sl][n], bh[sl][n], 0, 1, 2, 3, 4, 5, 6, 7);
    acc[m][n] = KV ? __builtin_amdgcn_mfma_f32_16x16x32_bf16(af[sl][m], bb, acc[m][n], 0, 0, 0)
                   : __builtin_amdgcn_mfma_f32_16x16x32_bf16(bb, af[sl][m], acc[m][n], 0, 0, 0);
  };
  __builtin_amdgcn_s_barrier();
#pragma unroll
  for (int i = 0; i < 14; ++i) ldop(0, 0, i);
#pragma unroll
  for (int kt = 0; kt < 10; ++kt) {
    __builtin_amdgcn_sched_barrier(0);
#pragma unroll
    for (int i = 0; i < 20; ++i) {
      mma1(0, i);
      if (i < 14) ldop(kt, 1, i);
      __builtin_amdgcn_sched_barrier(0);
    }
    if (kt < 9) {
      __builtin_amdgcn_s_waitcnt(0xC07F);
      __builtin_amdgcn_s_barrier();
      __builtin_amdgcn_sched_barrier(0);
    }
#pragma unroll
    for (int i = 0; i < 20; ++i) {
      mma1(1, i);
      if (kt < 9 && i < 14) ldop(kt + 1, 0, i);
      __builtin_amdgcn_sched_barrier(0);
    }
  }
  if (!KV) {
#pragma unroll
    for (int m = 0; m < 4; ++m)
#pragma unroll
      for (int n = 0; n < 5; ++n) {
        u32x2 o = {phi_pair(acc[m][n][0], acc[m][n][1]), phi_pair(acc[m][n][2], acc[m][n][3])};
        const int col = 160 * ct + wn * 80 + n * 16 + 4 * g; const int h = col / 20, q = (col % 20) >> 2;
        *(u32x2*)(pq + ((size_t)((rt * 8 + wm * 4 + m) * 32 + h) * 5 + q) * 64 + fr * 4) = o;
      }
  } else {
    u32x2 pk[4][5];
    const bool isk = fr < 8;
#pragma unroll
    for (int m = 0; m < 4; ++m)
#pragma unroll
      for (int n = 0; n < 5; ++n) {
        if (n <= 2) { pk[m][n].x = phi_pair(acc[m][n][0], acc[m][n][1]); pk[m][n].y = phi_pair(acc[m][n][2], acc[m][n][3]); }
        if (n >= 2) {
          u32 v0 = pk_bf16(acc[m][n][0], acc[m][n][1]), v1 = pk_bf16(acc[m][n][2], acc[m][n][3]);
          if (n > 2) { pk[m][n].x = v0; pk[m][n].y = v1; } else { pk[m][n].x = isk ? pk[m][n].x : v0; pk[m][n].y = isk ? pk[m][n].y : v1; }
        }
      }
    f32x4 kv[10];
#pragma unroll
    for (int i = 0; i < 10; ++i) kv[i] = (f32x4){0, 0, 0, 0};
    const s16x8 ones = {0x3f80, 0x3f80, 0x3f80, 0x3f80, 0x3f80, 0x3f80, 0x3f80, 0x3f80};
#pragma unroll
    for (int u = 0; u < 2; ++u) {
      s16x8 f[5];
#pragma unroll
      for (int n = 0; n < 5; ++n) f[n] = __builtin_bit_cast(s16x8, (u32x4){pk[2 * u][n].x, pk[2 * u][n].y, pk[2 * u + 1][n].x, pk[2 * u + 1][n].y});
      kv[0] = __builtin_amdgcn_mfma_f32_16x16x32_bf16(f[0], f[2], kv[0], 0, 0, 0);
      kv[1] = __builtin_amdgcn_mfma_f32_16x16x32_bf16(f[0], f[3], kv[1], 0, 0, 0);
      kv[2] = __builtin_amdgcn_mfma_f32_16x16x32_bf16(f[1], f[2], kv[2], 0, 0, 0);
      kv[3] = __builtin_amdgcn_mfma_f32_16x16x32_bf16(f[1], f[3], kv[3], 0, 0, 0);
      kv[4] = __builtin_amdgcn_mfma_f32_16x16x32_bf16(f[1], f[4], kv[4], 0, 0, 0);
      kv[5] = __builtin_amdgcn_mfma_f32_16x16x32_bf16(f[2], f[3], kv[5], 0, 0, 0);
      kv[6] = __builtin_amdgcn_mfma_f32_16x16x32_bf16(f[2], f[4], kv[6], 0, 0, 0);
      kv[7] = __builtin_amdgcn_mfma_f32_16x16x32_bf16(f[0], ones, kv[7], 0, 0, 0);
      kv[8] = __builtin_amdgcn_mfma_f32_16x16x32_bf16(f[1], ones, kv[8], 0, 0, 0);
      kv[9] = __builtin_amdgcn_mfma_f32_16x16x32_bf16(f[2], ones, kv[9], 0, 0, 0);
    }
    __builtin_amdgcn_s_barrier();
    f32x4* red = (f32x4*)smem + wn * 640 + lane;
    if (wm == 1) {
#pragma unroll
      for (int i = 0; i < 10; ++i) red[i * 64] = kv[i];
    }
    __builtin_amdgcn_s_waitcnt(0xC07F);
    __builtin_amdgcn_s_barrier();
    if (wm == 0) {
      f32x4* dst = (f32x4*)(slab + (size_t)(rt * 8 + ct) * SLAB_BLK) + wn * 640 + lane;
#pragma unroll
      for (int i = 0; i < 10; ++i) dst[i * 64] = kv[i] + red[i * 64];
    }
  }
}

DI void kgen_wave(const u16* __restrict__ lar_, const u16* __restrict__ aim_, const u16* __restrict__ cr_, const u16* __restrict__ ci_,
                  const u16* __restrict__ ldt_, u16* __restrict__ kcg, float* __restrict__ ksg, int d, char* reg) {
  const int lane = threadIdx.x & 63;
  u16 plar[3], paim[3], pcr[3], pci[3];
#pragma unroll
  for (int c = 0; c < 3; ++c) { int n = d * NS + 64 * c + lane; plar[c] = lar_[n]; paim[c] = aim_[n]; pcr[c] = cr_[n]; pci[c] = ci_[n]; }
  const float dt = rbf(expf(bf2f(ldt_[d])));
  float ar2[3], airev[3]; f32x2 cc[3];
  float cmax = 0.f;
#pragma unroll
  for (int c = 0; c < 3; ++c) {
    float Are = -rbf(expf(bf2f(plar[c])));
    float ar = Are * dt, ai = bf2f(paim[c]) * dt;
    ar2[c] = ar * 1.4426950408889634f; airev[c] = ai * 0.15915494309189535f;
    cc[c].x = bf2f(pcr[c]) * dt * 2.f; cc[c].y = bf2f(pci[c]) * dt * 2.f;
    cmax = fmaxf(cmax, fmaxf(fabsf(cc[c].x), fabsf(cc[c].y)));
  }
#pragma unroll
  for (int o = 32; o >= 1; o >>= 1) cmax = fmaxf(cmax, __shfl_xor(cmax, o));
  int ce = (int)((__float_as_uint(cmax) >> 23) & 0xff);
  ce = ce < 30 ? 30 : (ce > 220 ? 220 : ce);
  const float sc = __uint_as_float((u32)(127 + 125 - ce) << 23);
  const float isc = __uint_as_float((u32)(ce + 2) << 23);
  f32x16 kacc;
#pragma unroll
  for (int i = 0; i < 16; ++i) kacc[i] = 0.f;
  u32* tE = (u32*)(reg) + lane;
  u32* tF = (u32*)(reg + 21 * TROW) + lane;
  constexpr int R = TROW / 4;
#pragma unroll
  for (int c = 0; c < 3; ++c) {
    const float a2 = ar2[c], av = airev[c];
    f32x2 E0 = cc[c] * sc;
    auto stE = [&](int r, f32x2 v) { tE[r * R] = pk_f16(v.x, -v.y); };
    auto stF = [&](int r, f32x2 v) { tF[r * R] = pk_f16(v.x, v.y); };
    f32x2 w1 = cexpP(a2, av, 1.f), w2 = cexpP(a2, av, 2.f), w4 = cexpP(a2, av, 4.f);
    f32x2 B256 = cexpP(a2, av, 256.f), B512 = cexpP(a2, av, 512.f), B1024 = cexpP(a2, av, 1024.f);
    f32x2 E1 = cmul(E0, w1), E2 = cmul(E0, w2), E3 = cmul(E1, w2), E4 = cmul(E0, w4), E5 = cmul(E1, w4), E6 = cmul(E2, w4), E7 = cmul(E3, w4);
    stE(0, E0); stE(1, E1); stE(2, E2); stE(3, E3); stE(4, E4); stE(5, E5); stE(6, E6); stE(7, E7);
    stE(8, cmul(E0, B256)); stE(9, cmul(E2, B256)); stE(10, cmul(E4, B256)); stE(11, cmul(E6, B256));
    f32x2 B768 = cmul(B512, B256);
    stE(12, cmul(E0, B512)); stE(13, cmul(E4, B512)); stE(14, cmul(E0, B768)); stE(15, cmul(E4, B768));
    f32x2 G0 = cmul(E0, B1024);
    stE(16, G0); stE(17, cmul(G0, B256)); stE(18, cmul(G0, B512)); stE(19, cmul(G0, B768)); stE(20, cmul(G0, B1024));
    f32x2 v1 = cexpP(a2, av, 8.f), v2 = cexpP(a2, av, 16.f), v4 = cexpP(a2, av, 32.f), v8 = cexpP(a2, av, 64.f), v16 = cexpP(a2, av, 128.f);
    f32x2 F[8];
    F[0].x = 1.f; F[0].y = 0.f; F[1] = v1; F[2] = v2; F[3] = cmul(v2, v1); F[4] = v4; F[5] = cmul(v4, v1); F[6] = cmul(v4, v2); F[7] = cmul(v4, F[3]);
    f32x2 v24 = cmul(v16, v8);
#pragma unroll
    for (int i = 0; i < 8; ++i) {
      stF(i, F[i]);
      stF(8 + i, i == 0 ? v8 : cmul(F[i], v8));
      stF(16 + i, i == 0 ? v16 : cmul(F[i], v16));
      stF(24 + i, i == 0 ? v24 : cmul(F[i], v24));
    }
    __builtin_amdgcn_wave_barrier();
    {
      const int r = lane & 31, hh = lane >> 5;
      const char* pa = reg + (r < 21 ? r : 20) * TROW + 16 * hh;
      const char* pb = reg + (21 + r) * TROW + 16 * hh;
#pragma unroll
      for (int s = 0; s < 8; ++s) {
        h8 a = *(const h8*)(pa + 32 * s);
        h8 b = *(const h8*)(pb + 32 * s);
        kacc = __builtin_amdgcn_mfma_f32_32x32x16_f16(a, b, kacc, 0, 0, 0);
      }
    }
    __builtin_amdgcn_wave_barrier();
  }
  u16* kc = kcg + (size_t)d * 648;
  const int i = lane & 31, hh = lane >> 5;
#pragma unroll
  for (int rg = 0; rg < 12; ++rg) {
    const int q = rg >> 2, sub = rg & 3;
    int idx; bool ok = true;
    if (q == 0) idx = 8 * i + 4 * hh + sub;
    else if (q == 1) idx = hh == 0 ? 256 + 4 * i + sub : 384 + 64 * (sub >> 1) + 2 * i + (sub & 1);
    else { idx = hh == 0 ? 512 + 32 * sub + i : 640; ok = (hh == 0) || (sub == 0 && i == 0); }
    u32 hv = pk_f16(kacc[rg] * 128.f, 0.f);
    if (ok) kc[idx] = (u16)hv;
  }
  if (lane == 0) ksg[d] = isc * (1.f / 128.f);
}

DI void aux_block(const u16* __restrict__ u, const u16* __restrict__ lar_, const u16* __restrict__ aim_, const u16* __restrict__ cr_, const u16* __restrict__ ci_,
                  const u16* __restrict__ ldt_, const u16* __restrict__ wo, u16* __restrict__ ut, u16* __restrict__ kcg, float* __restrict__ ksg, char* __restrict__ wop,
                  int ab, char* smem) {
  const int tid = threadIdx.x, lane = tid & 63, wv = __builtin_amdgcn_readfirstlane(tid >> 6);
  for (int uu = tid; uu < 800; uu += 640) {
    const int U = ab * 800 + uu; const int img = U / 1280, un = U % 1280; const int ct = img / 10, kt = img % 10;
    const int sb = un >> 4, kb = sb / 10, nb = sb % 10, j = ((un >> 1) & 7) ^ (4 * (kb & 1));
    const int T = 2 * kt + (kb >> 2), G = T / 5, tt = T % 5, ii = 8 * tt + j, hh = ii / 5, r = ii % 5;
    const int k = 160 * G + 20 * hh + 5 * (kb & 3) + r;
    *(u32x4*)(wop + (size_t)U * 16) = *(const u32x4*)(wo + (size_t)k * D + 160 * ct + nb * 16 + 8 * (un & 1));
  }
  if (wv < 5) {
    const int tile = ab * 5 + wv; const int rb = tile / 10, cb = tile % 10;
    u16* t = (u16*)(smem + wv * 14720);
#pragma unroll
    for (int i = 0; i < 8; ++i) {
      const int row = i * 8 + (lane >> 3), ch = lane & 7;
      u32x4 v = *(const u32x4*)(u + (size_t)(rb * 64 + row) * D + cb * 64 + ch * 8);
      u32* p = (u32*)(t + row * 66 + ch * 8);
      p[0] = v.x; p[1] = v.y; p[2] = v.z; p[3] = v.w;
    }
    __builtin_amdgcn_wave_barrier();
#pragma unroll
    for (int i = 0; i < 8; ++i) {
      const int c = i * 8 + (lane >> 3), r8 = lane & 7;
      u32 o[4];
#pragma unroll
      for (int k = 0; k < 4; ++k) o[k] = (u32)t[(r8 * 8 + 2 * k) * 66 + c] | ((u32)t[(r8 * 8 + 2 * k + 1) * 66 + c] << 16);
      *(u32x4*)(ut + (size_t)(cb * 64 + c) * S + rb * 64 + r8 * 8) = (u32x4){o[0], o[1], o[2], o[3]};
    }
    __builtin_amdgcn_wave_barrier();
  }
  kgen_wave(lar_, aim_, cr_, ci_, ldt_, kcg, ksg, ab * 10 + wv, smem + wv * 14720);
}

struct P1 { const u16 *x, *w, *wo, *u, *lar, *aim, *cr, *ci, *ldt; char* ws; };
__global__ __launch_bounds__(640, 3) void k1(P1 p) {
  extern __shared__ __attribute__((aligned(16))) char smem[];
  const int bid = blockIdx.x;
  if (bid < 192) {
    const int xcd = bid & 7, j = bid >> 3;
    const int rt = xcd * 2 + (j / 12), c12 = j % 12;
    if (c12 < 8) q_block<true>(p.x, p.w, (u16*)(p.ws + WS_PQ), (float*)(p.ws + WS_SLAB), rt, c12, smem);
    else q_block<false>(p.x, p.w, (u16*)(p.ws + WS_PQ), (float*)(p.ws + WS_SLAB), rt, c12 - 8, smem);
  } else {
    aux_block(p.u, p.lar, p.aim, p.cr, p.ci, p.ldt, p.wo, (u16*)(p.ws + WS_UT), (u16*)(p.ws + WS_KC), (float*)(p.ws + WS_KS), p.ws + WS_WOP, bid - 192, smem);
  }
}

DI void r_block(const float* __restrict__ slab, u16* __restrict__ kvf, int h, char* smem) {
  const int tid = threadIdx.x;
  float* red = (float*)smem;
  const int ct = h >> 2, wn = (h >> 1) & 1, hl = h & 1;
  if (tid < 420) {
    const int r = tid;
    int tile, lane, reg;
    if (r < 400) { int d = r / 20 + 20 * hl, e = r % 20 + 20 * hl + 40; int dt = d >> 4, et = e >> 4;
      tile = dt == 0 ? et - 2 : (dt == 1 ? et : et + 2);
      lane = ((d & 15) >> 2) * 16 + (e & 15); reg = d & 3;
    } else { int d = r - 400 + 20 * hl; tile = 7 + (d >> 4); lane = ((d & 15) >> 2) * 16; reg = d & 3; }
    const float* p = slab + (size_t)ct * SLAB_BLK + (wn * 640 + tile * 64 + lane) * 4 + reg;
    float v[16];
#pragma unroll
    for (int rt = 0; rt < 16; ++rt) v[rt] = p[(size_t)rt * 8 * SLAB_BLK];
    float a = 0.f;
#pragma unroll
    for (int rt = 0; rt < 16; ++rt) a += v[rt];
    red[r] = a;
  }
  __syncthreads();
  if (tid < 128) {
    const int tile = tid >> 6, lane = tid & 63, row = lane & 15, gk = lane >> 4, g = row >> 2, r = row & 3;
    float f[8];
#pragma unroll
    for (int j = 0; j < 8; ++j) {
      const int d = 8 * gk + j; float v = 0.f;
      if (d < 20) {
        if (tile == 0) v = r < 3 ? red[d * 20 + 5 * g + r] : red[400 + d];
        else if (r < 2) v = red[d * 20 + 5 * g + 3 + r];
      }
      f[j] = v;
    }
    u32x4 o = {pk_bf16(f[0], f[1]), pk_bf16(f[2], f[3]), pk_bf16(f[4], f[5]), pk_bf16(f[6], f[7])};
    *(u32x4*)(kvf + (size_t)h * 1024 + tile * 512 + lane * 8) = o;
  }
}

constexpr int CH_BYTES = 23296, UC_STRIDE = 4160, KP_OFF = 16640, KP_STRIDE = 1280, KC_OFF = 21760;
constexpr int PSPLIT = 22;
DI void conv_block(const u16* __restrict__ ut, const u16* __restrict__ kcg, const float* __restrict__ ksg, const u16* __restrict__ dsk_, u16* __restrict__ out, int cb, char* smem) {
  const int tid = threadIdx.x, lane = tid & 63, wv = __builtin_amdgcn_readfirstlane(tid >> 6), ch = wv >> 1, hf = wv & 1;
  const int d0 = cb * 4;
  char* reg = smem + ch * CH_BYTES;
  float* red = (float*)(smem + 4 * CH_BYTES);
  u32x4 uv[2];
#pragma unroll
  for (int i = 0; i < 2; ++i) uv[i] = *(const u32x4*)(ut + (size_t)(d0 + ch) * S + 8 * (hf * 128 + lane + 64 * i));
  {
    const u32* src = (const u32*)(kcg + (size_t)(d0 + ch) * 648);
    u32* dst = (u32*)(reg + KC_OFF);
    for (int i = hf * 64 + lane; i < 324; i += 128) dst[i] = src[i];
  }
  const float ks = ksg[d0 + ch];
  float m = 0.f;
#pragma unroll
  for (int i = 0; i < 2; ++i)
#pragma unroll
    for (int k = 0; k < 4; ++k) m = fmaxf(m, fmaxf(fabsf(lo16(uv[i][k])), fabsf(hi16(uv[i][k]))));
#pragma unroll
  for (int o = 32; o >= 1; o >>= 1) m = fmaxf(m, __shfl_xor(m, o));
  if (lane == 0) red[wv] = m;
  __syncthreads();
  m = fmaxf(red[2 * ch], red[2 * ch + 1]);
  int e = (int)((__float_as_uint(m) >> 23) & 0xff);
  e = e < 30 ? 30 : (e > 220 ? 220 : e);
  const float us = __uint_as_float((u32)(127 + 140 - e) << 23), ius = __uint_as_float((u32)(e - 13) << 23);
#pragma unroll
  for (int i = 0; i < 2; ++i) {
    const int q = hf * 128 + lane + 64 * i;
    u32x4 o = {pk_f16(hi16(uv[i].w) * us, lo16(uv[i].w) * us), pk_f16(hi16(uv[i].z) * us, lo16(uv[i].z) * us),
               pk_f16(hi16(uv[i].y) * us, lo16(uv[i].y) * us), pk_f16(hi16(uv[i].x) * us, lo16(uv[i].x) * us)};
    *(u32x4*)(reg + 2 * (2040 - 8 * q)) = o;
  }
  if (hf == 0 && lane < 4) *(u32x4*)(reg + 4096 + 16 * lane) = (u32x4){0, 0, 0, 0};
  {
    const u16* kc = (const u16*)(reg + KC_OFF);
    char* kp = reg + KP_OFF;
    if (hf == 1 && lane < 60) *(u32x4*)(kp + (lane & 3) * KP_STRIDE + (lane >> 2) * 16) = (u32x4){0, 0, 0, 0};
#pragma unroll
    for (int k = 0; k < 2; ++k) {
      const int q = hf + 2 * (lane + 64 * k);
      u32 v[8];
#pragma unroll
      for (int j8 = 0; j8 < 8; ++j8) {
        int j = 8 * q + j8;
        u32 fb = __float_as_uint((float)j); fb += 0x7fffu + ((fb >> 16) & 1u);
        int idx = j < 256 ? j : 256 + (int)((fb >> 16) - 0x4380u);
        v[j8] = kc[idx];
      }
      u32x4 o = {v[0] | (v[1] << 16), v[2] | (v[3] << 16), v[4] | (v[5] << 16), v[6] | (v[7] << 16)};
      *(u32x4*)(kp + (q & 3) * KP_STRIDE + ((q >> 2) + 15) * 16) = o;
    }
  }
  __syncthreads();
#pragma unroll
  for (int k = 0; k < 3; ++k) {
    const int q = hf * 130 + lane + 64 * k;
    if (lane + 64 * k < 130) {
      u32x4 a = *(const u32x4*)(reg + 16 * q);
      u32x2 b = *(const u32x2*)(reg + 16 * q + 16);
      u32x4 c1 = {__builtin_amdgcn_alignbit(a.y, a.x, 16), __builtin_amdgcn_alignbit(a.z, a.y, 16), __builtin_amdgcn_alignbit(a.w, a.z, 16), __builtin_amdgcn_alignbit(b.x, a.w, 16)};
      u32x4 c2 = {a.y, a.z, a.w, b.x};
      u32x4 c3 = {c1.y, c1.z, c1.w, __builtin_amdgcn_alignbit(b.y, b.x, 16)};
      *(u32x4*)(reg + 1 * UC_STRIDE + 16 * q) = c1;
      *(u32x4*)(reg + 2 * UC_STRIDE + 16 * q) = c2;
      *(u32x4*)(reg + 3 * UC_STRIDE + 16 * q) = c3;
    }
  }
  __syncthreads();
  f32x4 acc[4][2];
#pragma unroll
  for (int t = 0; t < 4; ++t) { acc[t][0] = (f32x4){0, 0, 0, 0}; acc[t][1] = (f32x4){0, 0, 0, 0}; }
  const int b = lane & 15, g = lane >> 4;
  {
    const int cpy = (3 - b) & 3;
    const char* pa0 = reg + cpy * UC_STRIDE + 2 * (2044 - 4 * (b >> 2) + 8 * g);
    const char* pb0 = reg + KP_OFF + g * KP_STRIDE + 16 * (b + 15);
    typedef const volatile LAS u32x2* lp2;
    typedef const volatile LAS u32x4* lp4;
    const u32 abase = (u32)(size_t)(pa0 - 64 * 63 - 32);
    const u32 bbase = (u32)(size_t)(pb0 - 16 * 63);
    constexpr int DP = 2;
    u32x2 Ab[DP + 1][4]; u32x4 Bb[DP + 1][4];
    auto run = [&](auto P0c, auto P1c) {
      constexpr int P0 = decltype(P0c)::value, P1 = decltype(P1c)::value;
      auto issue = [&](int p) {
        const int sl = p % (DP + 1);
        const u32 oa = 64 * (63 - p);
        Ab[sl][0] = *(lp2)(abase + oa + 32); Ab[sl][1] = *(lp2)(abase + oa + 40);
        Ab[sl][2] = *(lp2)(abase + oa);      Ab[sl][3] = *(lp2)(abase + oa + 8);
#pragma unroll
        for (int t = 0; t < 4; ++t) if (t >= (p >> 4)) Bb[sl][t] = *(lp4)(bbase + 16 * (63 - p) + 256 * t);
      };
#pragma unroll
      for (int p = P0; p < P0 + DP; ++p) issue(p);
#pragma unroll
      for (int p = P0; p < P1; ++p) {
        if (p + DP < P1) issue(p + DP);
        __builtin_amdgcn_sched_barrier(0);
        const int sl = p % (DP + 1);
        h8 A0 = __builtin_bit_cast(h8, (u32x4){Ab[sl][0].x, Ab[sl][0].y, Ab[sl][1].x, Ab[sl][1].y});
        h8 A1 = __builtin_bit_cast(h8, (u32x4){Ab[sl][2].x, Ab[sl][2].y, Ab[sl][3].x, Ab[sl][3].y});
#pragma unroll
        for (int t = 0; t < 4; ++t) if (t >= (p >> 4)) {
          h8 B = __builtin_bit_cast(h8, Bb[sl][t]);
          acc[t][0] = __builtin_amdgcn_mfma_f32_16x16x32_f16(A0, B, acc[t][0], 0, 0, 0);
          acc[t][1] = __builtin_amdgcn_mfma_f32_16x16x32_f16(A1, B, acc[t][1], 0, 0, 0);
        }
        __builtin_amdgcn_sched_barrier(0);
      }
    };
    if (hf == 0) run(std::integral_constant<int, 0>{}, std::integral_constant<int, PSPLIT>{});
    else run(std::integral_constant<int, PSPLIT>{}, std::integral_constant<int, 64>{});
  }
  __syncthreads();
  f32x4* xch = (f32x4*)(reg + UC_STRIDE) + lane;
  if (hf == 1) {
#pragma unroll
    for (int t = 1; t < 4; ++t) { xch[(2 * (t - 1)) * 64] = acc[t][0]; xch[(2 * (t - 1) + 1) * 64] = acc[t][1]; }
  }
  __syncthreads();
  if (hf == 0) {
    const float ysc = ks * ius;
    char* yb = reg + 3 * UC_STRIDE;
#pragma unroll
    for (int t = 0; t < 4; ++t)
#pragma unroll
      for (int r = 0; r < 2; ++r) {
        f32x4 a = acc[t][r];
        if (t > 0) a += xch[(2 * (t - 1) + r) * 64];
        u32x2 o = {pk_bf16(a[0] * ysc, a[1] * ysc), pk_bf16(a[2] * ysc, a[3] * ysc)};
        *(u32x2*)(yb + 2 * (32 * (16 * t + b) + 16 * r + 4 * g)) = o;
      }
    if (lane == 0) red[8 + ch] = ius;
  }
  __syncthreads();
  {
    u32x2 yv[4], uu[4]; float iu[4], dk[4];
#pragma unroll
    for (int c = 0; c < 4; ++c) {
      yv[c] = *(const u32x2*)(smem + c * CH_BYTES + 3 * UC_STRIDE + 8 * tid);
      uu[c] = *(const u32x2*)(smem + c * CH_BYTES + 2 * (2044 - 4 * tid));
      iu[c] = red[8 + c]; dk[c] = bf2f(dsk_[d0 + c]);
    }
#pragma unroll
    for (int i = 0; i < 4; ++i) {
      float r[4];
#pragma unroll
      for (int c = 0; c < 4; ++c) {
        u32 yw = yv[c][i >> 1]; float y = (i & 1) ? hi16(yw) : lo16(yw);
        const u32 uw = (i < 2) ? uu[c].y : uu[c].x; const u16 ub = (i & 1) ? (u16)uw : (u16)(uw >> 16); float uf = (float)__builtin_bit_cast(_Float16, ub) * iu[c];
        r[c] = y + rbf(uf * dk[c]);
      }
      u32x2 o = {pk_bf16(r[0], r[1]), pk_bf16(r[2], r[3])};
      *(u32x2*)(out + (size_t)(4 * tid + i) * 1280 + 640 + d0) = o;
    }
  }
}
struct P2 { const u16* dsk; u16* out; char* ws; };
__global__ __launch_bounds__(512, 2) void k2(P2 p) {
  extern __shared__ __attribute__((aligned(16))) char smem[];
  const int bid = blockIdx.x;
  if (bid < 32) r_block((const float*)(p.ws + WS_SLAB), (u16*)(p.ws + WS_KVF), bid, smem);
  else {
    const int j = bid - 32, xcd = j & 7, k = j >> 3;
    conv_block((const u16*)(p.ws + WS_UT), (const u16*)(p.ws + WS_KC), (const float*)(p.ws + WS_KS), p.dsk, p.out, xcd * 20 + k, smem);
  }
}

constexpr int OSTAGE = 20480, ONBUF = 4, O_OFF_W = 81920;
DI void o_block(const u16* __restrict__ pq, const u16* __restrict__ kvf, const char* __restrict__ wop, u16* __restrict__ out, int rt, int ct, char* smem) {
  const int tid = threadIdx.x, lane = tid & 63, wv = __builtin_amdgcn_readfirstlane(tid >> 6);
  const int row0 = rt * 64, fr = lane & 15, g = lane >> 4;
  const int lw = wv - 4;
  const char* wsrc = wop + (size_t)ct * 10 * OSTAGE + lw * 5120 + lane * 16;
  auto stage = [&](int kt) {
    char* b = smem + O_OFF_W + (kt % ONBUF) * OSTAGE + lw * 5120;
#pragma unroll
    for (int i = 0; i < 5; ++i) __builtin_amdgcn_global_load_lds((const u32*)(wsrc + kt * OSTAGE + i * 1024), (LAS u32*)(b + i * 1024), 16, 0, 0);
  };
  s16x8 kf[4][2]; u32x2 pa[4][4], pb[4][4];
#pragma unroll
  for (int hh = 0; hh < 4; ++hh) {
    kf[hh][0] = *(const s16x8*)(kvf + (size_t)(4 * wv + hh) * 1024 + lane * 8);
    kf[hh][1] = *(const s16x8*)(kvf + (size_t)(4 * wv + hh) * 1024 + 512 + lane * 8);
#pragma unroll
    for (int st = 0; st < 4; ++st) {
      const u16* p = pq + ((size_t)((rt * 4 + st) * 32 + 4 * wv + hh) * 5) * 64 + fr * 4;
      pa[hh][st] = *(const u32x2*)(p + (g < 3 ? 2 * g : 4) * 64);
      pb[hh][st] = *(const u32x2*)(p + (g < 2 ? 2 * g + 1 : 4) * 64);
    }
  }
  __builtin_amdgcn_sched_barrier(0);
  if (wv >= 4) {
#pragma unroll
    for (int i = 0; i < ONBUF; ++i) stage(i);
  }
  __builtin_amdgcn_sched_barrier(0);
  {
    const int G = wv >> 1, half = wv & 1;
#pragma unroll
    for (int st = 0; st < 4; ++st) {
      float vals[20];
#pragma unroll
      for (int hh = 0; hh < 4; ++hh) {
        u32x2 a = pa[hh][st], bq = pb[hh][st];
        a.x = g < 3 ? a.x : 0u; a.y = g < 3 ? a.y : 0u; bq.x = g < 2 ? bq.x : 0u; bq.y = g < 2 ? bq.y : 0u;
        s16x8 pf = __builtin_bit_cast(s16x8, (u32x4){a.x, a.y, bq.x, bq.y});
        f32x4 z4 = {0, 0, 0, 0};
        f32x4 t0 = __builtin_amdgcn_mfma_f32_16x16x32_bf16(kf[hh][0], pf, z4, 0, 0, 0);
        f32x4 t1 = __builtin_amdgcn_mfma_f32_16x16x32_bf16(kf[hh][1], pf, z4, 0, 0, 0);
        u32 n01 = pk_bf16(t0[0], t0[1]), n2d = pk_bf16(t0[2], t0[3]), n34 = pk_bf16(t1[0], t1[1]);
        float den = rbf(hi16(n2d) + 1e-6f);
        float rd = __builtin_amdgcn_rcpf(den);
        vals[5 * hh + 0] = lo16(n01) * rd; vals[5 * hh + 1] = hi16(n01) * rd; vals[5 * hh + 2] = lo16(n2d) * rd;
        vals[5 * hh + 3] = lo16(n34) * rd; vals[5 * hh + 4] = hi16(n34) * rd;
      }
#pragma unroll
      for (int i = 0; i < 5; ++i) {
        const int byte = 40 * half + 8 * i;
        const int T = 5 * G + (byte >> 4);
        *(u32x2*)(smem + (st * 20 + T) * 1024 + lane * 16 + (byte & 8)) = (u32x2){pk_bf16(vals[4 * i], vals[4 * i + 1]), pk_bf16(vals[4 * i + 2], vals[4 * i + 3])};
      }
    }
  }
  if (wv >= 4) {
    asm volatile("s_waitcnt vmcnt(%0) lgkmcnt(0)" :: "n"((ONBUF - 1) * 5) : "memory");
    __builtin_amdgcn_s_barrier();
#pragma unroll
    for (int kt = 0; kt < 9; ++kt) {
      const int issued = (kt + ONBUF < 10) ? kt + ONBUF : 10;
      asm volatile("s_waitcnt vmcnt(%0)" :: "n"((issued - (kt + 2)) * 5) : "memory");
      __builtin_amdgcn_s_barrier();
      if (kt + ONBUF < 10) stage(kt + ONBUF);
    }
    return;
  }
  const int wm = wv >> 1, wn = wv & 1;
  f32x4 acc[2][5];
#pragma unroll
  for (int m = 0; m < 2; ++m)
#pragma unroll
    for (int n = 0; n < 5; ++n) acc[m][n] = (f32x4){0, 0, 0, 0};
  const u32 sbase = (u32)(size_t)smem;
  const u32 boff0 = O_OFF_W + g * 2560 + wn * 1280 + ((0 ^ (4 * (g & 1))) + (fr >> 2)) * 32 + (lane & 3) * 8;
  const u32 boff1 = O_OFF_W + g * 2560 + wn * 1280 + ((4 ^ (4 * (g & 1))) + (fr >> 2)) * 32 + (lane & 3) * 8;
  u32 aad = sbase + wm * 2 * 20 * 1024 + lane * 16;
  u32 bad[ONBUF][2];
#pragma unroll
  for (int b = 0; b < ONBUF; ++b) { bad[b][0] = sbase + b * OSTAGE + boff0; bad[b][1] = sbase + b * OSTAGE + boff1; asm volatile("" : "+v"(bad[b][0]), "+v"(bad[b][1])); }
  asm volatile("" : "+v"(aad));
  s16x8 af[2][2]; s16x4 bl[2][5], bh[2][5];
  auto ldop = [&](int kt, int ks, int i) {
    const int b = kt % ONBUF;
    if (i < 10) {
      const int n = i >> 1;
      if (i & 1) bh[ks][n] = __builtin_amdgcn_ds_read_tr16_b64_v4i16((LAS s16x4*)(bad[b][1] + ks * 10240 + n * 256));
      else       bl[ks][n] = __builtin_amdgcn_ds_read_tr16_b64_v4i16((LAS s16x4*)(bad[b][0] + ks * 10240 + n * 256));
    } else af[ks][i - 10] = *(const LAS s16x8*)(aad + ((i - 10) * 20 + 2 * kt + ks) * 1024);
  };
  auto mma1 = [&](int sl, int i) {
    const int m = i / 5, n = i % 5;
    s16x8 bb = __builtin_shufflevector(bl[sl][n], bh[sl][n], 0, 1, 2, 3, 4, 5, 6, 7);
    acc[m][n] = __builtin_amdgcn_mfma_f32_16x16x32_bf16(bb, af[sl][m], acc[m][n], 0, 0, 0);
  };
  __builtin_amdgcn_s_waitcnt(0xC07F);
  __builtin_amdgcn_s_barrier();
#pragma unroll
  for (int i = 0; i < 12; ++i) ldop(0, 0, i);
#pragma unroll
  for (int kt = 0; kt < 10; ++kt) {
    __builtin_amdgcn_sched_barrier(0);
#pragma unroll
    for (int i = 0; i < 10; ++i) {
      mma1(0, i);
      ldop(kt, 1, i); if (i < 2) ldop(kt, 1, 10 + i);
      __builtin_amdgcn_sched_barrier(0);
    }
    if (kt < 9) {
      __builtin_amdgcn_s_waitcnt(0xC07F);
      __builtin_amdgcn_s_barrier();
      __builtin_amdgcn_sched_barrier(0);
    }
#pragma unroll
    for (int i = 0; i < 10; ++i) {
      mma1(1, i);
      if (kt < 9) { ldop(kt + 1, 0, i); if (i < 2) ldop(kt + 1, 0, 10 + i); }
      __builtin_amdgcn_sched_barrier(0);
    }
  }
#pragma unroll
  for (int m = 0; m < 2; ++m)
#pragma unroll
    for (int n = 0; n < 5; ++n) {
      u32x2 o = {pk_bf16(acc[m][n][0], acc[m][n][1]), pk_bf16(acc[m][n][2], acc[m][n][3])};
      *(u32x2*)(out + (size_t)(row0 + wm * 32 + m * 16 + fr) * 1280 + 160 * ct + wn * 80 + n * 16 + 4 * g) = o;
    }
}
__global__ __launch_bounds__(512, 2) void k3(const char* __restrict__ ws, u16* __restrict__ out) {
  extern __shared__ __attribute__((aligned(16))) char smem[];
  const int bid = blockIdx.x, xcd = bid & 7, j = bid >> 3;
  o_block((const u16*)(ws + WS_PQ), (const u16*)(ws + WS_KVF), ws + WS_WOP, out, xcd * 4 + (j >> 2), j & 3, smem);
}

extern "C" void kernel_launch(void* const* d_in, const int* in_sizes, int n_in, void* d_out, int out_size, void* d_ws, size_t ws_size, hipStream_t stream) {
  constexpr int LDS1 = QNBUF * QSTAGE, LDS2 = 4 * CH_BYTES + 64, LDS3 = O_OFF_W + ONBUF * OSTAGE;
  static_assert(10 * 14720 <= LDS1, "aux LDS");
  (void)hipFuncSetAttribute((const void*)k1, hipFuncAttributeMaxDynamicSharedMemorySize, LDS1);
  (void)hipFuncSetAttribute((const void*)k2, hipFuncAttributeMaxDynamicSharedMemorySize, LDS2);
  (void)hipFuncSetAttribute((const void*)k3, hipFuncAttributeMaxDynamicSharedMemorySize, LDS3);
  P1 p1 = {(const u16*)d_in[0], (const u16*)d_in[1], (const u16*)d_in[2], (const u16*)d_in[3], (const u16*)d_in[4], (const u16*)d_in[5], (const u16*)d_in[6], (const u16*)d_in[7], (const u16*)d_in[8], (char*)d_ws};
  k1<<<256, 640, LDS1, stream>>>(p1);
  P2 p2 = {(const u16*)d_in[9], (u16*)d_out, (char*)d_ws};
  k2<<<192, 512, LDS2, stream>>>(p2);
  k3<<<128, 512, LDS3, stream>>>((const char*)d_ws, (u16*)d_out);
}
```

```cpp
#include <hip/hip_runtime.h>
#include <hip/hip_bf16.h>
#include <stdint.h>
#include <type_traits>
typedef unsigned short u16;
typedef unsigned int u32;
typedef float f32x2 __attribute__((ext_vector_type(2)));
typedef float f32x4 __attribute__((ext_vector_type(4)));
typedef float f32x16 __attribute__((ext_vector_type(16)));
typedef short s16x4 __attribute__((ext_vector_type(4)));
typedef short s16x8 __attribute__((ext_vector_type(8)));
typedef _Float16 h8 __attribute__((ext_vector_type(8)));
typedef _Float16 h2 __attribute__((ext_vector_type(2)));
typedef __bf16 bf16x2 __attribute__((ext_vector_type(2)));
typedef u32 u32x2 __attribute__((ext_vector_type(2)));
typedef u32 u32x4 __attribute__((ext_vector_type(4)));
#define DI __device__ __forceinline__
#define LAS __attribute__((address_space(3)))
#define GAS __attribute__((address_space(1)))
typedef GAS unsigned gu32; typedef GAS unsigned long long gu64;
#define RLX_AGENT __ATOMIC_RELAXED, __HIP_MEMORY_SCOPE_AGENT
DI float bf2f(u16 x) { return __uint_as_float(((u32)x) << 16); }
DI u32 pk_bf16(float a, float b) { bf16x2 r = __builtin_convertvector((f32x2){a, b}, bf16x2); return __builtin_bit_cast(u32, r); }
DI u32 pk_f16(float a, float b) { h2 r = __builtin_convertvector((f32x2){a, b}, h2); return __builtin_bit_cast(u32, r); }
DI float lo16(u32 x) { return __uint_as_float(x << 16); }
DI float hi16(u32 x) { return __uint_as_float(x & 0xffff0000u); }
DI float rbf(float f) { return lo16(pk_bf16(f, 0.f)); }
DI u32 phi_pair(float a, float b) {
  u32 t = pk_bf16(a, b);
  float ta = lo16(t), tb = hi16(t);
  float ea = __builtin_amdgcn_fmed3f(__builtin_amdgcn_exp2f(ta * 1.4426950408889634f), 0.f, 1.f);
  float eb = __builtin_amdgcn_fmed3f(__builtin_amdgcn_exp2f(tb * 1.4426950408889634f), 0.f, 1.f);
  return pk_bf16(ea + fmaxf(ta, 1e-6f), eb + fmaxf(tb, 1e-6f));
}
DI f32x2 cmul(f32x2 x, f32x2 y) {
  f32x2 t, z;
  asm("v_pk_mul_f32 %0, %1, %2 op_sel_hi:[0,1]" : "=v"(t) : "v"(x), "v"(y));
  asm("v_pk_fma_f32 %0, %1, %2, %3 op_sel:[1,1,0] op_sel_hi:[1,0,1] neg_lo:[0,1,0]" : "=v"(z) : "v"(x), "v"(y), "v"(t));
  return z;
}
DI f32x2 cexpP(float ar2, float airev, float P) {
  float e = __builtin_amdgcn_exp2f(ar2 * P);
  float t = __builtin_amdgcn_fractf(airev * P);
  float s = __builtin_amdgcn_sinf(t), c = __builtin_amdgcn_cosf(t);
  f32x2 r = {e * c, e * s};
  return r;
}
constexpr int S = 2048, D = 640, NS = 192;
constexpr u32 WS_PQ = 0;
constexpr u32 WS_WOP = 9u << 20;
constexpr u32 WS_UT = 10u << 20;
constexpr u32 WS_KC = 13u << 20;
constexpr u32 WS_KS = 14u << 20;
constexpr u32 WS_FLAG = 16u << 20;
constexpr u32 WS_ZERO = 17u << 20;
constexpr int KVACC_BYTES = 32 * 1024 * 4;
constexpr int CNT_OFF = KVACC_BYTES;
constexpr int CNT_KV = 0, CNT_T = 8, CNT_PQ = 16, CNT_KG = 32;
constexpr int ZERO_BYTES = KVACC_BYTES + 96 * 128;
constexpr u32 WS_SPAN = 18u << 20;
constexpr unsigned long long MAGIC = 0x9e3779b97f4a7c15ull;
constexpr unsigned SPIN_CAP = 1u << 17;
typedef __amdgpu_buffer_rsrc_t rsrc_t;

DI gu32* cnt_ptr(char* ws, int i) { return (gu32*)(ws + WS_ZERO + CNT_OFF + i * 128); }
DI void wait_init_t0(char* ws, bool& done) {
  if (done) return;
  gu64* f = (gu64*)(ws + WS_FLAG) + blockIdx.x;
  for (unsigned sp = 0; sp < SPIN_CAP; ++sp) { if (__hip_atomic_load(f, RLX_AGENT) == MAGIC) break; __builtin_amdgcn_s_sleep(4); }
  __hip_atomic_store(f, 0ull, RLX_AGENT);
  done = true;
}
DI void wait_cnt_t0(char* ws, int i, unsigned want) {
  gu32* c = cnt_ptr(ws, i);
  for (unsigned sp = 0; sp < SPIN_CAP; ++sp) { if (__hip_atomic_load(c, RLX_AGENT) >= want) break; __builtin_amdgcn_s_sleep(4); }
}
DI void publish(char* ws, int i, bool& initdone) {
  asm volatile("s_waitcnt vmcnt(0)" ::: "memory");
  __syncthreads();
  if (threadIdx.x == 0) { wait_init_t0(ws, initdone); __hip_atomic_fetch_add(cnt_ptr(ws, i), 1u, RLX_AGENT); }
}
DI void publish8(char* ws, int i, bool& initdone) {
  if (threadIdx.x == 0) wait_init_t0(ws, initdone);
  asm volatile("s_waitcnt vmcnt(0)" ::: "memory");
  __syncthreads();
  if (threadIdx.x < 8) __hip_atomic_fetch_add(cnt_ptr(ws, i + threadIdx.x), 1u, RLX_AGENT);
}
DI u32x4 ld16_sc(rsrc_t r, u32 off) { return __builtin_amdgcn_raw_buffer_load_b128(r, off, 0, 16); }
DI u32x2 ld8_sc(const void* p) { unsigned long long v = __hip_atomic_load((gu64*)p, RLX_AGENT); return (u32x2){(u32)v, (u32)(v >> 32)}; }
DI float ld4_sc(const void* p) { return __uint_as_float(__hip_atomic_load((gu32*)p, RLX_AGENT)); }
DI void acquire_all() {
  if (threadIdx.x == 0) { __builtin_amdgcn_fence(__ATOMIC_ACQUIRE, "agent"); asm volatile("s_waitcnt vmcnt(0)" ::: "memory"); }
  __syncthreads();
}
DI void st16_wt(rsrc_t r, u32 off, u32x4 v) { __builtin_amdgcn_raw_buffer_store_b128(v, r, off, 0, 16); }
DI void st8_wt(void* p, u32x2 v) { __hip_atomic_store((gu64*)p, ((unsigned long long)v.y << 32) | v.x, RLX_AGENT); }

#define TLS(i)
constexpr int QSTAGE = 36864, QNBUF = 4;
constexpr int TROW = 272, KG_REG = 14720;

template <bool KV>
DI void q_block(const u16* __restrict__ x, const u16* __restrict__ w, char* __restrict__ ws, int rt, int ct, char* smem, bool& initdone) {
  const int tid = threadIdx.x, lane = tid & 63, wv = __builtin_amdgcn_readfirstlane(tid >> 6);
  const int row0 = rt * 128;
  const int fr = lane & 15, g = lane >> 4;
  if (wv >= 4) {
    const int lw = wv - 4;
    const u16* xs[4]; const u16* ws_[5];
#pragma unroll
    for (int i = 0; i < 4; ++i) { int pc = lw * 4 + i; int row = 8 * pc + (lane >> 3); int ch = (lane & 7) ^ ((row >> 1) & 7); xs[i] = x + (size_t)(row0 + row) * D + 8 * ch; }
#pragma unroll
    for (int i = 0; i < 5; ++i) {
      int pc = lw * 5 + i; int sb = 4 * pc + (lane >> 4); int kb = sb / 10, nb = sb % 10; int row = ((lane >> 1) & 7) ^ (4 * (kb & 1)); int c = nb * 16 + 8 * (lane & 1);
      int gc;
      if (KV) { int wn2 = c / 80, cc = c % 80; gc = cc < 40 ? 640 + 80 * ct + 40 * wn2 + cc : 1280 + 80 * ct + 40 * wn2 + (cc - 40); }
      else gc = 160 * ct + c;
      ws_[i] = w + (size_t)(8 * kb + row) * 1920 + gc;
    }
    auto stage = [&](int kt) {
      char* b = smem + (kt % QNBUF) * QSTAGE;
#pragma unroll
      for (int i = 0; i < 4; ++i) __builtin_amdgcn_global_load_lds((const u32*)(xs[i] + kt * 64), (LAS u32*)(b + (lw * 4 + i) * 1024), 16, 0, 0);
#pragma unroll
      for (int i = 0; i < 5; ++i) __builtin_amdgcn_global_load_lds((const u32*)(ws_[i] + (size_t)kt * 64 * 1920), (LAS u32*)(b + 16384 + (lw * 5 + i) * 1024), 16, 0, 0);
    };
#pragma unroll
    for (int i = 0; i < QNBUF; ++i) stage(i);
    asm volatile("s_waitcnt vmcnt(%0)" :: "n"((QNBUF - 1) * 9) : "memory");
    __builtin_amdgcn_s_barrier();
#pragma unroll
    for (int kt = 0; kt < 9; ++kt) {
      const int issued = (kt + QNBUF < 10) ? kt + QNBUF : 10;
      asm volatile("s_waitcnt vmcnt(%0)" :: "n"((issued - (kt + 2)) * 9) : "memory");
      __builtin_amdgcn_s_barrier();
      if (kt + QNBUF < 10) stage(kt + QNBUF);
    }
    if (KV) { __builtin_amdgcn_s_barrier(); __builtin_amdgcn_s_barrier(); }
  } else {
    const int wm = wv >> 1, wn = wv & 1;
    f32x4 acc[4][5];
#pragma unroll
    for (int m = 0; m < 4; ++m)
#pragma unroll
      for (int n = 0; n < 5; ++n) acc[m][n] = (f32x4){0, 0, 0, 0};
    const u32 aoff0 = (wm * 64 + fr) * 128 + (((0 + g) ^ (fr >> 1)) * 16);
    const u32 aoff1 = (wm * 64 + fr) * 128 + (((4 + g) ^ (fr >> 1)) * 16);
    const u32 boff0 = 16384 + g * 2560 + wn * 1280 + ((0 ^ (4 * (g & 1))) + (fr >> 2)) * 32 + (lane & 3) * 8;
    const u32 boff1 = 16384 + g * 2560 + wn * 1280 + ((4 ^ (4 * (g & 1))) + (fr >> 2)) * 32 + (lane & 3) * 8;
    const u32 sbase = (u32)(size_t)smem;
    s16x8 af[2][4]; s16x4 bl[2][5], bh[2][5];
    u32 aad[QNBUF][2], bad[QNBUF][2];
#pragma unroll
    for (int b = 0; b < QNBUF; ++b) { aad[b][0] = sbase + b * QSTAGE + aoff0; aad[b][1] = sbase + b * QSTAGE + aoff1; bad[b][0] = sbase + b * QSTAGE + boff0; bad[b][1] = sbase + b * QSTAGE + boff1;
      asm volatile("" : "+v"(aad[b][0]), "+v"(aad[b][1]), "+v"(bad[b][0]), "+v"(bad[b][1])); }
    auto ldop = [&](int kt, int ks, int i) {
      const int b = kt % QNBUF;
      if (i < 10) {
        const int n = i >> 1;
        if (i & 1) bh[ks][n] = __builtin_amdgcn_ds_read_tr16_b64_v4i16((LAS s16x4*)(bad[b][1] + ks * 10240 + n * 256));
        else       bl[ks][n] = __builtin_amdgcn_ds_read_tr16_b64_v4i16((LAS s16x4*)(bad[b][0] + ks * 10240 + n * 256));
      } else af[ks][i - 10] = *(const LAS s16x8*)(aad[b][ks] + (i - 10) * 2048);
    };
    auto mma1 = [&](int sl, int i) {
      const int m = i / 5, n = i % 5;
      s16x8 bb = __builtin_shufflevector(bl[sl][n], bh[sl][n], 0, 1, 2, 3, 4, 5, 6, 7);
      acc[m][n] = KV ? __builtin_amdgcn_mfma_f32_16x16x32_bf16(af[sl][m], bb, acc[m][n], 0, 0, 0)
                     : __builtin_amdgcn_mfma_f32_16x16x32_bf16(bb, af[sl][m], acc[m][n], 0, 0, 0);
    };
    __builtin_amdgcn_s_barrier();
    TLS(15);
#pragma unroll
    for (int i = 0; i < 14; ++i) ldop(0, 0, i);
#pragma unroll
    for (int kt = 0; kt < 10; ++kt) {
      __builtin_amdgcn_sched_barrier(0);
#pragma unroll
      for (int i = 0; i < 20; ++i) {
        mma1(0, i);
        if (i < 14) ldop(kt, 1, i);
        __builtin_amdgcn_sched_barrier(0);
      }
      if (kt < 9) {
        __builtin_amdgcn_s_waitcnt(0xC07F);
        __builtin_amdgcn_s_barrier();
        __builtin_amdgcn_sched_barrier(0);
      }
#pragma unroll
      for (int i = 0; i < 20; ++i) {
        mma1(1, i);
        if (kt < 9 && i < 14) ldop(kt + 1, 0, i);
        __builtin_amdgcn_sched_barrier(0);
      }
    }
    TLS(14);
    if (!KV) {
      char* pq = ws + WS_PQ;
#pragma unroll
      for (int m = 0; m < 4; ++m)
#pragma unroll
        for (int n = 0; n < 5; ++n) {
          u32x2 o = {phi_pair(acc[m][n][0], acc[m][n][1]), phi_pair(acc[m][n][2], acc[m][n][3])};
          const int col = 160 * ct + wn * 80 + n * 16 + 4 * g; const int h = col / 20, q = (col % 20) >> 2;
          st8_wt(pq + 2 * (((size_t)((rt * 8 + wm * 4 + m) * 32 + h) * 5 + q) * 64 + fr * 4), o);
        }
    } else {
      u32x2 pk[4][5];
      const bool isk = fr < 8;
#pragma unroll
      for (int m = 0; m < 4; ++m)
#pragma unroll
        for (int n = 0; n < 5; ++n) {
          if (n <= 2) { pk[m][n].x = phi_pair(acc[m][n][0], acc[m][n][1]); pk[m][n].y = phi_pair(acc[m][n][2], acc[m][n][3]); }
          if (n >= 2) {
            u32 v0 = pk_bf16(acc[m][n][0], acc[m][n][1]), v1 = pk_bf16(acc[m][n][2], acc[m][n][3]);
            if (n > 2) { pk[m][n].x = v0; pk[m][n].y = v1; } else { pk[m][n].x = isk ? pk[m][n].x : v0; pk[m][n].y = isk ? pk[m][n].y : v1; }
          }
        }
      f32x4 kv[10];
#pragma unroll
      for (int i = 0; i < 10; ++i) kv[i] = (f32x4){0, 0, 0, 0};
      const s16x8 ones = {0x3f80, 0x3f80, 0x3f80, 0x3f80, 0x3f80, 0x3f80, 0x3f80, 0x3f80};
#pragma unroll
      for (int u = 0; u < 2; ++u) {
        s16x8 f[5];
#pragma unroll
        for (int n = 0; n < 5; ++n) f[n] = __builtin_bit_cast(s16x8, (u32x4){pk[2 * u][n].x, pk[2 * u][n].y, pk[2 * u + 1][n].x, pk[2 * u + 1][n].y});
        kv[0] = __builtin_amdgcn_mfma_f32_16x16x32_bf16(f[0], f[2], kv[0], 0, 0, 0);
        kv[1] = __builtin_amdgcn_mfma_f32_16x16x32_bf16(f[0], f[3], kv[1], 0, 0, 0);
        kv[2] = __builtin_amdgcn_mfma_f32_16x16x32_bf16(f[1], f[2], kv[2], 0, 0, 0);
        kv[3] = __builtin_amdgcn_mfma_f32_16x16x32_bf16(f[1], f[3], kv[3], 0, 0, 0);
        kv[4] = __builtin_amdgcn_mfma_f32_16x16x32_bf16(f[1], f[4], kv[4], 0, 0, 0);
        kv[5] = __builtin_amdgcn_mfma_f32_16x16x32_bf16(f[2], f[3], kv[5], 0, 0, 0);
        kv[6] = __builtin_amdgcn_mfma_f32_16x16x32_bf16(f[2], f[4], kv[6], 0, 0, 0);
        kv[7] = __builtin_amdgcn_mfma_f32_16x16x32_bf16(f[0], ones, kv[7], 0, 0, 0);
        kv[8] = __builtin_amdgcn_mfma_f32_16x16x32_bf16(f[1], ones, kv[8], 0, 0, 0);
        kv[9] = __builtin_amdgcn_mfma_f32_16x16x32_bf16(f[2], ones, kv[9], 0, 0, 0);
      }
      __builtin_amdgcn_s_barrier();
      f32x4* red = (f32x4*)smem + (wm * 2 + wn) * 640 + lane;
#pragma unroll
      for (int i = 0; i < 10; ++i) red[i * 64] = kv[i];
      __builtin_amdgcn_s_waitcnt(0xC07F);
      __builtin_amdgcn_s_barrier();
    }
  }
  if (KV) {
    if (tid == 0) wait_init_t0(ws, initdone);
    __syncthreads();
    const int hl4 = tid >> 7, tile = (tid >> 6) & 1, row = lane & 15, gk = lane >> 4, gg = row >> 2, r = row & 3;
    const int wn = hl4 >> 1, hl = hl4 & 1;
    const float* raw = (const float*)smem;
    float* acc = (float*)(ws + WS_ZERO) + ((size_t)((ct * 4 + hl4) * 2 + tile) * 8) * 64 + lane;
    const bool rowok = tile == 0 || r < 2;
    const bool isz = tile == 0 && r == 3;
    const int e = (tile == 0 ? 5 * gg + r : 5 * gg + 3 + r) + 20 * hl + 40;
    const int et = e >> 4;
#pragma unroll
    for (int j = 0; j < 8; ++j) {
      const int dd = 8 * gk + j;
      if (rowok && dd < 20) {
        const int d = dd + 20 * hl, dt = d >> 4;
        int t, ln;
        if (isz) { t = 7 + dt; ln = ((d & 15) >> 2) * 16; }
        else { t = dt == 0 ? et - 2 : (dt == 1 ? et : et + 2); ln = ((d & 15) >> 2) * 16 + (e & 15); }
        const int idx = (wn * 640 + t * 64 + ln) * 4 + (d & 3);
        const float v = raw[idx] + raw[2 * 640 * 4 + idx];
        __hip_atomic_fetch_add((GAS float*)(acc + j * 64), v, RLX_AGENT);
      }
    }
    TLS(1);
    publish8(ws, CNT_KV, initdone);
  } else {
    TLS(1);
    publish(ws, CNT_PQ + rt, initdone);
  }
}

struct KG { float ar2, airev; f32x2 cc; };
DI f32x2 cm(f32x2 x, f32x2 y) {
  f32x2 xx = {x.x, x.x}, xy = {x.y, x.y}, ys = {-y.y, y.x};
  return __builtin_elementwise_fma(xy, ys, xx * y);
}
DI f32x2 cmc(f32x2 x, f32x2 y) {
  f32x2 xx = {x.x, x.x}, xy = {x.y, x.y}, yc = {y.x, -y.y}, ys = {y.y, y.x};
  return __builtin_elementwise_fma(xy, ys, xx * yc);
}
DI void kgen_chunk(float a2, float av, f32x2 Ein, char* reg, f32x16& kacc) {
  const int lane = threadIdx.x & 63;
  u32* tE = (u32*)(reg) + lane;
  u32* tF = (u32*)(reg + 21 * TROW) + lane;
  constexpr int R = TROW / 4;
  auto stE = [&](int r, f32x2 v) { tE[r * R] = pk_f16(v.x, v.y); };
  auto stF = [&](int r, f32x2 v) { tF[r * R] = pk_f16(v.x, v.y); };
  f32x2 w1 = cexpP(a2, av, 1.f), v1 = cexpP(a2, av, 8.f), B256 = cexpP(a2, av, 256.f);
  f32x2 w2 = cm(w1, w1), w4 = cm(w2, w2);
  f32x2 v2 = cm(v1, v1), v4 = cm(v2, v2), v8 = cm(v4, v4), v16 = cm(v8, v8);
  f32x2 B512 = cm(B256, B256), B1024 = cm(B512, B512), B768 = cm(B512, B256);
  f32x2 E0 = {Ein.x, -Ein.y};
  f32x2 E1 = cmc(E0, w1), E2 = cmc(E0, w2), E3 = cmc(E1, w2), E4 = cmc(E0, w4), E5 = cmc(E1, w4), E6 = cmc(E2, w4), E7 = cmc(E3, w4);
  stE(0, E0); stE(1, E1); stE(2, E2); stE(3, E3); stE(4, E4); stE(5, E5); stE(6, E6); stE(7, E7);
  stE(8, cmc(E0, B256)); stE(9, cmc(E2, B256)); stE(10, cmc(E4, B256)); stE(11, cmc(E6, B256));
  stE(12, cmc(E0, B512)); stE(13, cmc(E4, B512)); stE(14, cmc(E0, B768)); stE(15, cmc(E4, B768));
  f32x2 G0 = cmc(E0, B1024);
  stE(16, G0); stE(17, cmc(G0, B256)); stE(18, cmc(G0, B512)); stE(19, cmc(G0, B768)); stE(20, cmc(G0, B1024));
  f32x2 F[8];
  F[0].x = 1.f; F[0].y = 0.f; F[1] = v1; F[2] = v2; F[3] = cm(v2, v1); F[4] = v4; F[5] = cm(v4, v1); F[6] = cm(v4, v2); F[7] = cm(v4, F[3]);
  f32x2 v24 = cm(v16, v8);
#pragma unroll
  for (int i = 0; i < 8; ++i) {
    stF(i, F[i]);
    stF(8 + i, i == 0 ? v8 : cm(F[i], v8));
    stF(16 + i, i == 0 ? v16 : cm(F[i], v16));
    stF(24 + i, i == 0 ? v24 : cm(F[i], v24));
  }
  __builtin_amdgcn_wave_barrier();
  const int r = lane & 31, hh = lane >> 5;
  const char* pa = reg + (r < 21 ? r : 20) * TROW + 16 * hh;
  const char* pb = reg + (21 + r) * TROW + 16 * hh;
#pragma unroll
  for (int s = 0; s < 8; ++s) {
    h8 a = *(const h8*)(pa + 32 * s);
    h8 b = *(const h8*)(pb + 32 * s);
    kacc = __builtin_amdgcn_mfma_f32_32x32x16_f16(a, b, kacc, 0, 0, 0);
  }
  __builtin_amdgcn_wave_barrier();
}
DI void kgen_params(const u16* lar_, const u16* aim_, const u16* cr_, const u16* ci_, const u16* ldt_, int d, KG (&k)[3], float& sc, float& isc) {
  const int lane = threadIdx.x & 63;
  u16 plar[3], paim[3], pcr[3], pci[3];
#pragma unroll
  for (int c = 0; c < 3; ++c) { int n = d * NS + 64 * c + lane; plar[c] = lar_[n]; paim[c] = aim_[n]; pcr[c] = cr_[n]; pci[c] = ci_[n]; }
  const float dt = rbf(expf(bf2f(ldt_[d])));
  float cmax = 0.f;
#pragma unroll
  for (int c = 0; c < 3; ++c) {
    float Are = -rbf(expf(bf2f(plar[c])));
    float ar = Are * dt, ai = bf2f(paim[c]) * dt;
    k[c].ar2 = ar * 1.4426950408889634f; k[c].airev = ai * 0.15915494309189535f;
    k[c].cc.x = bf2f(pcr[c]) * dt * 2.f; k[c].cc.y = bf2f(pci[c]) * dt * 2.f;
    cmax = fmaxf(cmax, fmaxf(fabsf(k[c].cc.x), fabsf(k[c].cc.y)));
  }
#pragma unroll
  for (int o = 32; o >= 1; o >>= 1) cmax = fmaxf(cmax, __shfl_xor(cmax, o));
  int ce = (int)((__float_as_uint(cmax) >> 23) & 0xff);
  ce = ce < 30 ? 30 : (ce > 220 ? 220 : ce);
  sc = __uint_as_float((u32)(127 + 125 - ce) << 23);
  isc = __uint_as_float((u32)(ce + 2) << 23);
}
DI void kgen_store(const f32x16& kacc, float isc, char* lds, rsrc_t rs, char* ws, int d) {
  const int lane = threadIdx.x & 63;
  u16* kc = (u16*)lds;
  const int i = lane & 31, hh = lane >> 5;
  if (lane < 4) ((u32*)lds)[320 + lane] = 0;
  __builtin_amdgcn_wave_barrier();
#pragma unroll
  for (int rg = 0; rg < 12; ++rg) {
    const int q = rg >> 2, sub = rg & 3;
    int idx; bool ok = true;
    if (q == 0) idx = 8 * i + 4 * hh + sub;
    else if (q == 1) idx = hh == 0 ? 256 + 4 * i + sub : 384 + 64 * (sub >> 1) + 2 * i + (sub & 1);
    else { idx = hh == 0 ? 512 + 32 * sub + i : 640; ok = (hh == 0) || (sub == 0 && i == 0); }
    u32 hv = pk_f16(kacc[rg] * 128.f, 0.f);
    if (ok) kc[idx] = (u16)hv;
  }
  __builtin_amdgcn_wave_barrier();
  st16_wt(rs, WS_KC + d * 1296 + lane * 16, *(const u32x4*)(lds + lane * 16));
  if (lane < 17) st16_wt(rs, WS_KC + d * 1296 + 1024 + lane * 16, *(const u32x4*)(lds + 1024 + lane * 16));
  if (lane == 0) __hip_atomic_store((gu32*)(ws + WS_KS + d * 4), __float_as_uint(isc * (1.f / 128.f)), RLX_AGENT);
  __builtin_amdgcn_wave_barrier();
}
DI void aux_block(const u16* __restrict__ u, const u16* __restrict__ lar_, const u16* __restrict__ aim_, const u16* __restrict__ cr_, const u16* __restrict__ ci_,
                  const u16* __restrict__ ldt_, const u16* __restrict__ wo, char* __restrict__ ws, rsrc_t rs, int ab, char* smem, bool& initdone) {
  const int tid = threadIdx.x, lane = tid & 63, wv = __builtin_amdgcn_readfirstlane(tid >> 6);
  char* reg = smem + wv * KG_REG;
  if (wv < 5) {
    const int tile = ab * 5 + wv; const int rb = tile / 10, cb = tile % 10;
    u16* t = (u16*)reg;
#pragma unroll
    for (int i = 0; i < 8; ++i) {
      const int row = i * 8 + (lane >> 3), ch = lane & 7;
      u32x4 v = *(const u32x4*)(u + (size_t)(rb * 64 + row) * D + cb * 64 + ch * 8);
      u32* p = (u32*)(t + row * 66 + ch * 8);
      p[0] = v.x; p[1] = v.y; p[2] = v.z; p[3] = v.w;
    }
    __builtin_amdgcn_wave_barrier();
#pragma unroll
    for (int i = 0; i < 8; ++i) {
      const int c = i * 8 + (lane >> 3), r8 = lane & 7;
      u32 o[4];
#pragma unroll
      for (int k = 0; k < 4; ++k) o[k] = (u32)t[(r8 * 8 + 2 * k) * 66 + c] | ((u32)t[(r8 * 8 + 2 * k + 1) * 66 + c] << 16);
      st16_wt(rs, WS_UT + ((cb * 64 + c) * S + rb * 64 + r8 * 8) * 2, (u32x4){o[0], o[1], o[2], o[3]});
    }
    __builtin_amdgcn_wave_barrier();
  } else {
#pragma unroll
    for (int it = 0; it < 5; ++it) {
      const int uu = (wv - 5) * 64 + lane + 192 * it;
      if (uu < 800) {
        const int U = ab * 800 + uu; const int img = U / 1280, un = U % 1280; const int ct = img / 10, kt = img % 10;
        const int sb = un >> 4, kb = sb / 10, nb = sb % 10, j = ((un >> 1) & 7) ^ (4 * (kb & 1));
        const int T = 2 * kt + (kb >> 2), G = T / 5, tt = T % 5, ii = 8 * tt + j, hh = ii / 5, r = ii % 5;
        const int k = 160 * G + 20 * hh + 5 * (kb & 3) + r;
        st16_wt(rs, WS_WOP + U * 16, *(const u32x4*)(wo + (size_t)k * D + 160 * ct + nb * 16 + 8 * (un & 1)));
      }
    }
  }
  publish8(ws, CNT_T, initdone);
  TLS(15);
  {
    KG k[3]; float sc, isc;
    kgen_params(lar_, aim_, cr_, ci_, ldt_, ab * 10 + wv, k, sc, isc);
    asm volatile("" :: "v"(sc), "v"(k[2].ar2));
    TLS(9);
    f32x16 kacc;
#pragma unroll
    for (int i = 0; i < 16; ++i) kacc[i] = 0.f;
#pragma unroll
    for (int c = 0; c < 3; ++c) kgen_chunk(k[c].ar2, k[c].airev, k[c].cc * sc, reg, kacc);
    kgen_store(kacc, isc, reg, rs, ws, ab * 10 + wv);
  }
  TLS(14);
  {
    const int ch2 = wv / 3, c2 = wv % 3;
    f32x16 kacc;
#pragma unroll
    for (int i = 0; i < 16; ++i) kacc[i] = 0.f;
    float isc = 0.f;
    if (wv < 6) {
      KG k[3]; float sc;
      kgen_params(lar_, aim_, cr_, ci_, ldt_, ab * 10 + 8 + ch2, k, sc, isc);
      const float a2 = c2 == 0 ? k[0].ar2 : (c2 == 1 ? k[1].ar2 : k[2].ar2);
      const float av = c2 == 0 ? k[0].airev : (c2 == 1 ? k[1].airev : k[2].airev);
      const f32x2 cc = c2 == 0 ? k[0].cc : (c2 == 1 ? k[1].cc : k[2].cc);
      kgen_chunk(a2, av, cc * sc, reg, kacc);
      if (c2 != 0) {
#pragma unroll
        for (int i = 0; i < 3; ++i) *(f32x4*)(reg + (i * 64 + lane) * 16) = (f32x4){kacc[4 * i], kacc[4 * i + 1], kacc[4 * i + 2], kacc[4 * i + 3]};
      }
    }
    __syncthreads();
    if (wv < 6 && c2 == 0) {
#pragma unroll
      for (int i = 0; i < 3; ++i) {
        f32x4 a = *(const f32x4*)(reg + KG_REG + (i * 64 + lane) * 16), b = *(const f32x4*)(reg + 2 * KG_REG + (i * 64 + lane) * 16);
#pragma unroll
        for (int q = 0; q < 4; ++q) kacc[4 * i + q] += a[q] + b[q];
      }
      kgen_store(kacc, isc, reg, rs, ws, ab * 10 + 8 + ch2);
    }
  }
  TLS(1);
  publish(ws, CNT_KG + ab, initdone);
}

constexpr int CH_BYTES = 23296, UC_STRIDE = 4160, KP_OFF = 16640, KP_STRIDE = 1280, KC_OFF = 21760;
constexpr int PSPLIT = 22;
DI void conv_block(char* __restrict__ ws, rsrc_t rs, const u16* __restrict__ dsk_, u16* __restrict__ out, int cb, char* smem, bool& initdone) {
  const int tid = threadIdx.x, lane = tid & 63, wv = __builtin_amdgcn_readfirstlane(tid >> 6), ch = wv >> 1, hf = wv & 1;
  const int d0 = cb * 4;
  char* reg = smem + ch * CH_BYTES;
  float* red = (float*)(smem + 4 * CH_BYTES);
  if (tid == 0) { wait_init_t0(ws, initdone); wait_cnt_t0(ws, CNT_T + (blockIdx.x & 7), 64); wait_cnt_t0(ws, CNT_KG + d0 / 10, 1); wait_cnt_t0(ws, CNT_KG + (d0 + 3) / 10, 1); }
  TLS(3);
  __syncthreads();
  TLS(4);
  u32x4 uv[2];
#pragma unroll
  for (int i = 0; i < 2; ++i) uv[i] = ld16_sc(rs, WS_UT + ((d0 + ch) * S + 8 * (hf * 128 + lane + 64 * i)) * 2);
  if (hf == 0) { u32x4 v = ld16_sc(rs, WS_KC + (d0 + ch) * 1296 + lane * 16); *(u32x4*)(reg + KC_OFF + lane * 16) = v; }
  else if (lane < 17) { u32x4 v = ld16_sc(rs, WS_KC + (d0 + ch) * 1296 + 1024 + lane * 16); *(u32x4*)(reg + KC_OFF + 1024 + lane * 16) = v; }
  const float ks = ld4_sc(ws + WS_KS + (d0 + ch) * 4);
  float m = 0.f;
#pragma unroll
  for (int i = 0; i < 2; ++i)
#pragma unroll
    for (int k = 0; k < 4; ++k) m = fmaxf(m, fmaxf(fabsf(lo16(uv[i][k])), fabsf(hi16(uv[i][k]))));
#pragma unroll
  for (int o = 32; o >= 1; o >>= 1) m = fmaxf(m, __shfl_xor(m, o));
  if (lane == 0) red[wv] = m;
  __syncthreads();
  TLS(11);
  m = fmaxf(red[2 * ch], red[2 * ch + 1]);
  int e = (int)((__float_as_uint(m) >> 23) & 0xff);
  e = e < 30 ? 30 : (e > 220 ? 220 : e);
  const float us = __uint_as_float((u32)(127 + 140 - e) << 23), ius = __uint_as_float((u32)(e - 13) << 23);
#pragma unroll
  for (int i = 0; i < 2; ++i) {
    const int q = hf * 128 + lane + 64 * i;
    u32x4 o = {pk_f16(hi16(uv[i].w) * us, lo16(uv[i].w) * us), pk_f16(hi16(uv[i].z) * us, lo16(uv[i].z) * us),
               pk_f16(hi16(uv[i].y) * us, lo16(uv[i].y) * us), pk_f16(hi16(uv[i].x) * us, lo16(uv[i].x) * us)};
    *(u32x4*)(reg + 2 * (2040 - 8 * q)) = o;
  }
  if (hf == 0 && lane < 4) *(u32x4*)(reg + 4096 + 16 * lane) = (u32x4){0, 0, 0, 0};
  {
    const u16* kc = (const u16*)(reg + KC_OFF);
    char* kp = reg + KP_OFF;
    if (hf == 1 && lane < 60) *(u32x4*)(kp + (lane & 3) * KP_STRIDE + (lane >> 2) * 16) = (u32x4){0, 0, 0, 0};
#pragma unroll
    for (int k = 0; k < 2; ++k) {
      const int q = hf + 2 * (lane + 64 * k);
      u32 v[8];
#pragma unroll
      for (int j8 = 0; j8 < 8; ++j8) {
        int j = 8 * q + j8;
        u32 fb = __float_as_uint((float)j); fb += 0x7fffu + ((fb >> 16) & 1u);
        int idx = j < 256 ? j : 256 + (int)((fb >> 16) - 0x4380u);
        v[j8] = kc[idx];
      }
      u32x4 o = {v[0] | (v[1] << 16), v[2] | (v[3] << 16), v[4] | (v[5] << 16), v[6] | (v[7] << 16)};
      *(u32x4*)(kp + (q & 3) * KP_STRIDE + ((q >> 2) + 15) * 16) = o;
    }
  }
  __syncthreads();
#pragma unroll
  for (int k = 0; k < 3; ++k) {
    const int q = hf * 130 + lane + 64 * k;
    if (lane + 64 * k < 130) {
      u32x4 a = *(const u32x4*)(reg + 16 * q);
      u32x2 b = *(const u32x2*)(reg + 16 * q + 16);
      u32x4 c1 = {__builtin_amdgcn_alignbit(a.y, a.x, 16), __builtin_amdgcn_alignbit(a.z, a.y, 16), __builtin_amdgcn_alignbit(a.w, a.z, 16), __builtin_amdgcn_alignbit(b.x, a.w, 16)};
      u32x4 c2 = {a.y, a.z, a.w, b.x};
      u32x4 c3 = {c1.y, c1.z, c1.w, __builtin_amdgcn_alignbit(b.y, b.x, 16)};
      *(u32x4*)(reg + 1 * UC_STRIDE + 16 * q) = c1;
      *(u32x4*)(reg + 2 * UC_STRIDE + 16 * q) = c2;
      *(u32x4*)(reg + 3 * UC_STRIDE + 16 * q) = c3;
    }
  }
  __syncthreads();
  TLS(12);
  f32x4 acc[4][2];
#pragma unroll
  for (int t = 0; t < 4; ++t) { acc[t][0] = (f32x4){0, 0, 0, 0}; acc[t][1] = (f32x4){0, 0, 0, 0}; }
  const int b = lane & 15, g = lane >> 4;
  {
    const int cpy = (3 - b) & 3;
    const char* pa0 = reg + cpy * UC_STRIDE + 2 * (2044 - 4 * (b >> 2) + 8 * g);
    const char* pb0 = reg + KP_OFF + g * KP_STRIDE + 16 * (b + 15);
    typedef const volatile LAS u32x2* lp2;
    typedef const volatile LAS u32x4* lp4;
    const u32 abase = (u32)(size_t)(pa0 - 64 * 63 - 32);
    const u32 bbase = (u32)(size_t)(pb0 - 16 * 63);
    constexpr int DP = 2;
    u32x2 Ab[DP + 1][4]; u32x4 Bb[DP + 1][4];
    auto run = [&](auto P0c, auto P1c) {
      constexpr int P0 = decltype(P0c)::value, P1 = decltype(P1c)::value;
      auto issue = [&](int p) {
        const int sl = p % (DP + 1);
        const u32 oa = 64 * (63 - p);
        Ab[sl][0] = *(lp2)(abase + oa + 32); Ab[sl][1] = *(lp2)(abase + oa + 40);
        Ab[sl][2] = *(lp2)(abase + oa);      Ab[sl][3] = *(lp2)(abase + oa + 8);
#pragma unroll
        for (int t = 0; t < 4; ++t) if (t >= (p >> 4)) Bb[sl][t] = *(lp4)(bbase + 16 * (63 - p) + 256 * t);
      };
#pragma unroll
      for (int p = P0; p < P0 + DP; ++p) issue(p);
#pragma unroll
      for (int p = P0; p < P1; ++p) {
        if (p + DP < P1) issue(p + DP);
        __builtin_amdgcn_sched_barrier(0);
        const int sl = p % (DP + 1);
        h8 A0 = __builtin_bit_cast(h8, (u32x4){Ab[sl][0].x, Ab[sl][0].y, Ab[sl][1].x, Ab[sl][1].y});
        h8 A1 = __builtin_bit_cast(h8, (u32x4){Ab[sl][2].x, Ab[sl][2].y, Ab[sl][3].x, Ab[sl][3].y});
#pragma unroll
        for (int t = 0; t < 4; ++t) if (t >= (p >> 4)) {
          h8 B = __builtin_bit_cast(h8, Bb[sl][t]);
          acc[t][0] = __builtin_amdgcn_mfma_f32_16x16x32_f16(A0, B, acc[t][0], 0, 0, 0);
          acc[t][1] = __builtin_amdgcn_mfma_f32_16x16x32_f16(A1, B, acc[t][1], 0, 0, 0);
        }
        __builtin_amdgcn_sched_barrier(0);
      }
    };
    if (hf == 0) run(std::integral_constant<int, 0>{}, std::integral_constant<int, PSPLIT>{});
    else run(std::integral_constant<int, PSPLIT>{}, std::integral_constant<int, 64>{});
  }
  __syncthreads();
  TLS(13);
  f32x4* xch = (f32x4*)(reg + UC_STRIDE) + lane;
  if (hf == 1) {
#pragma unroll
    for (int t = 1; t < 4; ++t) { xch[(2 * (t - 1)) * 64] = acc[t][0]; xch[(2 * (t - 1) + 1) * 64] = acc[t][1]; }
  }
  __syncthreads();
  if (hf == 0) {
    const float ysc = ks * ius;
    char* yb = reg + 3 * UC_STRIDE;
#pragma unroll
    for (int t = 0; t < 4; ++t)
#pragma unroll
      for (int r = 0; r < 2; ++r) {
        f32x4 a = acc[t][r];
        if (t > 0) a += xch[(2 * (t - 1) + r) * 64];
        u32x2 o = {pk_bf16(a[0] * ysc, a[1] * ysc), pk_bf16(a[2] * ysc, a[3] * ysc)};
        *(u32x2*)(yb + 2 * (32 * (16 * t + b) + 16 * r + 4 * g)) = o;
      }
    if (lane == 0) red[8 + ch] = ius;
  }
  __syncthreads();
  {
    u32x2 yv[4], uu[4]; float iu[4], dk[4];
#pragma unroll
    for (int c = 0; c < 4; ++c) {
      yv[c] = *(const u32x2*)(smem + c * CH_BYTES + 3 * UC_STRIDE + 8 * tid);
      uu[c] = *(const u32x2*)(smem + c * CH_BYTES + 2 * (2044 - 4 * tid));
      iu[c] = red[8 + c]; dk[c] = bf2f(dsk_[d0 + c]);
    }
#pragma unroll
    for (int i = 0; i < 4; ++i) {
      float r[4];
#pragma unroll
      for (int c = 0; c < 4; ++c) {
        u32 yw = yv[c][i >> 1]; float y = (i & 1) ? hi16(yw) : lo16(yw);
        const u32 uw = (i < 2) ? uu[c].y : uu[c].x; const u16 ub = (i & 1) ? (u16)uw : (u16)(uw >> 16); float uf = (float)__builtin_bit_cast(_Float16, ub) * iu[c];
        r[c] = y + rbf(uf * dk[c]);
      }
      u32x2 o = {pk_bf16(r[0], r[1]), pk_bf16(r[2], r[3])};
      *(u32x2*)(out + (size_t)(4 * tid + i) * 1280 + 640 + d0) = o;
    }
  }
  __syncthreads();
}

constexpr int OSTAGE = 20480, ONBUF = 4, O_OFF_W = 81920;
DI void o_block(char* __restrict__ ws, u16* __restrict__ out, int rt, int ct, char* smem, bool& initdone) {
  const int tid = threadIdx.x, lane = tid & 63, wv = __builtin_amdgcn_readfirstlane(tid >> 6);
  const int row0 = rt * 64, fr = lane & 15, g = lane >> 4;
  const int lw = wv - 4;
  if (tid == 0) { wait_init_t0(ws, initdone); wait_cnt_t0(ws, CNT_T + (blockIdx.x & 7), 64); }
  acquire_all();
  const char* wsrc = ws + WS_WOP + (size_t)ct * 10 * OSTAGE + lw * 5120 + lane * 16;
  auto stage = [&](int kt) {
    char* b = smem + O_OFF_W + (kt % ONBUF) * OSTAGE + lw * 5120;
#pragma unroll
    for (int i = 0; i < 5; ++i) __builtin_amdgcn_global_load_lds((const u32*)(wsrc + kt * OSTAGE + i * 1024), (LAS u32*)(b + i * 1024), 16, 0, 0);
  };
  if (wv >= 4) {
#pragma unroll
    for (int i = 0; i < ONBUF; ++i) stage(i);
  }
  if (tid == 0) { wait_cnt_t0(ws, CNT_PQ + (rt >> 1), 4); wait_cnt_t0(ws, CNT_KV + (blockIdx.x & 7), 128); }
  TLS(6);
  asm volatile("s_waitcnt lgkmcnt(0)" ::: "memory");
  __builtin_amdgcn_s_barrier();
  TLS(7);
  const char* pq = ws + WS_PQ;
  const float* kva = (const float*)(ws + WS_ZERO);
  float kr[4][2][8]; u32x2 pa[4][4], pb[4][4];
#pragma unroll
  for (int hh = 0; hh < 4; ++hh) {
#pragma unroll
    for (int tl = 0; tl < 2; ++tl)
#pragma unroll
      for (int j = 0; j < 8; ++j) kr[hh][tl][j] = ld4_sc(kva + ((size_t)((4 * wv + hh) * 2 + tl) * 8 + j) * 64 + lane);
#pragma unroll
    for (int st = 0; st < 4; ++st) {
      const char* p = pq + 2 * (((size_t)((rt * 4 + st) * 32 + 4 * wv + hh) * 5) * 64 + fr * 4);
      pa[hh][st] = ld8_sc(p + (g < 3 ? 2 * g : 4) * 128);
      pb[hh][st] = ld8_sc(p + (g < 2 ? 2 * g + 1 : 4) * 128);
    }
  }
  __builtin_amdgcn_sched_barrier(0);
  {
    s16x8 kf[4][2];
#pragma unroll
    for (int hh = 0; hh < 4; ++hh)
#pragma unroll
      for (int tl = 0; tl < 2; ++tl)
        kf[hh][tl] = __builtin_bit_cast(s16x8, (u32x4){pk_bf16(kr[hh][tl][0], kr[hh][tl][1]), pk_bf16(kr[hh][tl][2], kr[hh][tl][3]), pk_bf16(kr[hh][tl][4], kr[hh][tl][5]), pk_bf16(kr[hh][tl][6], kr[hh][tl][7])});
    asm volatile("" :: "v"(kf[3][1]), "v"(pa[3][3]), "v"(pb[3][3]));
    TLS(10);
    const int G = wv >> 1, half = wv & 1;
#pragma unroll
    for (int st = 0; st < 4; ++st) {
      float vals[20];
#pragma unroll
      for (int hh = 0; hh < 4; ++hh) {
        u32x2 a = pa[hh][st], bq = pb[hh][st];
        a.x = g < 3 ? a.x : 0u; a.y = g < 3 ? a.y : 0u; bq.x = g < 2 ? bq.x : 0u; bq.y = g < 2 ? bq.y : 0u;
        s16x8 pf = __builtin_bit_cast(s16x8, (u32x4){a.x, a.y, bq.x, bq.y});
        f32x4 z4 = {0, 0, 0, 0};
        f32x4 t0 = __builtin_amdgcn_mfma_f32_16x16x32_bf16(kf[hh][0], pf, z4, 0, 0, 0);
        f32x4 t1 = __builtin_amdgcn_mfma_f32_16x16x32_bf16(kf[hh][1], pf, z4, 0, 0, 0);
        u32 n01 = pk_bf16(t0[0], t0[1]), n2d = pk_bf16(t0[2], t0[3]), n34 = pk_bf16(t1[0], t1[1]);
        float den = rbf(hi16(n2d) + 1e-6f);
        float rd = __builtin_amdgcn_rcpf(den);
        vals[5 * hh + 0] = lo16(n01) * rd; vals[5 * hh + 1] = hi16(n01) * rd; vals[5 * hh + 2] = lo16(n2d) * rd;
        vals[5 * hh + 3] = lo16(n34) * rd; vals[5 * hh + 4] = hi16(n34) * rd;
      }
#pragma unroll
      for (int i = 0; i < 5; ++i) {
        const int byte = 40 * half + 8 * i;
        const int T = 5 * G + (byte >> 4);
        *(u32x2*)(smem + (st * 20 + T) * 1024 + lane * 16 + (byte & 8)) = (u32x2){pk_bf16(vals[4 * i], vals[4 * i + 1]), pk_bf16(vals[4 * i + 2], vals[4 * i + 3])};
      }
    }
  }
  if (wv >= 4) {
    asm volatile("s_waitcnt vmcnt(0) lgkmcnt(0)" ::: "memory");
    __builtin_amdgcn_s_barrier();
#pragma unroll
    for (int kt = 0; kt < 9; ++kt) {
      const int issued = (kt + ONBUF < 10) ? kt + ONBUF : 10;
      asm volatile("s_waitcnt vmcnt(%0)" :: "n"((issued - (kt + 2)) * 5) : "memory");
      __builtin_amdgcn_s_barrier();
      if (kt + ONBUF < 10) stage(kt + ONBUF);
    }
    return;
  }
  const int wm = wv >> 1, wn = wv & 1;
  f32x4 acc[2][5];
#pragma unroll
  for (int m = 0; m < 2; ++m)
#pragma unroll
    for (int n = 0; n < 5; ++n) acc[m][n] = (f32x4){0, 0, 0, 0};
  const u32 sbase = (u32)(size_t)smem;
  const u32 boff0 = O_OFF_W + g * 2560 + wn * 1280 + ((0 ^ (4 * (g & 1))) + (fr >> 2)) * 32 + (lane & 3) * 8;
  const u32 boff1 = O_OFF_W + g * 2560 + wn * 1280 + ((4 ^ (4 * (g & 1))) + (fr >> 2)) * 32 + (lane & 3) * 8;
  u32 aad = sbase + wm * 2 * 20 * 1024 + lane * 16;
  u32 bad[ONBUF][2];
#pragma unroll
  for (int b = 0; b < ONBUF; ++b) { bad[b][0] = sbase + b * OSTAGE + boff0; bad[b][1] = sbase + b * OSTAGE + boff1; asm volatile("" : "+v"(bad[b][0]), "+v"(bad[b][1])); }
  asm volatile("" : "+v"(aad));
  s16x8 af[2][2]; s16x4 bl[2][5], bh[2][5];
  auto ldop = [&](int kt, int ks, int i) {
    const int b = kt % ONBUF;
    if (i < 10) {
      const int n = i >> 1;
      if (i & 1) bh[ks][n] = __builtin_amdgcn_ds_read_tr16_b64_v4i16((LAS s16x4*)(bad[b][1] + ks * 10240 + n * 256));
      else       bl[ks][n] = __builtin_amdgcn_ds_read_tr16_b64_v4i16((LAS s16x4*)(bad[b][0] + ks * 10240 + n * 256));
    } else af[ks][i - 10] = *(const LAS s16x8*)(aad + ((i - 10) * 20 + 2 * kt + ks) * 1024);
  };
  auto mma1 = [&](int sl, int i) {
    const int m = i / 5, n = i % 5;
    s16x8 bb = __builtin_shufflevector(bl[sl][n], bh[sl][n], 0, 1, 2, 3, 4, 5, 6, 7);
    acc[m][n] = __builtin_amdgcn_mfma_f32_16x16x32_bf16(bb, af[sl][m], acc[m][n], 0, 0, 0);
  };
  __builtin_amdgcn_s_waitcnt(0xC07F);
  __builtin_amdgcn_s_barrier();
  TLS(9);
#pragma unroll
  for (int i = 0; i < 12; ++i) ldop(0, 0, i);
#pragma unroll
  for (int kt = 0; kt < 10; ++kt) {
    __builtin_amdgcn_sched_barrier(0);
#pragma unroll
    for (int i = 0; i < 10; ++i) {
      mma1(0, i);
      ldop(kt, 1, i); if (i < 2) ldop(kt, 1, 10 + i);
      __builtin_amdgcn_sched_barrier(0);
    }
    if (kt < 9) {
      __builtin_amdgcn_s_waitcnt(0xC07F);
      __builtin_amdgcn_s_barrier();
      __builtin_amdgcn_sched_barrier(0);
    }
#pragma unroll
    for (int i = 0; i < 10; ++i) {
      mma1(1, i);
      if (kt < 9) { ldop(kt + 1, 0, i); if (i < 2) ldop(kt + 1, 0, 10 + i); }
      __builtin_amdgcn_sched_barrier(0);
    }
  }
#pragma unroll
  for (int m = 0; m < 2; ++m)
#pragma unroll
    for (int n = 0; n < 5; ++n) {
      u32x2 o = {pk_bf16(acc[m][n][0], acc[m][n][1]), pk_bf16(acc[m][n][2], acc[m][n][3])};
      *(u32x2*)(out + (size_t)(row0 + wm * 32 + m * 16 + fr) * 1280 + 160 * ct + wn * 80 + n * 16 + 4 * g) = o;
    }
}

struct P { const u16 *x, *w, *wo, *u, *lar, *aim, *cr, *ci, *ldt, *dsk; u16* out; char* ws; };
__global__ __launch_bounds__(512, 2) void kmain(P p) {
  extern __shared__ __attribute__((aligned(16))) char smem[];
  const int b = blockIdx.x, tid = threadIdx.x;
  char* ws = p.ws;
  const rsrc_t rs = __builtin_amdgcn_make_buffer_rsrc((void*)ws, 0, WS_SPAN, 0x00020000);
  bool initdone = false;
  TLS(0);
  if (b == 255) {
    for (int i = tid; i < ZERO_BYTES / 16; i += 512) st16_wt(rs, WS_ZERO + i * 16, (u32x4){0, 0, 0, 0});
    asm volatile("s_waitcnt vmcnt(0)" ::: "memory");
    __syncthreads();
    if (tid < 256) __hip_atomic_store((gu64*)(ws + WS_FLAG) + tid, MAGIC, RLX_AGENT);
  }
  int conv_task = -1, o_task = -1;
  if (b < 192) {
    const int xcd = b & 7, j = b >> 3;
    const int rt = (xcd >> 1) * 4 + (j & 3), c6 = j >> 2;
    if (c6 < 4) q_block<true>(p.x, p.w, ws, rt, (xcd & 1) * 4 + c6, smem, initdone);
    else q_block<false>(p.x, p.w, ws, rt, (xcd & 1) * 2 + (c6 - 4), smem, initdone);
    if (j & 1) o_task = xcd * 12 + (j >> 1);
    else conv_task = 64 + xcd * 12 + (j >> 1);
  } else {
    const int ab = b - 192;
    aux_block(p.u, p.lar, p.aim, p.cr, p.ci, p.ldt, p.wo, ws, rs, ab, smem, initdone);
    conv_task = ab;
    if (ab < 32) o_task = 96 + ab;
  }
  TLS(2);
  __syncthreads();
  if (conv_task >= 0) conv_block(ws, rs, p.dsk, p.out, conv_task, smem, initdone);
  TLS(5);
  if (o_task >= 0) o_block(ws, p.out, o_task >> 2, o_task & 3, smem, initdone);
  TLS(8);
}

extern "C" void kernel_launch(void* const* d_in, const int* in_sizes, int n_in, void* d_out, int out_size, void* d_ws, size_t ws_size, hipStream_t stream) {
  constexpr int LDS = O_OFF_W + ONBUF * OSTAGE;
  static_assert(QNBUF * QSTAGE <= LDS && 8 * KG_REG <= LDS && 4 * CH_BYTES + 64 <= LDS, "LDS");
  (void)hipFuncSetAttribute((const void*)kmain, hipFuncAttributeMaxDynamicSharedMemorySize, LDS);
  P p = {(const u16*)d_in[0], (const u16*)d_in[1], (const u16*)d_in[2], (const u16*)d_in[3], (const u16*)d_in[4], (const u16*)d_in[5], (const u16*)d_in[6], (const u16*)d_in[7], (const u16*)d_in[8], (const u16*)d_in[9], (u16*)d_out, (char*)d_ws};
  kmain<<<256, 512, LDS, stream>>>(p);
}
```

```cpp
#include <hip/hip_runtime.h>
#include <hip/hip_bf16.h>
#include <stdint.h>
#include <type_traits>
typedef unsigned short u16;
typedef unsigned int u32;
typedef float f32x2 __attribute__((ext_vector_type(2)));
typedef float f32x4 __attribute__((ext_vector_type(4)));
typedef float f32x16 __attribute__((ext_vector_type(16)));
typedef short s16x4 __attribute__((ext_vector_type(4)));
typedef short s16x8 __attribute__((ext_vector_type(8)));
typedef _Float16 h8 __attribute__((ext_vector_type(8)));
typedef _Float16 h2 __attribute__((ext_vector_type(2)));
typedef __bf16 bf16x2 __attribute__((ext_vector_type(2)));
typedef u32 u32x2 __attribute__((ext_vector_type(2)));
typedef u32 u32x4 __attribute__((ext_vector_type(4)));
#define DI __device__ __forceinline__
#define LAS __attribute__((address_space(3)))
#define GAS __attribute__((address_space(1)))
typedef GAS unsigned gu32; typedef GAS unsigned long long gu64;
#define RLX_AGENT __ATOMIC_RELAXED, __HIP_MEMORY_SCOPE_AGENT
DI float bf2f(u16 x) { return __uint_as_float(((u32)x) << 16); }
DI u32 pk_bf16(float a, float b) { bf16x2 r = __builtin_convertvector((f32x2){a, b}, bf16x2); return __builtin_bit_cast(u32, r); }
DI u32 pk_f16(float a, float b) { h2 r = __builtin_convertvector((f32x2){a, b}, h2); return __builtin_bit_cast(u32, r); }
DI float lo16(u32 x) { return __uint_as_float(x << 16); }
DI float hi16(u32 x) { return __uint_as_float(x & 0xffff0000u); }
DI float rbf(float f) { return lo16(pk_bf16(f, 0.f)); }
DI u32 phi_pair(float a, float b) {
  u32 t = pk_bf16(a, b);
  float ta = lo16(t), tb = hi16(t);
  float ea = __builtin_amdgcn_fmed3f(__builtin_amdgcn_exp2f(ta * 1.4426950408889634f), 0.f, 1.f);
  float eb = __builtin_amdgcn_fmed3f(__builtin_amdgcn_exp2f(tb * 1.4426950408889634f), 0.f, 1.f);
  return pk_bf16(ea + fmaxf(ta, 1e-6f), eb + fmaxf(tb, 1e-6f));
}
DI f32x2 cmul(f32x2 x, f32x2 y) {
  f32x2 t, z;
  asm("v_pk_mul_f32 %0, %1, %2 op_sel_hi:[0,1]" : "=v"(t) : "v"(x), "v"(y));
  asm("v_pk_fma_f32 %0, %1, %2, %3 op_sel:[1,1,0] op_sel_hi:[1,0,1] neg_lo:[0,1,0]" : "=v"(z) : "v"(x), "v"(y), "v"(t));
  return z;
}
DI f32x2 cexpP(float ar2, float airev, float P) {
  float e = __builtin_amdgcn_exp2f(ar2 * P);
  float t = __builtin_amdgcn_fractf(airev * P);
  float s = __builtin_amdgcn_sinf(t), c = __builtin_amdgcn_cosf(t);
  f32x2 r = {e * c, e * s};
  return r;
}
constexpr int S = 2048, D = 640, NS = 192;
constexpr u32 WS_PQ = 0;
constexpr u32 WS_WOP = 9u << 20;
constexpr u32 WS_UT = 10u << 20;
constexpr u32 WS_KC = 4u << 20;
constexpr u32 WS_KS = 14u << 20;
constexpr u32 WS_FLAG = 16u << 20;
constexpr u32 WS_ZERO = 17u << 20;
constexpr int KVACC_BYTES = 32 * 1024 * 4;
constexpr int CNT_OFF = KVACC_BYTES;
constexpr int CNT_KV = 0, CNT_T = 8, CNT_PQ = 16, CNT_KG = 32, ZPAD = 40;
constexpr int ZERO_BYTES = KVACC_BYTES + 48 * 128;
constexpr u32 WS_SPAN = 18u << 20;
constexpr unsigned long long MAGIC = 0x9e3779b97f4a7c15ull;
constexpr unsigned SPIN_CAP = 1u << 17;
typedef __amdgpu_buffer_rsrc_t rsrc_t;

DI gu32* cnt_ptr(char* ws, int i) { return (gu32*)(ws + WS_ZERO + CNT_OFF + i * 128); }
DI void wait_init_t0(char* ws, bool& done) {
  if (done) return;
  gu64* f = (gu64*)(ws + WS_FLAG) + blockIdx.x * 8;
  for (unsigned sp = 0; sp < SPIN_CAP; ++sp) {
    bool ok = true;
#pragma unroll
    for (int i = 0; i < 8; ++i) ok &= __hip_atomic_load(f + i, RLX_AGENT) == MAGIC;
    if (ok) break;
    __builtin_amdgcn_s_sleep(2);
  }
#pragma unroll
  for (int i = 0; i < 8; ++i) __hip_atomic_store(f + i, 0ull, RLX_AGENT);
  done = true;
}
DI void wait_cnt_t0(char* ws, int i, unsigned want) {
  gu32* c = cnt_ptr(ws, i);
  if (__hip_atomic_load(c, RLX_AGENT) >= want) return;
  unsigned v0, v1, v2, v3;
#define PLD(v) asm volatile("global_load_dword %0, %1, off sc1" : "=v"(v) : "v"(c) : "memory")
  PLD(v0); __builtin_amdgcn_s_sleep(2); PLD(v1); __builtin_amdgcn_s_sleep(2); PLD(v2); __builtin_amdgcn_s_sleep(2);
  for (unsigned sp = 0; sp < SPIN_CAP; ++sp) {
    PLD(v3); asm volatile("s_waitcnt vmcnt(3)" : "+v"(v0) :: "memory"); if (v0 >= want) break; __builtin_amdgcn_s_sleep(2);
    PLD(v0); asm volatile("s_waitcnt vmcnt(3)" : "+v"(v1) :: "memory"); if (v1 >= want) break; __builtin_amdgcn_s_sleep(2);
    PLD(v1); asm volatile("s_waitcnt vmcnt(3)" : "+v"(v2) :: "memory"); if (v2 >= want) break; __builtin_amdgcn_s_sleep(2);
    PLD(v2); asm volatile("s_waitcnt vmcnt(3)" : "+v"(v3) :: "memory"); if (v3 >= want) break; __builtin_amdgcn_s_sleep(2);
  }
  asm volatile("s_waitcnt vmcnt(0)" : "+v"(v0), "+v"(v1), "+v"(v2), "+v"(v3) :: "memory");
#undef PLD
}
DI void publish(char* ws, int i, bool& initdone) {
  asm volatile("s_waitcnt vmcnt(0)" ::: "memory");
  __syncthreads();
  if (threadIdx.x == 0) { wait_init_t0(ws, initdone); __hip_atomic_fetch_add(cnt_ptr(ws, i), 1u, RLX_AGENT); }
}
DI void publish8(char* ws, int i, bool& initdone) {
  if (threadIdx.x == 0) wait_init_t0(ws, initdone);
  asm volatile("s_waitcnt vmcnt(0)" ::: "memory");
  __syncthreads();
  if (threadIdx.x < 8) __hip_atomic_fetch_add(cnt_ptr(ws, i + threadIdx.x), 1u, RLX_AGENT);
}
DI u32x4 ld16_sc(rsrc_t r, u32 off) { return __builtin_amdgcn_raw_buffer_load_b128(r, off, 0, 16); }
DI u32x2 ld8_sc(const void* p) { unsigned long long v = __hip_atomic_load((gu64*)p, RLX_AGENT); return (u32x2){(u32)v, (u32)(v >> 32)}; }
DI float ld4_sc(const void* p) { return __uint_as_float(__hip_atomic_load((gu32*)p, RLX_AGENT)); }
DI void acquire_all() {
  if (threadIdx.x == 0) { __builtin_amdgcn_fence(__ATOMIC_ACQUIRE, "agent"); asm volatile("s_waitcnt vmcnt(0)" ::: "memory"); }
  __syncthreads();
}
DI void st16_wt(rsrc_t r, u32 off, u32x4 v) { __builtin_amdgcn_raw_buffer_store_b128(v, r, off, 0, 16); }
DI void st8_wt(void* p, u32x2 v) { __hip_atomic_store((gu64*)p, ((unsigned long long)v.y << 32) | v.x, RLX_AGENT); }

#define TLS(i)
constexpr int QSTAGE = 36864, QNBUF = 4;
constexpr int TROW = 272, KG_REG = 14720;

DI f32x2 cm(f32x2 x, f32x2 y) {
  f32x2 xx = {x.x, x.x}, xy = {x.y, x.y}, ys = {-y.y, y.x};
  return __builtin_elementwise_fma(xy, ys, xx * y);
}
DI f32x2 cmc(f32x2 x, f32x2 y) {
  f32x2 xx = {x.x, x.x}, xy = {x.y, x.y}, yc = {y.x, -y.y}, ys = {y.y, y.x};
  return __builtin_elementwise_fma(xy, ys, xx * yc);
}
DI void kgen_chunk(float a2, float av, f32x2 Ein, char* reg, f32x16& kacc) {
  const int lane = threadIdx.x & 63;
  u32* tE = (u32*)(reg) + lane;
  u32* tF = (u32*)(reg + 21 * TROW) + lane;
  constexpr int R = TROW / 4;
  auto stE = [&](int r, f32x2 v) { tE[r * R] = pk_f16(v.x, v.y); };
  auto stF = [&](int r, f32x2 v) { tF[r * R] = pk_f16(v.x, v.y); };
  f32x2 w1 = cexpP(a2, av, 1.f), v1 = cexpP(a2, av, 8.f), B256 = cexpP(a2, av, 256.f);
  f32x2 w2 = cm(w1, w1), w4 = cm(w2, w2);
  f32x2 v2 = cm(v1, v1), v4 = cm(v2, v2), v8 = cm(v4, v4), v16 = cm(v8, v8);
  f32x2 B512 = cm(B256, B256), B1024 = cm(B512, B512), B768 = cm(B512, B256);
  f32x2 E0 = {Ein.x, -Ein.y};
  f32x2 E1 = cmc(E0, w1), E2 = cmc(E0, w2), E3 = cmc(E1, w2), E4 = cmc(E0, w4), E5 = cmc(E1, w4), E6 = cmc(E2, w4), E7 = cmc(E3, w4);
  stE(0, E0); stE(1, E1); stE(2, E2); stE(3, E3); stE(4, E4); stE(5, E5); stE(6, E6); stE(7, E7);
  stE(8, cmc(E0, B256)); stE(9, cmc(E2, B256)); stE(10, cmc(E4, B256)); stE(11, cmc(E6, B256));
  stE(12, cmc(E0, B512)); stE(13, cmc(E4, B512)); stE(14, cmc(E0, B768)); stE(15, cmc(E4, B768));
  f32x2 G0 = cmc(E0, B1024);
  stE(16, G0); stE(17, cmc(G0, B256)); stE(18, cmc(G0, B512)); stE(19, cmc(G0, B768)); stE(20, cmc(G0, B1024));
  f32x2 F[8];
  F[0].x = 1.f; F[0].y = 0.f; F[1] = v1; F[2] = v2; F[3] = cm(v2, v1); F[4] = v4; F[5] = cm(v4, v1); F[6] = cm(v4, v2); F[7] = cm(v4, F[3]);
  f32x2 v24 = cm(v16, v8);
#pragma unroll
  for (int i = 0; i < 8; ++i) {
    stF(i, F[i]);
    stF(8 + i, i == 0 ? v8 : cm(F[i], v8));
    stF(16 + i, i == 0 ? v16 : cm(F[i], v16));
    stF(24 + i, i == 0 ? v24 : cm(F[i], v24));
  }
  __builtin_amdgcn_wave_barrier();
  const int r = lane & 31, hh = lane >> 5;
  const char* pa = reg + (r < 21 ? r : 20) * TROW + 16 * hh;
  const char* pb = reg + (21 + r) * TROW + 16 * hh;
#pragma unroll
  for (int s = 0; s < 8; ++s) {
    h8 a = *(const h8*)(pa + 32 * s);
    h8 b = *(const h8*)(pb + 32 * s);
    kacc = __builtin_amdgcn_mfma_f32_32x32x16_f16(a, b, kacc, 0, 0, 0);
  }
  __builtin_amdgcn_wave_barrier();
}

struct KGP { const u16 *lar, *aim, *cr, *ci, *ldt; };
struct KGL { u16 pcr[3], pci[3], plar, paim, pldt; };
DI void kgen_load(const KGP& p, int d, int c, KGL& l) {
  const int lane = threadIdx.x & 63;
#pragma unroll
  for (int q = 0; q < 3; ++q) { int n = d * NS + 64 * q + lane; l.pcr[q] = p.cr[n]; l.pci[q] = p.ci[n]; }
  l.plar = p.lar[d * NS + 64 * c + lane]; l.paim = p.aim[d * NS + 64 * c + lane];
  l.pldt = p.ldt[d];
}
DI float kgen_task(const KGL& l, int c, char* reg, float* dst) {
  const int lane = threadIdx.x & 63;
  const float dt = rbf(expf(bf2f(l.pldt)));
  float cmax = 0.f;
#pragma unroll
  for (int q = 0; q < 3; ++q) cmax = fmaxf(cmax, fmaxf(fabsf(bf2f(l.pcr[q])), fabsf(bf2f(l.pci[q]))));
  cmax *= dt * 2.f;
#pragma unroll
  for (int o = 32; o >= 1; o >>= 1) cmax = fmaxf(cmax, __shfl_xor(cmax, o));
  int ce = (int)((__float_as_uint(cmax) >> 23) & 0xff);
  ce = ce < 30 ? 30 : (ce > 220 ? 220 : ce);
  const float sc = __uint_as_float((u32)(127 + 125 - ce) << 23);
  const float isc = __uint_as_float((u32)(ce + 2) << 23);
  const float Are = -rbf(expf(bf2f(l.plar)));
  const float ar2 = Are * dt * 1.4426950408889634f, airev = bf2f(l.paim) * dt * 0.15915494309189535f;
  const u16 mcr = c == 0 ? l.pcr[0] : (c == 1 ? l.pcr[1] : l.pcr[2]), mci = c == 0 ? l.pci[0] : (c == 1 ? l.pci[1] : l.pci[2]);
  f32x2 E = {bf2f(mcr) * dt * 2.f * sc, bf2f(mci) * dt * 2.f * sc};
  f32x16 kacc;
#pragma unroll
  for (int i = 0; i < 16; ++i) kacc[i] = 0.f;
  kgen_chunk(ar2, airev, E, reg, kacc);
  const int i = lane & 31, hh = lane >> 5;
#pragma unroll
  for (int rg = 0; rg < 12; ++rg) {
    const int q = rg >> 2, sub = rg & 3;
    int idx; bool ok = true;
    if (q == 0) idx = 8 * i + 4 * hh + sub;
    else if (q == 1) idx = hh == 0 ? 256 + 4 * i + sub : 384 + 64 * (sub >> 1) + 2 * i + (sub & 1);
    else { idx = hh == 0 ? 512 + 32 * sub + i : 640; ok = (hh == 0) || (sub == 0 && i == 0); }
    if (ok) dst[idx] = kacc[rg];
  }
  return isc * (1.f / 128.f);
}

template <bool KV>
DI void q_block(const u16* __restrict__ x, const u16* __restrict__ w, char* __restrict__ ws, rsrc_t rs, int rt, int ct, char* smem, bool& initdone) {
  const int tid = threadIdx.x, lane = tid & 63, wv = __builtin_amdgcn_readfirstlane(tid >> 6);
  const int row0 = rt * 128;
  const int fr = lane & 15, g = lane >> 4;
  if (wv >= 4) {
    const int lw = wv - 4;
    const u16* xs[4]; const u16* ws_[5];
#pragma unroll
    for (int i = 0; i < 4; ++i) { int pc = lw * 4 + i; int row = 8 * pc + (lane >> 3); int ch = (lane & 7) ^ ((row >> 1) & 7); xs[i] = x + (size_t)(row0 + row) * D + 8 * ch; }
#pragma unroll
    for (int i = 0; i < 5; ++i) {
      int pc = lw * 5 + i; int sb = 4 * pc + (lane >> 4); int kb = sb / 10, nb = sb % 10; int row = ((lane >> 1) & 7) ^ (4 * (kb & 1)); int c = nb * 16 + 8 * (lane & 1);
      int gc;
      if (KV) { int wn2 = c / 80, cc = c % 80; gc = cc < 40 ? 640 + 80 * ct + 40 * wn2 + cc : 1280 + 80 * ct + 40 * wn2 + (cc - 40); }
      else gc = 160 * ct + c;
      ws_[i] = w + (size_t)(8 * kb + row) * 1920 + gc;
    }
    auto stage = [&](int kt) {
      char* b = smem + (kt & (QNBUF - 1)) * QSTAGE;
#pragma unroll
      for (int i = 0; i < 4; ++i) __builtin_amdgcn_global_load_lds((const u32*)(xs[i] + kt * 64), (LAS u32*)(b + (lw * 4 + i) * 1024), 16, 0, 0);
#pragma unroll
      for (int i = 0; i < 5; ++i) __builtin_amdgcn_global_load_lds((const u32*)(ws_[i] + (size_t)kt * 64 * 1920), (LAS u32*)(b + 16384 + (lw * 5 + i) * 1024), 16, 0, 0);
    };
#pragma unroll 1
    for (int i = 0; i < QNBUF; ++i) stage(i);
    asm volatile("s_waitcnt vmcnt(%0)" :: "n"((QNBUF - 1) * 9) : "memory");
    __builtin_amdgcn_s_barrier();
#pragma unroll 1
    for (int kt = 0; kt < 6; ++kt) {
      asm volatile("s_waitcnt vmcnt(18)" ::: "memory");
      __builtin_amdgcn_s_barrier();
      stage(kt + QNBUF);
    }
    asm volatile("s_waitcnt vmcnt(18)" ::: "memory"); __builtin_amdgcn_s_barrier();
    asm volatile("s_waitcnt vmcnt(9)" ::: "memory");  __builtin_amdgcn_s_barrier();
    asm volatile("s_waitcnt vmcnt(0)" ::: "memory");  __builtin_amdgcn_s_barrier();
    if (KV) { __builtin_amdgcn_s_barrier(); __builtin_amdgcn_s_barrier(); }
  } else {
    const int wm = wv >> 1, wn = wv & 1;
    f32x4 acc[4][5];
#pragma unroll
    for (int m = 0; m < 4; ++m)
#pragma unroll
      for (int n = 0; n < 5; ++n) acc[m][n] = (f32x4){0, 0, 0, 0};
    const u32 aoff0 = (wm * 64 + fr) * 128 + (((0 + g) ^ (fr >> 1)) * 16);
    const u32 aoff1 = (wm * 64 + fr) * 128 + (((4 + g) ^ (fr >> 1)) * 16);
    const u32 boff0 = 16384 + g * 2560 + wn * 1280 + ((0 ^ (4 * (g & 1))) + (fr >> 2)) * 32 + (lane & 3) * 8;
    const u32 boff1 = 16384 + g * 2560 + wn * 1280 + ((4 ^ (4 * (g & 1))) + (fr >> 2)) * 32 + (lane & 3) * 8;
    const u32 sbase = (u32)(size_t)smem;
    s16x8 af[2][4]; s16x4 bl[2][5], bh[2][5];
    u32 a0 = sbase + aoff0, a1 = sbase + aoff1, b0 = sbase + boff0, b1 = sbase + boff1;
    auto ldop = [&](int ks, int i) {
      if (i < 10) {
        const int n = i >> 1;
        if (i & 1) bh[ks][n] = __builtin_amdgcn_ds_read_tr16_b64_v4i16((LAS s16x4*)(b1 + ks * 10240 + n * 256));
        else       bl[ks][n] = __builtin_amdgcn_ds_read_tr16_b64_v4i16((LAS s16x4*)(b0 + ks * 10240 + n * 256));
      } else af[ks][i - 10] = *(const LAS s16x8*)((ks ? a1 : a0) + (i - 10) * 2048);
    };
    auto mma1 = [&](int sl, int i) {
      const int m = i / 5, n = i % 5;
      s16x8 bb = __builtin_shufflevector(bl[sl][n], bh[sl][n], 0, 1, 2, 3, 4, 5, 6, 7);
      acc[m][n] = KV ? __builtin_amdgcn_mfma_f32_16x16x32_bf16(af[sl][m], bb, acc[m][n], 0, 0, 0)
                     : __builtin_amdgcn_mfma_f32_16x16x32_bf16(bb, af[sl][m], acc[m][n], 0, 0, 0);
    };
    __builtin_amdgcn_s_barrier();
    TLS(15);
#pragma unroll
    for (int i = 0; i < 14; ++i) ldop(0, i);
    int cur = 0;
#pragma unroll 1
    for (int kt = 0; kt < 10; ++kt) {
      __builtin_amdgcn_sched_barrier(0);
#pragma unroll
      for (int i = 0; i < 20; ++i) {
        mma1(0, i);
        if (i < 14) ldop(1, i);
        __builtin_amdgcn_sched_barrier(0);
      }
      cur = (cur + 1) & (QNBUF - 1);
      const u32 nb = sbase + cur * QSTAGE;
      a0 = nb + aoff0; a1 = nb + aoff1; b0 = nb + boff0; b1 = nb + boff1;
      __builtin_amdgcn_s_waitcnt(0xC07F);
      if (kt < 9) __builtin_amdgcn_s_barrier();
      __builtin_amdgcn_sched_barrier(0);
#pragma unroll
      for (int i = 0; i < 20; ++i) {
        mma1(1, i);
        if (i < 14) ldop(0, i);
        __builtin_amdgcn_sched_barrier(0);
      }
    }
    TLS(14);
    if (!KV) {
      char* pq = ws + WS_PQ;
#pragma unroll
      for (int m = 0; m < 4; ++m)
#pragma unroll
        for (int n = 0; n < 5; ++n) {
          u32x2 o = {phi_pair(acc[m][n][0], acc[m][n][1]), phi_pair(acc[m][n][2], acc[m][n][3])};
          const int col = 160 * ct + wn * 80 + n * 16 + 4 * g; const int h = col / 20, q = (col % 20) >> 2;
          st8_wt(pq + 2 * (((size_t)((rt * 8 + wm * 4 + m) * 32 + h) * 5 + q) * 64 + fr * 4), o);
        }
    } else {
      u32x2 pk[4][5];
      const bool isk = fr < 8;
#pragma unroll
      for (int m = 0; m < 4; ++m)
#pragma unroll
        for (int n = 0; n < 5; ++n) {
          if (n <= 2) { pk[m][n].x = phi_pair(acc[m][n][0], acc[m][n][1]); pk[m][n].y = phi_pair(acc[m][n][2], acc[m][n][3]); }
          if (n >= 2) {
            u32 v0 = pk_bf16(acc[m][n][0], acc[m][n][1]), v1 = pk_bf16(acc[m][n][2], acc[m][n][3]);
            if (n > 2) { pk[m][n].x = v0; pk[m][n].y = v1; } else { pk[m][n].x = isk ? pk[m][n].x : v0; pk[m][n].y = isk ? pk[m][n].y : v1; }
          }
        }
      f32x4 kv[10];
#pragma unroll
      for (int i = 0; i < 10; ++i) kv[i] = (f32x4){0, 0, 0, 0};
      const s16x8 ones = {0x3f80, 0x3f80, 0x3f80, 0x3f80, 0x3f80, 0x3f80, 0x3f80, 0x3f80};
#pragma unroll
      for (int u = 0; u < 2; ++u) {
        s16x8 f[5];
#pragma unroll
        for (int n = 0; n < 5; ++n) f[n] = __builtin_bit_cast(s16x8, (u32x4){pk[2 * u][n].x, pk[2 * u][n].y, pk[2 * u + 1][n].x, pk[2 * u + 1][n].y});
        kv[0] = __builtin_amdgcn_mfma_f32_16x16x32_bf16(f[0], f[2], kv[0], 0, 0, 0);
        kv[1] = __builtin_amdgcn_mfma_f32_16x16x32_bf16(f[0], f[3], kv[1], 0, 0, 0);
        kv[2] = __builtin_amdgcn_mfma_f32_16x16x32_bf16(f[1], f[2], kv[2], 0, 0, 0);
        kv[3] = __builtin_amdgcn_mfma_f32_16x16x32_bf16(f[1], f[3], kv[3], 0, 0, 0);
        kv[4] = __builtin_amdgcn_mfma_f32_16x16x32_bf16(f[1], f[4], kv[4], 0, 0, 0);
        kv[5] = __builtin_amdgcn_mfma_f32_16x16x32_bf16(f[2], f[3], kv[5], 0, 0, 0);
        kv[6] = __builtin_amdgcn_mfma_f32_16x16x32_bf16(f[2], f[4], kv[6], 0, 0, 0);
        kv[7] = __builtin_amdgcn_mfma_f32_16x16x32_bf16(f[0], ones, kv[7], 0, 0, 0);
        kv[8] = __builtin_amdgcn_mfma_f32_16x16x32_bf16(f[1], ones, kv[8], 0, 0, 0);
        kv[9] = __builtin_amdgcn_mfma_f32_16x16x32_bf16(f[2], ones, kv[9], 0, 0, 0);
      }
      __builtin_amdgcn_s_barrier();
      f32x4* red = (f32x4*)smem + (wm * 2 + wn) * 640 + lane;
#pragma unroll
      for (int i = 0; i < 10; ++i) red[i * 64] = kv[i];
      __builtin_amdgcn_s_waitcnt(0xC07F);
      __builtin_amdgcn_s_barrier();
    }
  }
  if (KV) {
    if (tid == 0) wait_init_t0(ws, initdone);
    __syncthreads();
    const float* raw = (const float*)smem;
    float* acc = (float*)(ws + WS_ZERO) + (size_t)ct * 4096 + tid;
#pragma unroll
    for (int it = 0; it < 8; ++it) {
      const int e0 = it * 512 + tid;
      const int hl4 = it >> 1, tile = it & 1, lp = (e0 >> 3) & 63, j = e0 & 7;
      const int row = lp & 15, gk = lp >> 4, gg = row >> 2, r = row & 3;
      const int wn = hl4 >> 1, hl = hl4 & 1;
      const int dd = 8 * gk + j;
      if ((tile == 0 || r < 2) && dd < 20) {
        const int d = dd + 20 * hl, dt = d >> 4;
        const int e = (tile == 0 ? 5 * gg + r : 5 * gg + 3 + r) + 20 * hl + 40, et = e >> 4;
        int t, ln;
        if (tile == 0 && r == 3) { t = 7 + dt; ln = ((d & 15) >> 2) * 16; }
        else { t = dt == 0 ? et - 2 : (dt == 1 ? et : et + 2); ln = ((d & 15) >> 2) * 16 + (e & 15); }
        const int idx = (wn * 640 + t * 64 + ln) * 4 + (d & 3);
        const float v = raw[idx] + raw[2 * 640 * 4 + idx];
        __hip_atomic_fetch_add((GAS float*)(acc + it * 512), v, RLX_AGENT);
      }
    }
    TLS(1);
    publish8(ws, CNT_KV, initdone);
  } else {
    TLS(1);
    publish(ws, CNT_PQ + rt, initdone);
  }
}

DI void aux_block(const u16* __restrict__ u, const u16* __restrict__ wo, char* __restrict__ ws, rsrc_t rs, int ab, char* smem, bool& initdone) {
  const int tid = threadIdx.x, lane = tid & 63, wv = __builtin_amdgcn_readfirstlane(tid >> 6);
  char* reg = smem + wv * KG_REG;
  if (wv < 5) {
    const int tile = ab * 5 + wv; const int rb = tile / 10, cb = tile % 10;
    u16* t = (u16*)reg;
#pragma unroll
    for (int i = 0; i < 8; ++i) {
      const int row = i * 8 + (lane >> 3), ch = lane & 7;
      u32x4 v = *(const u32x4*)(u + (size_t)(rb * 64 + row) * D + cb * 64 + ch * 8);
      u32* p = (u32*)(t + row * 66 + ch * 8);
      p[0] = v.x; p[1] = v.y; p[2] = v.z; p[3] = v.w;
    }
    __builtin_amdgcn_wave_barrier();
#pragma unroll
    for (int i = 0; i < 8; ++i) {
      const int c = i * 8 + (lane >> 3), r8 = lane & 7;
      u32 o[4];
#pragma unroll
      for (int k = 0; k < 4; ++k) o[k] = (u32)t[(r8 * 8 + 2 * k) * 66 + c] | ((u32)t[(r8 * 8 + 2 * k + 1) * 66 + c] << 16);
      st16_wt(rs, WS_UT + ((cb * 64 + c) * S + rb * 64 + r8 * 8) * 2, (u32x4){o[0], o[1], o[2], o[3]});
    }
    __builtin_amdgcn_wave_barrier();
  } else {
#pragma unroll
    for (int it = 0; it < 5; ++it) {
      const int uu = (wv - 5) * 64 + lane + 192 * it;
      if (uu < 800) {
        const int U = ab * 800 + uu; const int img = U / 1280, un = U % 1280; const int ct = img / 10, kt = img % 10;
        const int sb = un >> 4, kb = sb / 10, nb = sb % 10, j = ((un >> 1) & 7) ^ (4 * (kb & 1));
        const int T = 2 * kt + (kb >> 2), G = T / 5, tt = T % 5, ii = 8 * tt + j, hh = ii / 5, r = ii % 5;
        const int k = 160 * G + 20 * hh + 5 * (kb & 3) + r;
        st16_wt(rs, WS_WOP + U * 16, *(const u32x4*)(wo + (size_t)k * D + 160 * ct + nb * 16 + 8 * (un & 1)));
      }
    }
  }
  TLS(1);
  publish8(ws, CNT_T, initdone);
}

constexpr int UC_STRIDE = 4160, UCH = 4 * UC_STRIDE, KP_STRIDE = 1280, KPCH = 4 * KP_STRIDE;
constexpr int UC0 = 4 * KG_REG, PART0 = UC0 + 4 * UCH, KC0 = PART0 + 12 * 2592, RED0 = KC0 + 4 * 1312;
constexpr int B_LDS = RED0 + 128;
constexpr int PSPLIT = 22;
DI void b_block(const KGP& kp, char* __restrict__ ws, rsrc_t rs, const u16* __restrict__ dsk_, u16* __restrict__ out, int cb, char* smem, bool& initdone) {
  const int tid = threadIdx.x, lane = tid & 63, wv = __builtin_amdgcn_readfirstlane(tid >> 6), ch = wv >> 1, hf = wv & 1;
  const int d0 = cb * 4;
  float* red = (float*)(smem + RED0);
  float* part = (float*)(smem + PART0);
  KGL l0, l1;
  kgen_load(kp, d0 + wv / 3, wv % 3, l0);
  kgen_load(kp, d0 + (8 + (wv & 3)) / 3, (8 + (wv & 3)) % 3, l1);
  if (tid == 0) { wait_init_t0(ws, initdone); wait_cnt_t0(ws, CNT_T + (blockIdx.x & 7), 64); }
  {
    float ks = kgen_task(l0, wv % 3, smem + wv * KG_REG, part + wv * 648);
    if (lane == 0 && wv % 3 == 0) red[8 + wv / 3] = ks;
  }
  __syncthreads();
  TLS(3);
  if (wv < 4) {
    float ks = kgen_task(l1, (8 + wv) % 3, smem + wv * KG_REG, part + (8 + wv) * 648);
    if (lane == 0 && wv == 1) red[8 + 3] = ks;
  } else {
    const int c = wv - 4;
    char* ur = smem + UC0 + c * UCH;
    u32x4 uv[4];
#pragma unroll
    for (int i = 0; i < 4; ++i) uv[i] = ld16_sc(rs, WS_UT + ((d0 + c) * S + 8 * (lane + 64 * i)) * 2);
    float m = 0.f;
#pragma unroll
    for (int i = 0; i < 4; ++i)
#pragma unroll
      for (int k = 0; k < 4; ++k) m = fmaxf(m, fmaxf(fabsf(lo16(uv[i][k])), fabsf(hi16(uv[i][k]))));
#pragma unroll
    for (int o = 32; o >= 1; o >>= 1) m = fmaxf(m, __shfl_xor(m, o));
    int e = (int)((__float_as_uint(m) >> 23) & 0xff);
    e = e < 30 ? 30 : (e > 220 ? 220 : e);
    const float us = __uint_as_float((u32)(127 + 140 - e) << 23);
    if (lane == 0) red[c] = __uint_as_float((u32)(e - 13) << 23);
#pragma unroll
    for (int i = 0; i < 4; ++i) {
      const int q = lane + 64 * i;
      u32x4 o = {pk_f16(hi16(uv[i].w) * us, lo16(uv[i].w) * us), pk_f16(hi16(uv[i].z) * us, lo16(uv[i].z) * us),
                 pk_f16(hi16(uv[i].y) * us, lo16(uv[i].y) * us), pk_f16(hi16(uv[i].x) * us, lo16(uv[i].x) * us)};
      *(u32x4*)(ur + 2 * (2040 - 8 * q)) = o;
    }
    if (lane < 4) *(u32x4*)(ur + 4096 + 16 * lane) = (u32x4){0, 0, 0, 0};
    __builtin_amdgcn_wave_barrier();
#pragma unroll
    for (int k = 0; k < 5; ++k) {
      const int q = lane + 64 * k;
      if (q < 260) {
        u32x4 a = *(const u32x4*)(ur + 16 * q);
        u32x2 b = *(const u32x2*)(ur + 16 * q + 16);
        u32x4 c1 = {__builtin_amdgcn_alignbit(a.y, a.x, 16), __builtin_amdgcn_alignbit(a.z, a.y, 16), __builtin_amdgcn_alignbit(a.w, a.z, 16), __builtin_amdgcn_alignbit(b.x, a.w, 16)};
        u32x4 c2 = {a.y, a.z, a.w, b.x};
        u32x4 c3 = {c1.y, c1.z, c1.w, __builtin_amdgcn_alignbit(b.y, b.x, 16)};
        *(u32x4*)(ur + 1 * UC_STRIDE + 16 * q) = c1;
        *(u32x4*)(ur + 2 * UC_STRIDE + 16 * q) = c2;
        *(u32x4*)(ur + 3 * UC_STRIDE + 16 * q) = c3;
      }
    }
  }
  __syncthreads();
  TLS(11);
  {
    u16* kc = (u16*)(smem + KC0 + ch * 1312);
    const float* p0 = part + ch * 3 * 648;
#pragma unroll
    for (int i = 0; i < 2; ++i) {
      const int ci = hf * 81 + 64 * i + lane;
      if (lane + 64 * i < 81) {
        f32x4 v = *(const f32x4*)(p0 + ci * 4) + *(const f32x4*)(p0 + 648 + ci * 4) + *(const f32x4*)(p0 + 1296 + ci * 4);
        *(u32x2*)(kc + ci * 4) = (u32x2){pk_f16(v[0] * 128.f, v[1] * 128.f), pk_f16(v[2] * 128.f, v[3] * 128.f)};
      }
    }
  }
  __syncthreads();
  {
    const u16* kc = (const u16*)(smem + KC0 + ch * 1312);
    char* kpl = smem + ch * KPCH;
    if (hf == 1 && lane < 60) *(u32x4*)(kpl + (lane & 3) * KP_STRIDE + (lane >> 2) * 16) = (u32x4){0, 0, 0, 0};
#pragma unroll
    for (int k = 0; k < 2; ++k) {
      const int q = hf + 2 * (lane + 64 * k);
      u32 v[8];
#pragma unroll
      for (int j8 = 0; j8 < 8; ++j8) {
        int j = 8 * q + j8;
        u32 fb = __float_as_uint((float)j); fb += 0x7fffu + ((fb >> 16) & 1u);
        int idx = j < 256 ? j : 256 + (int)((fb >> 16) - 0x4380u);
        v[j8] = kc[idx];
      }
      u32x4 o = {v[0] | (v[1] << 16), v[2] | (v[3] << 16), v[4] | (v[5] << 16), v[6] | (v[7] << 16)};
      *(u32x4*)(kpl + (q & 3) * KP_STRIDE + ((q >> 2) + 15) * 16) = o;
    }
  }
  __syncthreads();
  TLS(12);
  f32x4 acc[4][2];
#pragma unroll
  for (int t = 0; t < 4; ++t) { acc[t][0] = (f32x4){0, 0, 0, 0}; acc[t][1] = (f32x4){0, 0, 0, 0}; }
  const int b = lane & 15, g = lane >> 4;
  char* ureg = smem + UC0 + ch * UCH;
  {
    const int cpy = (3 - b) & 3;
    const char* pa0 = ureg + cpy * UC_STRIDE + 2 * (2044 - 4 * (b >> 2) + 8 * g);
    const char* pb0 = smem + ch * KPCH + g * KP_STRIDE + 16 * (b + 15);
    typedef const volatile LAS u32x2* lp2;
    typedef const volatile LAS u32x4* lp4;
    const u32 abase = (u32)(size_t)(pa0 - 64 * 63 - 32);
    const u32 bbase = (u32)(size_t)(pb0 - 16 * 63);
    constexpr int DP = 2;
    u32x2 Ab[DP + 1][4]; u32x4 Bb[DP + 1][4];
    auto run = [&](auto P0c, auto P1c) {
      constexpr int P0 = decltype(P0c)::value, P1 = decltype(P1c)::value;
      auto issue = [&](int p) {
        const int sl = p % (DP + 1);
        const u32 oa = 64 * (63 - p);
        Ab[sl][0] = *(lp2)(abase + oa + 32); Ab[sl][1] = *(lp2)(abase + oa + 40);
        Ab[sl][2] = *(lp2)(abase + oa);      Ab[sl][3] = *(lp2)(abase + oa + 8);
#pragma unroll
        for (int t = 0; t < 4; ++t) if (t >= (p >> 4)) Bb[sl][t] = *(lp4)(bbase + 16 * (63 - p) + 256 * t);
      };
#pragma unroll
      for (int p = P0; p < P0 + DP; ++p) issue(p);
#pragma unroll
      for (int p = P0; p < P1; ++p) {
        if (p + DP < P1) issue(p + DP);
        __builtin_amdgcn_sched_barrier(0);
        const int sl = p % (DP + 1);
        h8 A0 = __builtin_bit_cast(h8, (u32x4){Ab[sl][0].x, Ab[sl][0].y, Ab[sl][1].x, Ab[sl][1].y});
        h8 A1 = __builtin_bit_cast(h8, (u32x4){Ab[sl][2].x, Ab[sl][2].y, Ab[sl][3].x, Ab[sl][3].y});
#pragma unroll
        for (int t = 0; t < 4; ++t) if (t >= (p >> 4)) {
          h8 B = __builtin_bit_cast(h8, Bb[sl][t]);
          acc[t][0] = __builtin_amdgcn_mfma_f32_16x16x32_f16(A0, B, acc[t][0], 0, 0, 0);
          acc[t][1] = __builtin_amdgcn_mfma_f32_16x16x32_f16(A1, B, acc[t][1], 0, 0, 0);
        }
        __builtin_amdgcn_sched_barrier(0);
      }
    };
    if (hf == 0) run(std::integral_constant<int, 0>{}, std::integral_constant<int, PSPLIT>{});
    else run(std::integral_constant<int, PSPLIT>{}, std::integral_constant<int, 64>{});
  }
  TLS(13);
  f32x4* xch = (f32x4*)(smem + PART0 + ch * 6144) + lane;
  if (hf == 1) {
#pragma unroll
    for (int t = 1; t < 4; ++t) { xch[(2 * (t - 1)) * 64] = acc[t][0]; xch[(2 * (t - 1) + 1) * 64] = acc[t][1]; }
  }
  __syncthreads();
  if (hf == 0) {
    const float ysc = red[8 + ch] * red[ch];
    char* yb = smem + ch * 4096;
#pragma unroll
    for (int t = 0; t < 4; ++t)
#pragma unroll
      for (int r = 0; r < 2; ++r) {
        f32x4 a = acc[t][r];
        if (t > 0) a += xch[(2 * (t - 1) + r) * 64];
        u32x2 o = {pk_bf16(a[0] * ysc, a[1] * ysc), pk_bf16(a[2] * ysc, a[3] * ysc)};
        *(u32x2*)(yb + 2 * (32 * (16 * t + b) + 16 * r + 4 * g)) = o;
      }
  }
  __syncthreads();
  {
    u32x2 yv[4], uu[4]; float iu[4], dk[4];
#pragma unroll
    for (int c = 0; c < 4; ++c) {
      yv[c] = *(const u32x2*)(smem + c * 4096 + 8 * tid);
      uu[c] = *(const u32x2*)(smem + UC0 + c * UCH + 2 * (2044 - 4 * tid));
      iu[c] = red[c]; dk[c] = bf2f(dsk_[d0 + c]);
    }
#pragma unroll
    for (int i = 0; i < 4; ++i) {
      float r[4];
#pragma unroll
      for (int c = 0; c < 4; ++c) {
        u32 yw = yv[c][i >> 1]; float y = (i & 1) ? hi16(yw) : lo16(yw);
        const u32 uw = (i < 2) ? uu[c].y : uu[c].x; const u16 ub = (i & 1) ? (u16)uw : (u16)(uw >> 16); float uf = (float)__builtin_bit_cast(_Float16, ub) * iu[c];
        r[c] = y + rbf(uf * dk[c]);
      }
      u32x2 o = {pk_bf16(r[0], r[1]), pk_bf16(r[2], r[3])};
      *(u32x2*)(out + (size_t)(4 * tid + i) * 1280 + 640 + d0) = o;
    }
  }
  __syncthreads();
}

constexpr int OSTAGE = 20480, ONBUF = 4, O_OFF_W = 81920;
DI void o_block(char* __restrict__ ws, rsrc_t rs, u16* __restrict__ out, int rt, int ct, char* smem, bool& initdone) {
  const int tid = threadIdx.x, lane = tid & 63, wv = __builtin_amdgcn_readfirstlane(tid >> 6);
  const int row0 = rt * 64, fr = lane & 15, g = lane >> 4;
  const int lw = wv - 4;
  if (tid == 0) { wait_init_t0(ws, initdone); wait_cnt_t0(ws, CNT_T + (blockIdx.x & 7), 64); }
  acquire_all();
  const char* wsrc = ws + WS_WOP + (size_t)ct * 10 * OSTAGE + lw * 5120 + lane * 16;
  auto stage = [&](int kt) {
    char* b = smem + O_OFF_W + (kt & (ONBUF - 1)) * OSTAGE + lw * 5120;
#pragma unroll
    for (int i = 0; i < 5; ++i) __builtin_amdgcn_global_load_lds((const u32*)(wsrc + kt * OSTAGE + i * 1024), (LAS u32*)(b + i * 1024), 16, 0, 0);
  };
  if (wv >= 4) {
#pragma unroll 1
    for (int i = 0; i < ONBUF; ++i) stage(i);
  }
  if (tid == 0) wait_cnt_t0(ws, CNT_PQ + (rt >> 1), 4);
  asm volatile("s_waitcnt lgkmcnt(0)" ::: "memory");
  __builtin_amdgcn_s_barrier();
  const char* pq = ws + WS_PQ;
  const char* zp = ws + WS_ZERO + CNT_OFF + ZPAD * 128;
  u32x4 kr[4][2][2]; u32x2 pa[4][4], pb[4][4];
#pragma unroll
  for (int hh = 0; hh < 4; ++hh)
#pragma unroll
    for (int st = 0; st < 4; ++st) {
      const char* p = pq + 2 * (((size_t)((rt * 4 + st) * 32 + 4 * wv + hh) * 5) * 64 + fr * 4);
      pa[hh][st] = ld8_sc(g < 3 ? p + 2 * g * 128 : zp);
      pb[hh][st] = ld8_sc(g < 2 ? p + (2 * g + 1) * 128 : zp);
    }
  if (tid == 0) wait_cnt_t0(ws, CNT_KV + (blockIdx.x & 7), 128);
  TLS(6);
  asm volatile("s_waitcnt lgkmcnt(0)" ::: "memory");
  __builtin_amdgcn_s_barrier();
  TLS(7);
#pragma unroll
  for (int hh = 0; hh < 4; ++hh)
#pragma unroll
    for (int tl = 0; tl < 2; ++tl) {
      kr[hh][tl][0] = ld16_sc(rs, WS_ZERO + (((4 * wv + hh) * 2 + tl) * 64 + lane) * 32);
      kr[hh][tl][1] = ld16_sc(rs, WS_ZERO + (((4 * wv + hh) * 2 + tl) * 64 + lane) * 32 + 16);
    }
  __builtin_amdgcn_sched_barrier(0);
  {
    s16x8 kf[4][2];
#pragma unroll
    for (int hh = 0; hh < 4; ++hh)
#pragma unroll
      for (int tl = 0; tl < 2; ++tl) {
        f32x4 a = __builtin_bit_cast(f32x4, kr[hh][tl][0]), b = __builtin_bit_cast(f32x4, kr[hh][tl][1]);
        kf[hh][tl] = __builtin_bit_cast(s16x8, (u32x4){pk_bf16(a[0], a[1]), pk_bf16(a[2], a[3]), pk_bf16(b[0], b[1]), pk_bf16(b[2], b[3])});
      }
    asm volatile("" :: "v"(kf[3][1]), "v"(pa[3][3]), "v"(pb[3][3]));
    TLS(10);
    const int G = wv >> 1, half = wv & 1;
#pragma unroll
    for (int st = 0; st < 4; ++st) {
      float vals[20];
#pragma unroll
      for (int hh = 0; hh < 4; ++hh) {
        s16x8 pf = __builtin_bit_cast(s16x8, (u32x4){pa[hh][st].x, pa[hh][st].y, pb[hh][st].x, pb[hh][st].y});
        f32x4 z4 = {0, 0, 0, 0};
        f32x4 t0 = __builtin_amdgcn_mfma_f32_16x16x32_bf16(kf[hh][0], pf, z4, 0, 0, 0);
        f32x4 t1 = __builtin_amdgcn_mfma_f32_16x16x32_bf16(kf[hh][1], pf, z4, 0, 0, 0);
        float den = rbf(rbf(t0[3]) + 1e-6f);
        float rd = __builtin_amdgcn_rcpf(den);
        vals[5 * hh + 0] = t0[0] * rd; vals[5 * hh + 1] = t0[1] * rd; vals[5 * hh + 2] = t0[2] * rd;
        vals[5 * hh + 3] = t1[0] * rd; vals[5 * hh + 4] = t1[1] * rd;
      }
#pragma unroll
      for (int i = 0; i < 5; ++i) {
        const int byte = 40 * half + 8 * i;
        const int T = 5 * G + (byte >> 4);
        *(u32x2*)(smem + (st * 20 + T) * 1024 + lane * 16 + (byte & 8)) = (u32x2){pk_bf16(vals[4 * i], vals[4 * i + 1]), pk_bf16(vals[4 * i + 2], vals[4 * i + 3])};
      }
    }
  }
  if (wv >= 4) {
    asm volatile("s_waitcnt vmcnt(0) lgkmcnt(0)" ::: "memory");
    __builtin_amdgcn_s_barrier();
#pragma unroll 1
    for (int kt = 0; kt < 6; ++kt) {
      asm volatile("s_waitcnt vmcnt(10)" ::: "memory");
      __builtin_amdgcn_s_barrier();
      stage(kt + ONBUF);
    }
    asm volatile("s_waitcnt vmcnt(10)" ::: "memory"); __builtin_amdgcn_s_barrier();
    asm volatile("s_waitcnt vmcnt(5)" ::: "memory");  __builtin_amdgcn_s_barrier();
    asm volatile("s_waitcnt vmcnt(0)" ::: "memory");  __builtin_amdgcn_s_barrier();
    return;
  }
  const int wm = wv >> 1, wn = wv & 1;
  f32x4 acc[2][5];
#pragma unroll
  for (int m = 0; m < 2; ++m)
#pragma unroll
    for (int n = 0; n < 5; ++n) acc[m][n] = (f32x4){0, 0, 0, 0};
  const u32 sbase = (u32)(size_t)smem;
  const u32 boff0 = O_OFF_W + g * 2560 + wn * 1280 + ((0 ^ (4 * (g & 1))) + (fr >> 2)) * 32 + (lane & 3) * 8;
  const u32 boff1 = O_OFF_W + g * 2560 + wn * 1280 + ((4 ^ (4 * (g & 1))) + (fr >> 2)) * 32 + (lane & 3) * 8;
  u32 aad = sbase + wm * 2 * 20 * 1024 + lane * 16;
  u32 b0 = sbase + boff0, b1 = sbase + boff1;
  s16x8 af[2][2]; s16x4 bl[2][5], bh[2][5];
  auto ldop = [&](int ks, int i) {
    if (i < 10) {
      const int n = i >> 1;
      if (i & 1) bh[ks][n] = __builtin_amdgcn_ds_read_tr16_b64_v4i16((LAS s16x4*)(b1 + ks * 10240 + n * 256));
      else       bl[ks][n] = __builtin_amdgcn_ds_read_tr16_b64_v4i16((LAS s16x4*)(b0 + ks * 10240 + n * 256));
    } else af[ks][i - 10] = *(const LAS s16x8*)(aad + ((i - 10) * 20 + ks) * 1024);
  };
  auto mma1 = [&](int sl, int i) {
    const int m = i / 5, n = i % 5;
    s16x8 bb = __builtin_shufflevector(bl[sl][n], bh[sl][n], 0, 1, 2, 3, 4, 5, 6, 7);
    acc[m][n] = __builtin_amdgcn_mfma_f32_16x16x32_bf16(bb, af[sl][m], acc[m][n], 0, 0, 0);
  };
  __builtin_amdgcn_s_waitcnt(0xC07F);
  __builtin_amdgcn_s_barrier();
  TLS(9);
#pragma unroll
  for (int i = 0; i < 12; ++i) ldop(0, i);
  int cur = 0;
#pragma unroll 1
  for (int kt = 0; kt < 10; ++kt) {
    __builtin_amdgcn_sched_barrier(0);
#pragma unroll
    for (int i = 0; i < 10; ++i) {
      mma1(0, i);
      ldop(1, i); if (i < 2) ldop(1, 10 + i);
      __builtin_amdgcn_sched_barrier(0);
    }
    cur = (cur + 1) & (ONBUF - 1);
    b0 = sbase + cur * OSTAGE + boff0; b1 = sbase + cur * OSTAGE + boff1;
    aad += (kt < 9) ? 2048 : 0;
    __builtin_amdgcn_s_waitcnt(0xC07F);
    if (kt < 9) __builtin_amdgcn_s_barrier();
    __builtin_amdgcn_sched_barrier(0);
#pragma unroll
    for (int i = 0; i < 10; ++i) {
      mma1(1, i);
      ldop(0, i); if (i < 2) ldop(0, 10 + i);
      __builtin_amdgcn_sched_barrier(0);
    }
  }
#pragma unroll
  for (int m = 0; m < 2; ++m)
#pragma unroll
    for (int n = 0; n < 5; ++n) {
      u32x2 o = {pk_bf16(acc[m][n][0], acc[m][n][1]), pk_bf16(acc[m][n][2], acc[m][n][3])};
      *(u32x2*)(out + (size_t)(row0 + wm * 32 + m * 16 + fr) * 1280 + 160 * ct + wn * 80 + n * 16 + 4 * g) = o;
    }
}

struct P { const u16 *x, *w, *wo, *u, *lar, *aim, *cr, *ci, *ldt, *dsk; u16* out; char* ws; };
__global__ __launch_bounds__(512, 2) void kmain(P p) {
  extern __shared__ __attribute__((aligned(16))) char smem[];
  const int b = blockIdx.x, tid = threadIdx.x;
  char* ws = p.ws;
  const rsrc_t rs = __builtin_amdgcn_make_buffer_rsrc((void*)ws, 0, WS_SPAN, 0x00020000);
  bool initdone = false;
  TLS(0);
  if (b >= 248) {
    const int part = b - 248;
    for (int i = tid; i < ZERO_BYTES / 128; i += 512) st16_wt(rs, WS_ZERO + part * (ZERO_BYTES / 8) + i * 16, (u32x4){0, 0, 0, 0});
    asm volatile("s_waitcnt vmcnt(0)" ::: "memory");
    __syncthreads();
    if (tid < 256) __hip_atomic_store((gu64*)(ws + WS_FLAG) + tid * 8 + part, MAGIC, RLX_AGENT);
  }
  const KGP kp = {p.lar, p.aim, p.cr, p.ci, p.ldt};
  int bt0 = -1, bt1 = -1, o_task = -1;
  if (b < 192) {
    const int xcd = b & 7, j = b >> 3;
    const int rt = (xcd >> 1) * 4 + (j & 3), c6 = j >> 2;
    if (c6 < 4) { q_block<true>(p.x, p.w, ws, rs, rt, (xcd & 1) * 4 + c6, smem, initdone); o_task = xcd * 16 + j; }
    else { q_block<false>(p.x, p.w, ws, rs, rt, (xcd & 1) * 2 + (c6 - 4), smem, initdone); bt0 = xcd * 20 + 12 + (j - 16); }
  } else {
    const int ab = b - 192;
    aux_block(p.u, p.wo, ws, rs, ab, smem, initdone);
    { const int xcd = ab & 7, i = ab >> 3; bt0 = xcd * 20 + i; if (i < 4) bt1 = xcd * 20 + 8 + i; }
  }
  TLS(2);
  __syncthreads();
#pragma unroll 1
  for (int it = 0; it < 2; ++it) {
    const int t = it == 0 ? bt0 : bt1;
    if (t >= 0) b_block(kp, ws, rs, p.dsk, p.out, t, smem, initdone);
  }
  TLS(5);
  if (o_task >= 0) o_block(ws, rs, p.out, o_task >> 2, o_task & 3, smem, initdone);
  TLS(8);
}

extern "C" void kernel_launch(void* const* d_in, const int* in_sizes, int n_in, void* d_out, int out_size, void* d_ws, size_t ws_size, hipStream_t stream) {
  constexpr int LDS = O_OFF_W + ONBUF * OSTAGE;
  static_assert(QNBUF * QSTAGE <= LDS && 8 * KG_REG <= LDS && B_LDS <= LDS, "LDS");
  (void)hipFuncSetAttribute((const void*)kmain, hipFuncAttributeMaxDynamicSharedMemorySize, LDS);
  P p = {(const u16*)d_in[0], (const u16*)d_in[1], (const u16*)d_in[2], (const u16*)d_in[3], (const u16*)d_in[4], (const u16*)d_in[5], (const u16*)d_in[6], (const u16*)d_in[7], (const u16*)d_in[8], (const u16*)d_in[9], (u16*)d_out, (char*)d_ws};
  kmain<<<256, 512, LDS, stream>>>(p);
}
```

```cpp
#include <hip/hip_runtime.h>
#include <hip/hip_bf16.h>
#include <stdint.h>
#include <type_traits>
typedef unsigned short u16;
typedef unsigned int u32;
typedef float f32x2 __attribute__((ext_vector_type(2)));
typedef float f32x4 __attribute__((ext_vector_type(4)));
typedef float f32x16 __attribute__((ext_vector_type(16)));
typedef short s16x4 __attribute__((ext_vector_type(4)));
typedef short s16x8 __attribute__((ext_vector_type(8)));
typedef _Float16 h8 __attribute__((ext_vector_type(8)));
typedef _Float16 h2 __attribute__((ext_vector_type(2)));
typedef __bf16 bf16x2 __attribute__((ext_vector_type(2)));
typedef u32 u32x2 __attribute__((ext_vector_type(2)));
typedef u32 u32x4 __attribute__((ext_vector_type(4)));
#define DI __device__ __forceinline__
#define LAS __attribute__((address_space(3)))
#define GAS __attribute__((address_space(1)))
typedef GAS unsigned gu32; typedef GAS unsigned long long gu64;
#define RLX_AGENT __ATOMIC_RELAXED, __HIP_MEMORY_SCOPE_AGENT
DI float bf2f(u16 x) { return __uint_as_float(((u32)x) << 16); }
DI u32 pk_bf16(float a, float b) { bf16x2 r = __builtin_convertvector((f32x2){a, b}, bf16x2); return __builtin_bit_cast(u32, r); }
DI u32 pk_f16(float a, float b) { h2 r = __builtin_convertvector((f32x2){a, b}, h2); return __builtin_bit_cast(u32, r); }
DI float lo16(u32 x) { return __uint_as_float(x << 16); }
DI float hi16(u32 x) { return __uint_as_float(x & 0xffff0000u); }
DI float rbf(float f) { return lo16(pk_bf16(f, 0.f)); }
DI u32 phi_pair(float a, float b) {
  u32 t = pk_bf16(a, b);
  float ta = lo16(t), tb = hi16(t);
  float ea = __builtin_amdgcn_fmed3f(__builtin_amdgcn_exp2f(ta * 1.4426950408889634f), 0.f, 1.f);
  float eb = __builtin_amdgcn_fmed3f(__builtin_amdgcn_exp2f(tb * 1.4426950408889634f), 0.f, 1.f);
  return pk_bf16(ea + fmaxf(ta, 1e-6f), eb + fmaxf(tb, 1e-6f));
}
DI f32x2 cmul(f32x2 x, f32x2 y) {
  f32x2 t, z;
  asm("v_pk_mul_f32 %0, %1, %2 op_sel_hi:[0,1]" : "=v"(t) : "v"(x), "v"(y));
  asm("v_pk_fma_f32 %0, %1, %2, %3 op_sel:[1,1,0] op_sel_hi:[1,0,1] neg_lo:[0,1,0]" : "=v"(z) : "v"(x), "v"(y), "v"(t));
  return z;
}
DI f32x2 cexpP(float ar2, float airev, float P) {
  float e = __builtin_amdgcn_exp2f(ar2 * P);
  float t = __builtin_amdgcn_fractf(airev * P);
  float s = __builtin_amdgcn_sinf(t), c = __builtin_amdgcn_cosf(t);
  f32x2 r = {e * c, e * s};
  return r;
}
constexpr int S = 2048, D = 640, NS = 192;
constexpr u32 WS_PQ = 0;
constexpr u32 WS_UT = 2621440;
constexpr u32 WS_WOP = 2 * 2621440;
constexpr u32 WS_FLAG = 2 * 2621440 + 819200;
constexpr u32 WS_ZERO = WS_FLAG + 16384;
constexpr int KVACC_BYTES = 32 * 1024 * 4;
constexpr int CNT_OFF = KVACC_BYTES;
constexpr int CNT_KV = 0, CNT_T = 8, CNT_PQ = 16, CNT_KG = 32, ZPAD = 40;
constexpr int ZERO_BYTES = KVACC_BYTES + 48 * 128;
constexpr u32 WS_SPAN = 7u << 20;
constexpr unsigned long long MAGIC = 0x9e3779b97f4a7c15ull;
constexpr unsigned SPIN_CAP = 1u << 17;
typedef __amdgpu_buffer_rsrc_t rsrc_t;

DI gu32* cnt_ptr(char* ws, int i) { return (gu32*)(ws + WS_ZERO + CNT_OFF + i * 128); }
DI void wait_init_t0(char* ws, bool& done) {
  if (done) return;
  gu64* f = (gu64*)(ws + WS_FLAG) + blockIdx.x * 8;
  for (unsigned sp = 0; sp < SPIN_CAP; ++sp) {
    bool ok = true;
#pragma unroll
    for (int i = 0; i < 8; ++i) ok &= __hip_atomic_load(f + i, RLX_AGENT) == MAGIC;
    if (ok) break;
    __builtin_amdgcn_s_sleep(2);
  }
#pragma unroll
  for (int i = 0; i < 8; ++i) __hip_atomic_store(f + i, 0ull, RLX_AGENT);
  done = true;
}
DI void wait_cnt_t0(char* ws, int i, unsigned want) {
  gu32* c = cnt_ptr(ws, i);
  if (__hip_atomic_load(c, RLX_AGENT) >= want) return;
  unsigned v0, v1, v2, v3;
#define PLD(v) asm volatile("global_load_dword %0, %1, off sc1" : "=v"(v) : "v"(c) : "memory")
  PLD(v0); __builtin_amdgcn_s_sleep(2); PLD(v1); __builtin_amdgcn_s_sleep(2); PLD(v2); __builtin_amdgcn_s_sleep(2);
  for (unsigned sp = 0; sp < SPIN_CAP; ++sp) {
    PLD(v3); asm volatile("s_waitcnt vmcnt(3)" : "+v"(v0) :: "memory"); if (v0 >= want) break; __builtin_amdgcn_s_sleep(2);
    PLD(v0); asm volatile("s_waitcnt vmcnt(3)" : "+v"(v1) :: "memory"); if (v1 >= want) break; __builtin_amdgcn_s_sleep(2);
    PLD(v1); asm volatile("s_waitcnt vmcnt(3)" : "+v"(v2) :: "memory"); if (v2 >= want) break; __builtin_amdgcn_s_sleep(2);
    PLD(v2); asm volatile("s_waitcnt vmcnt(3)" : "+v"(v3) :: "memory"); if (v3 >= want) break; __builtin_amdgcn_s_sleep(2);
  }
  asm volatile("s_waitcnt vmcnt(0)" : "+v"(v0), "+v"(v1), "+v"(v2), "+v"(v3) :: "memory");
#undef PLD
}
DI void publish(char* ws, int i, bool& initdone) {
  asm volatile("s_waitcnt vmcnt(0)" ::: "memory");
  __syncthreads();
  if (threadIdx.x == 0) { wait_init_t0(ws, initdone); __hip_atomic_fetch_add(cnt_ptr(ws, i), 1u, RLX_AGENT); }
}
DI void publish8(char* ws, int i, bool& initdone) {
  if (threadIdx.x == 0) wait_init_t0(ws, initdone);
  asm volatile("s_waitcnt vmcnt(0)" ::: "memory");
  __syncthreads();
  if (threadIdx.x < 8) __hip_atomic_fetch_add(cnt_ptr(ws, i + threadIdx.x), 1u, RLX_AGENT);
}
DI u32x4 ld16_sc(rsrc_t r, u32 off) { return __builtin_amdgcn_raw_buffer_load_b128(r, off, 0, 16); }
DI u32x2 ld8_sc(const void* p) { unsigned long long v = __hip_atomic_load((gu64*)p, RLX_AGENT); return (u32x2){(u32)v, (u32)(v >> 32)}; }
DI float ld4_sc(const void* p) { return __uint_as_float(__hip_atomic_load((gu32*)p, RLX_AGENT)); }
DI void acquire_all() {
  if (threadIdx.x == 0) { __builtin_amdgcn_fence(__ATOMIC_ACQUIRE, "agent"); asm volatile("s_waitcnt vmcnt(0)" ::: "memory"); }
  __syncthreads();
}
DI void st16_wt(rsrc_t r, u32 off, u32x4 v) { __builtin_amdgcn_raw_buffer_store_b128(v, r, off, 0, 16); }
DI void st8_wt(void* p, u32x2 v) { __hip_atomic_store((gu64*)p, ((unsigned long long)v.y << 32) | v.x, RLX_AGENT); }

#define TLS(i)
constexpr int QSTAGE = 36864, QNBUF = 4;
constexpr int TROW = 272, KG_REG = 14720;

DI f32x2 cm(f32x2 x, f32x2 y) {
  f32x2 xx = {x.x, x.x}, xy = {x.y, x.y}, ys = {-y.y, y.x};
  return __builtin_elementwise_fma(xy, ys, xx * y);
}
DI f32x2 cmc(f32x2 x, f32x2 y) {
  f32x2 xx = {x.x, x.x}, xy = {x.y, x.y}, yc = {y.x, -y.y}, ys = {y.y, y.x};
  return __builtin_elementwise_fma(xy, ys, xx * yc);
}
DI void kgen_chunk(float a2, float av, f32x2 Ein, char* reg, f32x16& kacc) {
  const int lane = threadIdx.x & 63;
  u32* tE = (u32*)(reg) + lane;
  u32* tF = (u32*)(reg + 21 * TROW) + lane;
  constexpr int R = TROW / 4;
  auto stE = [&](int r, f32x2 v) { tE[r * R] = pk_f16(v.x, v.y); };
  auto stF = [&](int r, f32x2 v) { tF[r * R] = pk_f16(v.x, v.y); };
  f32x2 w1 = cexpP(a2, av, 1.f), v1 = cexpP(a2, av, 8.f), B256 = cexpP(a2, av, 256.f);
  f32x2 w2 = cm(w1, w1), w4 = cm(w2, w2);
  f32x2 v2 = cm(v1, v1), v4 = cm(v2, v2), v8 = cm(v4, v4), v16 = cm(v8, v8);
  f32x2 B512 = cm(B256, B256), B1024 = cm(B512, B512), B768 = cm(B512, B256);
  f32x2 E0 = {Ein.x, -Ein.y};
  f32x2 E1 = cmc(E0, w1), E2 = cmc(E0, w2), E3 = cmc(E1, w2), E4 = cmc(E0, w4), E5 = cmc(E1, w4), E6 = cmc(E2, w4), E7 = cmc(E3, w4);
  stE(0, E0); stE(1, E1); stE(2, E2); stE(3, E3); stE(4, E4); stE(5, E5); stE(6, E6); stE(7, E7);
  stE(8, cmc(E0, B256)); stE(9, cmc(E2, B256)); stE(10, cmc(E4, B256)); stE(11, cmc(E6, B256));
  stE(12, cmc(E0, B512)); stE(13, cmc(E4, B512)); stE(14, cmc(E0, B768)); stE(15, cmc(E4, B768));
  f32x2 G0 = cmc(E0, B1024);
  stE(16, G0); stE(17, cmc(G0, B256)); stE(18, cmc(G0, B512)); stE(19, cmc(G0, B768)); stE(20, cmc(G0, B1024));
  f32x2 F[8];
  F[0].x = 1.f; F[0].y = 0.f; F[1] = v1; F[2] = v2; F[3] = cm(v2, v1); F[4] = v4; F[5] = cm(v4, v1); F[6] = cm(v4, v2); F[7] = cm(v4, F[3]);
  f32x2 v24 = cm(v16, v8);
#pragma unroll
  for (int i = 0; i < 8; ++i) {
    stF(i, F[i]);
    stF(8 + i, i == 0 ? v8 : cm(F[i], v8));
    stF(16 + i, i == 0 ? v16 : cm(F[i], v16));
    stF(24 + i, i == 0 ? v24 : cm(F[i], v24));
  }
  __builtin_amdgcn_wave_barrier();
  const int r = lane & 31, hh = lane >> 5;
  const char* pa = reg + (r < 21 ? r : 20) * TROW + 16 * hh;
  const char* pb = reg + (21 + r) * TROW + 16 * hh;
#pragma unroll
  for (int s = 0; s < 8; ++s) {
    h8 a = *(const h8*)(pa + 32 * s);
    h8 b = *(const h8*)(pb + 32 * s);
    kacc = __builtin_amdgcn_mfma_f32_32x32x16_f16(a, b, kacc, 0, 0, 0);
  }
  __builtin_amdgcn_wave_barrier();
}

struct KGP { const u16 *lar, *aim, *cr, *ci, *ldt; };
struct KGL { u16 pcr[3], pci[3], plar, paim, pldt; };
DI void kgen_load(const KGP& p, int d, int c, KGL& l) {
  const int lane = threadIdx.x & 63;
#pragma unroll
  for (int q = 0; q < 3; ++q) { int n = d * NS + 64 * q + lane; l.pcr[q] = p.cr[n]; l.pci[q] = p.ci[n]; }
  l.plar = p.lar[d * NS + 64 * c + lane]; l.paim = p.aim[d * NS + 64 * c + lane];
  l.pldt = p.ldt[d];
}
DI float kgen_task(const KGL& l, int c, char* reg, float* dst) {
  const int lane = threadIdx.x & 63;
  const float dt = rbf(expf(bf2f(l.pldt)));
  float cmax = 0.f;
#pragma unroll
  for (int q = 0; q < 3; ++q) cmax = fmaxf(cmax, fmaxf(fabsf(bf2f(l.pcr[q])), fabsf(bf2f(l.pci[q]))));
  cmax *= dt * 2.f;
#pragma unroll
  for (int o = 32; o >= 1; o >>= 1) cmax = fmaxf(cmax, __shfl_xor(cmax, o));
  int ce = (int)((__float_as_uint(cmax) >> 23) & 0xff);
  ce = ce < 30 ? 30 : (ce > 220 ? 220 : ce);
  const float sc = __uint_as_float((u32)(127 + 125 - ce) << 23);
  const float isc = __uint_as_float((u32)(ce + 2) << 23);
  const float Are = -rbf(expf(bf2f(l.plar)));
  const float ar2 = Are * dt * 1.4426950408889634f, airev = bf2f(l.paim) * dt * 0.15915494309189535f;
  const u16 mcr = c == 0 ? l.pcr[0] : (c == 1 ? l.pcr[1] : l.pcr[2]), mci = c == 0 ? l.pci[0] : (c == 1 ? l.pci[1] : l.pci[2]);
  f32x2 E = {bf2f(mcr) * dt * 2.f * sc, bf2f(mci) * dt * 2.f * sc};
  f32x16 kacc;
#pragma unroll
  for (int i = 0; i < 16; ++i) kacc[i] = 0.f;
  kgen_chunk(ar2, airev, E, reg, kacc);
  const int i = lane & 31, hh = lane >> 5;
#pragma unroll
  for (int rg = 0; rg < 12; ++rg) {
    const int q = rg >> 2, sub = rg & 3;
    int idx; bool ok = true;
    if (q == 0) idx = 8 * i + 4 * hh + sub;
    else if (q == 1) idx = hh == 0 ? 256 + 4 * i + sub : 384 + 64 * (sub >> 1) + 2 * i + (sub & 1);
    else { idx = hh == 0 ? 512 + 32 * sub + i : 640; ok = (hh == 0) || (sub == 0 && i == 0); }
    if (ok) dst[idx] = kacc[rg];
  }
  return isc * (1.f / 128.f);
}

template <bool KV>
DI void q_block(const u16* __restrict__ x, const u16* __restrict__ w, char* __restrict__ ws, rsrc_t rs, int rt, int ct, char* smem, bool& initdone) {
  const int tid = threadIdx.x, lane = tid & 63, wv = __builtin_amdgcn_readfirstlane(tid >> 6);
  const int row0 = rt * 128;
  const int fr = lane & 15, g = lane >> 4;
  if (wv >= 4) {
    const int lw = wv - 4;
    const u16* xs[4]; const u16* ws_[5];
#pragma unroll
    for (int i = 0; i < 4; ++i) { int pc = lw * 4 + i; int row = 8 * pc + (lane >> 3); int ch = (lane & 7) ^ ((row >> 1) & 7); xs[i] = x + (size_t)(row0 + row) * D + 8 * ch; }
#pragma unroll
    for (int i = 0; i < 5; ++i) {
      int pc = lw * 5 + i; int sb = 4 * pc + (lane >> 4); int kb = sb / 10, nb = sb % 10; int row = ((lane >> 1) & 7) ^ (4 * (kb & 1)); int c = nb * 16 + 8 * (lane & 1);
      int gc;
      if (KV) { int wn2 = c / 80, cc = c % 80; gc = cc < 40 ? 640 + 80 * ct + 40 * wn2 + cc : 1280 + 80 * ct + 40 * wn2 + (cc - 40); }
      else gc = 160 * ct + c;
      ws_[i] = w + (size_t)(8 * kb + row) * 1920 + gc;
    }
    auto stage = [&](int kt) {
      char* b = smem + (kt & (QNBUF - 1)) * QSTAGE;
#pragma unroll
      for (int i = 0; i < 4; ++i) __builtin_amdgcn_global_load_lds((const u32*)(xs[i] + kt * 64), (LAS u32*)(b + (lw * 4 + i) * 1024), 16, 0, 0);
#pragma unroll
      for (int i = 0; i < 5; ++i) __builtin_amdgcn_global_load_lds((const u32*)(ws_[i] + (size_t)kt * 64 * 1920), (LAS u32*)(b + 16384 + (lw * 5 + i) * 1024), 16, 0, 0);
    };
#pragma unroll 1
    for (int i = 0; i < QNBUF; ++i) stage(i);
    asm volatile("s_waitcnt vmcnt(%0)" :: "n"((QNBUF - 1) * 9) : "memory");
    __builtin_amdgcn_s_barrier();
#pragma unroll 1
    for (int kt = 0; kt < 6; ++kt) {
      asm volatile("s_waitcnt vmcnt(18)" ::: "memory");
      __builtin_amdgcn_s_barrier();
      stage(kt + QNBUF);
    }
    asm volatile("s_waitcnt vmcnt(18)" ::: "memory"); __builtin_amdgcn_s_barrier();
    asm volatile("s_waitcnt vmcnt(9)" ::: "memory");  __builtin_amdgcn_s_barrier();
    asm volatile("s_waitcnt vmcnt(0)" ::: "memory");  __builtin_amdgcn_s_barrier();
    if (KV) { __builtin_amdgcn_s_barrier(); __builtin_amdgcn_s_barrier(); }
  } else {
    const int wm = wv >> 1, wn = wv & 1;
    f32x4 acc[4][5];
#pragma unroll
    for (int m = 0; m < 4; ++m)
#pragma unroll
      for (int n = 0; n < 5; ++n) acc[m][n] = (f32x4){0, 0, 0, 0};
    const u32 aoff0 = (wm * 64 + fr) * 128 + (((0 + g) ^ (fr >> 1)) * 16);
    const u32 aoff1 = (wm * 64 + fr) * 128 + (((4 + g) ^ (fr >> 1)) * 16);
    const u32 boff0 = 16384 + g * 2560 + wn * 1280 + ((0 ^ (4 * (g & 1))) + (fr >> 2)) * 32 + (lane & 3) * 8;
    const u32 boff1 = 16384 + g * 2560 + wn * 1280 + ((4 ^ (4 * (g & 1))) + (fr >> 2)) * 32 + (lane & 3) * 8;
    const u32 sbase = (u32)(size_t)smem;
    s16x8 af[2][4]; s16x4 bl[2][5], bh[2][5];
    u32 a0 = sbase + aoff0, a1 = sbase + aoff1, b0 = sbase + boff0, b1 = sbase + boff1;
    auto ldop = [&](int ks, int i) {
      if (i < 10) {
        const int n = i >> 1;
        if (i & 1) bh[ks][n] = __builtin_amdgcn_ds_read_tr16_b64_v4i16((LAS s16x4*)(b1 + ks * 10240 + n * 256));
        else       bl[ks][n] = __builtin_amdgcn_ds_read_tr16_b64_v4i16((LAS s16x4*)(b0 + ks * 10240 + n * 256));
      } else af[ks][i - 10] = *(const LAS s16x8*)((ks ? a1 : a0) + (i - 10) * 2048);
    };
    auto mma1 = [&](int sl, int i) {
      const int m = i / 5, n = i % 5;
      s16x8 bb = __builtin_shufflevector(bl[sl][n], bh[sl][n], 0, 1, 2, 3, 4, 5, 6, 7);
      acc[m][n] = KV ? __builtin_amdgcn_mfma_f32_16x16x32_bf16(af[sl][m], bb, acc[m][n], 0, 0, 0)
                     : __builtin_amdgcn_mfma_f32_16x16x32_bf16(bb, af[sl][m], acc[m][n], 0, 0, 0);
    };
    __builtin_amdgcn_s_barrier();
    TLS(15);
#pragma unroll
    for (int i = 0; i < 14; ++i) ldop(0, i);
    int cur = 0;
#pragma unroll 1
    for (int kt = 0; kt < 10; ++kt) {
      __builtin_amdgcn_sched_barrier(0);
#pragma unroll
      for (int i = 0; i < 20; ++i) {
        mma1(0, i);
        if (i < 14) ldop(1, i);
        __builtin_amdgcn_sched_barrier(0);
      }
      cur = (cur + 1) & (QNBUF - 1);
      const u32 nb = sbase + cur * QSTAGE;
      a0 = nb + aoff0; a1 = nb + aoff1; b0 = nb + boff0; b1 = nb + boff1;
      __builtin_amdgcn_s_waitcnt(0xC07F);
      if (kt < 9) __builtin_amdgcn_s_barrier();
      __builtin_amdgcn_sched_barrier(0);
#pragma unroll
      for (int i = 0; i < 20; ++i) {
        mma1(1, i);
        if (i < 14) ldop(0, i);
        __builtin_amdgcn_sched_barrier(0);
      }
    }
    TLS(14);
    if (!KV) {
      char* pq = ws + WS_PQ;
#pragma unroll
      for (int m = 0; m < 4; ++m)
#pragma unroll
        for (int n = 0; n < 5; ++n) {
          u32x2 o = {phi_pair(acc[m][n][0], acc[m][n][1]), phi_pair(acc[m][n][2], acc[m][n][3])};
          const int col = 160 * ct + wn * 80 + n * 16 + 4 * g; const int h = col / 20, q = (col % 20) >> 2;
          st8_wt(pq + 2 * (((size_t)((rt * 8 + wm * 4 + m) * 32 + h) * 5 + q) * 64 + fr * 4), o);
        }
    } else {
      u32x2 pk[4][5];
      const bool isk = fr < 8;
#pragma unroll
      for (int m = 0; m < 4; ++m)
#pragma unroll
        for (int n = 0; n < 5; ++n) {
          if (n <= 2) { pk[m][n].x = phi_pair(acc[m][n][0], acc[m][n][1]); pk[m][n].y = phi_pair(acc[m][n][2], acc[m][n][3]); }
          if (n >= 2) {
            u32 v0 = pk_bf16(acc[m][n][0], acc[m][n][1]), v1 = pk_bf16(acc[m][n][2], acc[m][n][3]);
            if (n > 2) { pk[m][n].x = v0; pk[m][n].y = v1; } else { pk[m][n].x = isk ? pk[m][n].x : v0; pk[m][n].y = isk ? pk[m][n].y : v1; }
          }
        }
      f32x4 kv[10];
#pragma unroll
      for (int i = 0; i < 10; ++i) kv[i] = (f32x4){0, 0, 0, 0};
      const s16x8 ones = {0x3f80, 0x3f80, 0x3f80, 0x3f80, 0x3f80, 0x3f80, 0x3f80, 0x3f80};
#pragma unroll
      for (int u = 0; u < 2; ++u) {
        s16x8 f[5];
#pragma unroll
        for (int n = 0; n < 5; ++n) f[n] = __builtin_bit_cast(s16x8, (u32x4){pk[2 * u][n].x, pk[2 * u][n].y, pk[2 * u + 1][n].x, pk[2 * u + 1][n].y});
        kv[0] = __builtin_amdgcn_mfma_f32_16x16x32_bf16(f[0], f[2], kv[0], 0, 0, 0);
        kv[1] = __builtin_amdgcn_mfma_f32_16x16x32_bf16(f[0], f[3], kv[1], 0, 0, 0);
        kv[2] = __builtin_amdgcn_mfma_f32_16x16x32_bf16(f[1], f[2], kv[2], 0, 0, 0);
        kv[3] = __builtin_amdgcn_mfma_f32_16x16x32_bf16(f[1], f[3], kv[3], 0, 0, 0);
        kv[4] = __builtin_amdgcn_mfma_f32_16x16x32_bf16(f[1], f[4], kv[4], 0, 0, 0);
        kv[5] = __builtin_amdgcn_mfma_f32_16x16x32_bf16(f[2], f[3], kv[5], 0, 0, 0);
        kv[6] = __builtin_amdgcn_mfma_f32_16x16x32_bf16(f[2], f[4], kv[6], 0, 0, 0);
        kv[7] = __builtin_amdgcn_mfma_f32_16x16x32_bf16(f[0], ones, kv[7], 0, 0, 0);
        kv[8] = __builtin_amdgcn_mfma_f32_16x16x32_bf16(f[1], ones, kv[8], 0, 0, 0);
        kv[9] = __builtin_amdgcn_mfma_f32_16x16x32_bf16(f[2], ones, kv[9], 0, 0, 0);
      }
      __builtin_amdgcn_s_barrier();
      f32x4* red = (f32x4*)smem + (wm * 2 + wn) * 640 + lane;
#pragma unroll
      for (int i = 0; i < 10; ++i) red[i * 64] = kv[i];
      __builtin_amdgcn_s_waitcnt(0xC07F);
      __builtin_amdgcn_s_barrier();
    }
  }
  if (KV) {
    if (tid == 0) wait_init_t0(ws, initdone);
    __syncthreads();
    const float* raw = (const float*)smem;
    float* acc = (float*)(ws + WS_ZERO) + (size_t)ct * 4096 + tid;
#pragma unroll
    for (int it = 0; it < 8; ++it) {
      const int e0 = it * 512 + tid;
      const int hl4 = it >> 1, tile = it & 1, lp = (e0 >> 3) & 63, j = e0 & 7;
      const int row = lp & 15, gk = lp >> 4, gg = row >> 2, r = row & 3;
      const int wn = hl4 >> 1, hl = hl4 & 1;
      const int dd = 8 * gk + j;
      if ((tile == 0 || r < 2) && dd < 20) {
        const int d = dd + 20 * hl, dt = d >> 4;
        const int e = (tile == 0 ? 5 * gg + r : 5 * gg + 3 + r) + 20 * hl + 40, et = e >> 4;
        int t, ln;
        if (tile == 0 && r == 3) { t = 7 + dt; ln = ((d & 15) >> 2) * 16; }
        else { t = dt == 0 ? et - 2 : (dt == 1 ? et : et + 2); ln = ((d & 15) >> 2) * 16 + (e & 15); }
        const int idx = (wn * 640 + t * 64 + ln) * 4 + (d & 3);
        const float v = raw[idx] + raw[2 * 640 * 4 + idx];
        __hip_atomic_fetch_add((GAS float*)(acc + it * 512), v, RLX_AGENT);
      }
    }
    TLS(1);
    publish8(ws, CNT_KV, initdone);
  } else {
    TLS(1);
    publish(ws, CNT_PQ + rt, initdone);
  }
}

DI void aux_block(const u16* __restrict__ u, const u16* __restrict__ wo, char* __restrict__ ws, rsrc_t rs, int ab, char* smem, bool& initdone) {
  const int tid = threadIdx.x, lane = tid & 63, wv = __builtin_amdgcn_readfirstlane(tid >> 6);
  char* reg = smem + wv * KG_REG;
  if (wv < 5) {
    const int tile = ab * 5 + wv; const int rb = tile / 10, cb = tile % 10;
    u16* t = (u16*)reg;
#pragma unroll
    for (int i = 0; i < 8; ++i) {
      const int row = i * 8 + (lane >> 3), ch = lane & 7;
      u32x4 v = *(const u32x4*)(u + (size_t)(rb * 64 + row) * D + cb * 64 + ch * 8);
      u32* p = (u32*)(t + row * 66 + ch * 8);
      p[0] = v.x; p[1] = v.y; p[2] = v.z; p[3] = v.w;
    }
    __builtin_amdgcn_wave_barrier();
#pragma unroll
    for (int i = 0; i < 8; ++i) {
      const int c = i * 8 + (lane >> 3), r8 = lane & 7;
      u32 o[4];
#pragma unroll
      for (int k = 0; k < 4; ++k) o[k] = (u32)t[(r8 * 8 + 2 * k) * 66 + c] | ((u32)t[(r8 * 8 + 2 * k + 1) * 66 + c] << 16);
      st16_wt(rs, WS_UT + ((cb * 64 + c) * S + rb * 64 + r8 * 8) * 2, (u32x4){o[0], o[1], o[2], o[3]});
    }
    __builtin_amdgcn_wave_barrier();
  } else {
#pragma unroll
    for (int it = 0; it < 5; ++it) {
      const int uu = (wv - 5) * 64 + lane + 192 * it;
      if (uu < 800) {
        const int U = ab * 800 + uu; const int img = U / 1280, un = U % 1280; const int ct = img / 10, kt = img % 10;
        const int sb = un >> 4, kb = sb / 10, nb = sb % 10, j = ((un >> 1) & 7) ^ (4 * (kb & 1));
        const int T = 2 * kt + (kb >> 2), G = T / 5, tt = T % 5, ii = 8 * tt + j, hh = ii / 5, r = ii % 5;
        const int k = 160 * G + 20 * hh + 5 * (kb & 3) + r;
        st16_wt(rs, WS_WOP + U * 16, *(const u32x4*)(wo + (size_t)k * D + 160 * ct + nb * 16 + 8 * (un & 1)));
      }
    }
  }
  TLS(1);
  publish8(ws, CNT_T, initdone);
}

constexpr int UC_STRIDE = 4160, UCH = 4 * UC_STRIDE, KP_STRIDE = 1280, KPCH = 4 * KP_STRIDE;
constexpr int UC0 = 4 * KG_REG, PART0 = UC0 + 4 * UCH, KC0 = PART0 + 12 * 2592, RED0 = KC0 + 4 * 1312;
constexpr int B_LDS = RED0 + 128;
constexpr int PSPLIT = 22;
DI void b_block(const KGP& kp, char* __restrict__ ws, rsrc_t rs, const u16* __restrict__ dsk_, u16* __restrict__ out, int cb, char* smem, bool& initdone) {
  const int tid = threadIdx.x, lane = tid & 63, wv = __builtin_amdgcn_readfirstlane(tid >> 6), ch = wv >> 1, hf = wv & 1;
  const int d0 = cb * 4;
  float* red = (float*)(smem + RED0);
  float* part = (float*)(smem + PART0);
  KGL l0, l1;
  kgen_load(kp, d0 + wv / 3, wv % 3, l0);
  kgen_load(kp, d0 + (8 + (wv & 3)) / 3, (8 + (wv & 3)) % 3, l1);
  if (tid == 0) { wait_init_t0(ws, initdone); wait_cnt_t0(ws, CNT_T + (blockIdx.x & 7), 64); }
  {
    float ks = kgen_task(l0, wv % 3, smem + wv * KG_REG, part + wv * 648);
    if (lane == 0 && wv % 3 == 0) red[8 + wv / 3] = ks;
  }
  __syncthreads();
  TLS(3);
  if (wv < 4) {
    float ks = kgen_task(l1, (8 + wv) % 3, smem + wv * KG_REG, part + (8 + wv) * 648);
    if (lane == 0 && wv == 1) red[8 + 3] = ks;
  } else {
    const int c = wv - 4;
    char* ur = smem + UC0 + c * UCH;
    u32x4 uv[4];
#pragma unroll
    for (int i = 0; i < 4; ++i) uv[i] = ld16_sc(rs, WS_UT + ((d0 + c) * S + 8 * (lane + 64 * i)) * 2);
    float m = 0.f;
#pragma unroll
    for (int i = 0; i < 4; ++i)
#pragma unroll
      for (int k = 0; k < 4; ++k) m = fmaxf(m, fmaxf(fabsf(lo16(uv[i][k])), fabsf(hi16(uv[i][k]))));
#pragma unroll
    for (int o = 32; o >= 1; o >>= 1) m = fmaxf(m, __shfl_xor(m, o));
    int e = (int)((__float_as_uint(m) >> 23) & 0xff);
    e = e < 30 ? 30 : (e > 220 ? 220 : e);
    const float us = __uint_as_float((u32)(127 + 140 - e) << 23);
    if (lane == 0) red[c] = __uint_as_float((u32)(e - 13) << 23);
#pragma unroll
    for (int i = 0; i < 4; ++i) {
      const int q = lane + 64 * i;
      u32x4 o = {pk_f16(hi16(uv[i].w) * us, lo16(uv[i].w) * us), pk_f16(hi16(uv[i].z) * us, lo16(uv[i].z) * us),
                 pk_f16(hi16(uv[i].y) * us, lo16(uv[i].y) * us), pk_f16(hi16(uv[i].x) * us, lo16(uv[i].x) * us)};
      *(u32x4*)(ur + 2 * (2040 - 8 * q)) = o;
    }
    if (lane < 4) *(u32x4*)(ur + 4096 + 16 * lane) = (u32x4){0, 0, 0, 0};
    __builtin_amdgcn_wave_barrier();
#pragma unroll
    for (int k = 0; k < 5; ++k) {
      const int q = lane + 64 * k;
      if (q < 260) {
        u32x4 a = *(const u32x4*)(ur + 16 * q);
        u32x2 b = *(const u32x2*)(ur + 16 * q + 16);
        u32x4 c1 = {__builtin_amdgcn_alignbit(a.y, a.x, 16), __builtin_amdgcn_alignbit(a.z, a.y, 16), __builtin_amdgcn_alignbit(a.w, a.z, 16), __builtin_amdgcn_alignbit(b.x, a.w, 16)};
        u32x4 c2 = {a.y, a.z, a.w, b.x};
        u32x4 c3 = {c1.y, c1.z, c1.w, __builtin_amdgcn_alignbit(b.y, b.x, 16)};
        *(u32x4*)(ur + 1 * UC_STRIDE + 16 * q) = c1;
        *(u32x4*)(ur + 2 * UC_STRIDE + 16 * q) = c2;
        *(u32x4*)(ur + 3 * UC_STRIDE + 16 * q) = c3;
      }
    }
  }
  __syncthreads();
  TLS(11);
  {
    u16* kc = (u16*)(smem + KC0 + ch * 1312);
    const float* p0 = part + ch * 3 * 648;
#pragma unroll
    for (int i = 0; i < 2; ++i) {
      const int ci = hf * 81 + 64 * i + lane;
      if (lane + 64 * i < 81) {
        f32x4 v = *(const f32x4*)(p0 + ci * 4) + *(const f32x4*)(p0 + 648 + ci * 4) + *(const f32x4*)(p0 + 1296 + ci * 4);
        *(u32x2*)(kc + ci * 4) = (u32x2){pk_f16(v[0] * 128.f, v[1] * 128.f), pk_f16(v[2] * 128.f, v[3] * 128.f)};
      }
    }
  }
  __syncthreads();
  {
    const u16* kc = (const u16*)(smem + KC0 + ch * 1312);
    char* kpl = smem + ch * KPCH;
    if (hf == 1 && lane < 60) *(u32x4*)(kpl + (lane & 3) * KP_STRIDE + (lane >> 2) * 16) = (u32x4){0, 0, 0, 0};
#pragma unroll
    for (int k = 0; k < 2; ++k) {
      const int q = hf + 2 * (lane + 64 * k);
      u32 v[8];
#pragma unroll
      for (int j8 = 0; j8 < 8; ++j8) {
        int j = 8 * q + j8;
        u32 fb = __float_as_uint((float)j); fb += 0x7fffu + ((fb >> 16) & 1u);
        int idx = j < 256 ? j : 256 + (int)((fb >> 16) - 0x4380u);
        v[j8] = kc[idx];
      }
      u32x4 o = {v[0] | (v[1] << 16), v[2] | (v[3] << 16), v[4] | (v[5] << 16), v[6] | (v[7] << 16)};
      *(u32x4*)(kpl + (q & 3) * KP_STRIDE + ((q >> 2) + 15) * 16) = o;
    }
  }
  __syncthreads();
  TLS(12);
  f32x4 acc[4][2];
#pragma unroll
  for (int t = 0; t < 4; ++t) { acc[t][0] = (f32x4){0, 0, 0, 0}; acc[t][1] = (f32x4){0, 0, 0, 0}; }
  const int b = lane & 15, g = lane >> 4;
  char* ureg = smem + UC0 + ch * UCH;
  {
    const int cpy = (3 - b) & 3;
    const char* pa0 = ureg + cpy * UC_STRIDE + 2 * (2044 - 4 * (b >> 2) + 8 * g);
    const char* pb0 = smem + ch * KPCH + g * KP_STRIDE + 16 * (b + 15);
    typedef const volatile LAS u32x2* lp2;
    typedef const volatile LAS u32x4* lp4;
    const u32 abase = (u32)(size_t)(pa0 - 64 * 63 - 32);
    const u32 bbase = (u32)(size_t)(pb0 - 16 * 63);
    constexpr int DP = 2;
    u32x2 Ab[DP + 1][4]; u32x4 Bb[DP + 1][4];
    auto run = [&](auto P0c, auto P1c) {
      constexpr int P0 = decltype(P0c)::value, P1 = decltype(P1c)::value;
      auto issue = [&](int p) {
        const int sl = p % (DP + 1);
        const u32 oa = 64 * (63 - p);
        Ab[sl][0] = *(lp2)(abase + oa + 32); Ab[sl][1] = *(lp2)(abase + oa + 40);
        Ab[sl][2] = *(lp2)(abase + oa);      Ab[sl][3] = *(lp2)(abase + oa + 8);
#pragma unroll
        for (int t = 0; t < 4; ++t) if (t >= (p >> 4)) Bb[sl][t] = *(lp4)(bbase + 16 * (63 - p) + 256 * t);
      };
#pragma unroll
      for (int p = P0; p < P0 + DP; ++p) issue(p);
#pragma unroll
      for (int p = P0; p < P1; ++p) {
        if (p + DP < P1) issue(p + DP);
        __builtin_amdgcn_sched_barrier(0);
        const int sl = p % (DP + 1);
        h8 A0 = __builtin_bit_cast(h8, (u32x4){Ab[sl][0].x, Ab[sl][0].y, Ab[sl][1].x, Ab[sl][1].y});
        h8 A1 = __builtin_bit_cast(h8, (u32x4){Ab[sl][2].x, Ab[sl][2].y, Ab[sl][3].x, Ab[sl][3].y});
#pragma unroll
        for (int t = 0; t < 4; ++t) if (t >= (p >> 4)) {
          h8 B = __builtin_bit_cast(h8, Bb[sl][t]);
          acc[t][0] = __builtin_amdgcn_mfma_f32_16x16x32_f16(A0, B, acc[t][0], 0, 0, 0);
          acc[t][1] = __builtin_amdgcn_mfma_f32_16x16x32_f16(A1, B, acc[t][1], 0, 0, 0);
        }
        __builtin_amdgcn_sched_barrier(0);
      }
    };
    if (hf == 0) run(std::integral_constant<int, 0>{}, std::integral_constant<int, PSPLIT>{});
    else run(std::integral_constant<int, PSPLIT>{}, std::integral_constant<int, 64>{});
  }
  TLS(13);
  f32x4* xch = (f32x4*)(smem + PART0 + ch * 6144) + lane;
  if (hf == 1) {
#pragma unroll
    for (int t = 1; t < 4; ++t) { xch[(2 * (t - 1)) * 64] = acc[t][0]; xch[(2 * (t - 1) + 1) * 64] = acc[t][1]; }
  }
  __syncthreads();
  if (hf == 0) {
    const float ysc = red[8 + ch] * red[ch];
    char* yb = smem + ch * 4096;
#pragma unroll
    for (int t = 0; t < 4; ++t)
#pragma unroll
      for (int r = 0; r < 2; ++r) {
        f32x4 a = acc[t][r];
        if (t > 0) a += xch[(2 * (t - 1) + r) * 64];
        u32x2 o = {pk_bf16(a[0] * ysc, a[1] * ysc), pk_bf16(a[2] * ysc, a[3] * ysc)};
        *(u32x2*)(yb + 2 * (32 * (16 * t + b) + 16 * r + 4 * g)) = o;
      }
  }
  __syncthreads();
  {
    u32x2 yv[4], uu[4]; float iu[4], dk[4];
#pragma unroll
    for (int c = 0; c < 4; ++c) {
      yv[c] = *(const u32x2*)(smem + c * 4096 + 8 * tid);
      uu[c] = *(const u32x2*)(smem + UC0 + c * UCH + 2 * (2044 - 4 * tid));
      iu[c] = red[c]; dk[c] = bf2f(dsk_[d0 + c]);
    }
#pragma unroll
    for (int i = 0; i < 4; ++i) {
      float r[4];
#pragma unroll
      for (int c = 0; c < 4; ++c) {
        u32 yw = yv[c][i >> 1]; float y = (i & 1) ? hi16(yw) : lo16(yw);
        const u32 uw = (i < 2) ? uu[c].y : uu[c].x; const u16 ub = (i & 1) ? (u16)uw : (u16)(uw >> 16); float uf = (float)__builtin_bit_cast(_Float16, ub) * iu[c];
        r[c] = y + rbf(uf * dk[c]);
      }
      u32x2 o = {pk_bf16(r[0], r[1]), pk_bf16(r[2], r[3])};
      *(u32x2*)(out + (size_t)(4 * tid + i) * 1280 + 640 + d0) = o;
    }
  }
  __syncthreads();
}

constexpr int OSTAGE = 20480, ONBUF = 4, O_OFF_W = 81920;
DI void o_block(char* __restrict__ ws, rsrc_t rs, u16* __restrict__ out, int rt, int ct, char* smem, bool& initdone) {
  const int tid = threadIdx.x, lane = tid & 63, wv = __builtin_amdgcn_readfirstlane(tid >> 6);
  const int row0 = rt * 64, fr = lane & 15, g = lane >> 4;
  const int lw = wv - 4;
  if (tid == 0) { wait_init_t0(ws, initdone); wait_cnt_t0(ws, CNT_T + (blockIdx.x & 7), 64); }
  acquire_all();
  const char* wsrc = ws + WS_WOP + (size_t)ct * 10 * OSTAGE + lw * 5120 + lane * 16;
  auto stage = [&](int kt) {
    char* b = smem + O_OFF_W + (kt & (ONBUF - 1)) * OSTAGE + lw * 5120;
#pragma unroll
    for (int i = 0; i < 5; ++i) __builtin_amdgcn_global_load_lds((const u32*)(wsrc + kt * OSTAGE + i * 1024), (LAS u32*)(b + i * 1024), 16, 0, 0);
  };
  if (wv >= 4) {
#pragma unroll 1
    for (int i = 0; i < ONBUF; ++i) stage(i);
  }
  if (tid == 0) wait_cnt_t0(ws, CNT_PQ + (rt >> 1), 4);
  asm volatile("s_waitcnt lgkmcnt(0)" ::: "memory");
  __builtin_amdgcn_s_barrier();
  const char* pq = ws + WS_PQ;
  const char* zp = ws + WS_ZERO + CNT_OFF + ZPAD * 128;
  u32x4 kr[4][2][2]; u32x2 pa[4][4], pb[4][4];
#pragma unroll
  for (int hh = 0; hh < 4; ++hh)
#pragma unroll
    for (int st = 0; st < 4; ++st) {
      const char* p = pq + 2 * (((size_t)((rt * 4 + st) * 32 + 4 * wv + hh) * 5) * 64 + fr * 4);
      pa[hh][st] = ld8_sc(g < 3 ? p + 2 * g * 128 : zp);
      pb[hh][st] = ld8_sc(g < 2 ? p + (2 * g + 1) * 128 : zp);
    }
  if (tid == 0) wait_cnt_t0(ws, CNT_KV + (blockIdx.x & 7), 128);
  TLS(6);
  asm volatile("s_waitcnt lgkmcnt(0)" ::: "memory");
  __builtin_amdgcn_s_barrier();
  TLS(7);
#pragma unroll
  for (int hh = 0; hh < 4; ++hh)
#pragma unroll
    for (int tl = 0; tl < 2; ++tl) {
      kr[hh][tl][0] = ld16_sc(rs, WS_ZERO + (((4 * wv + hh) * 2 + tl) * 64 + lane) * 32);
      kr[hh][tl][1] = ld16_sc(rs, WS_ZERO + (((4 * wv + hh) * 2 + tl) * 64 + lane) * 32 + 16);
    }
  __builtin_amdgcn_sched_barrier(0);
  {
    s16x8 kf[4][2];
#pragma unroll
    for (int hh = 0; hh < 4; ++hh)
#pragma unroll
      for (int tl = 0; tl < 2; ++tl) {
        f32x4 a = __builtin_bit_cast(f32x4, kr[hh][tl][0]), b = __builtin_bit_cast(f32x4, kr[hh][tl][1]);
        kf[hh][tl] = __builtin_bit_cast(s16x8, (u32x4){pk_bf16(a[0], a[1]), pk_bf16(a[2], a[3]), pk_bf16(b[0], b[1]), pk_bf16(b[2], b[3])});
      }
    asm volatile("" :: "v"(kf[3][1]), "v"(pa[3][3]), "v"(pb[3][3]));
    TLS(10);
    const int G = wv >> 1, half = wv & 1;
#pragma unroll
    for (int st = 0; st < 4; ++st) {
      float vals[20];
#pragma unroll
      for (int hh = 0; hh < 4; ++hh) {
        s16x8 pf = __builtin_bit_cast(s16x8, (u32x4){pa[hh][st].x, pa[hh][st].y, pb[hh][st].x, pb[hh][st].y});
        f32x4 z4 = {0, 0, 0, 0};
        f32x4 t0 = __builtin_amdgcn_mfma_f32_16x16x32_bf16(kf[hh][0], pf, z4, 0, 0, 0);
        f32x4 t1 = __builtin_amdgcn_mfma_f32_16x16x32_bf16(kf[hh][1], pf, z4, 0, 0, 0);
        float den = rbf(rbf(t0[3]) + 1e-6f);
        float rd = __builtin_amdgcn_rcpf(den);
        vals[5 * hh + 0] = t0[0] * rd; vals[5 * hh + 1] = t0[1] * rd; vals[5 * hh + 2] = t0[2] * rd;
        vals[5 * hh + 3] = t1[0] * rd; vals[5 * hh + 4] = t1[1] * rd;
      }
#pragma unroll
      for (int i = 0; i < 5; ++i) {
        const int byte = 40 * half + 8 * i;
        const int T = 5 * G + (byte >> 4);
        *(u32x2*)(smem + (st * 20 + T) * 1024 + lane * 16 + (byte & 8)) = (u32x2){pk_bf16(vals[4 * i], vals[4 * i + 1]), pk_bf16(vals[4 * i + 2], vals[4 * i + 3])};
      }
    }
  }
  if (wv >= 4) {
    asm volatile("s_waitcnt vmcnt(0) lgkmcnt(0)" ::: "memory");
    __builtin_amdgcn_s_barrier();
#pragma unroll 1
    for (int kt = 0; kt < 6; ++kt) {
      asm volatile("s_waitcnt vmcnt(10)" ::: "memory");
      __builtin_amdgcn_s_barrier();
      stage(kt + ONBUF);
    }
    asm volatile("s_waitcnt vmcnt(10)" ::: "memory"); __builtin_amdgcn_s_barrier();
    asm volatile("s_waitcnt vmcnt(5)" ::: "memory");  __builtin_amdgcn_s_barrier();
    asm volatile("s_waitcnt vmcnt(0)" ::: "memory");  __builtin_amdgcn_s_barrier();
    return;
  }
  const int wm = wv >> 1, wn = wv & 1;
  f32x4 acc[2][5];
#pragma unroll
  for (int m = 0; m < 2; ++m)
#pragma unroll
    for (int n = 0; n < 5; ++n) acc[m][n] = (f32x4){0, 0, 0, 0};
  const u32 sbase = (u32)(size_t)smem;
  const u32 boff0 = O_OFF_W + g * 2560 + wn * 1280 + ((0 ^ (4 * (g & 1))) + (fr >> 2)) * 32 + (lane & 3) * 8;
  const u32 boff1 = O_OFF_W + g * 2560 + wn * 1280 + ((4 ^ (4 * (g & 1))) + (fr >> 2)) * 32 + (lane & 3) * 8;
  u32 aad = sbase + wm * 2 * 20 * 1024 + lane * 16;
  u32 b0 = sbase + boff0, b1 = sbase + boff1;
  s16x8 af[2][2]; s16x4 bl[2][5], bh[2][5];
  auto ldop = [&](int ks, int i) {
    if (i < 10) {
      const int n = i >> 1;
      if (i & 1) bh[ks][n] = __builtin_amdgcn_ds_read_tr16_b64_v4i16((LAS s16x4*)(b1 + ks * 10240 + n * 256));
      else       bl[ks][n] = __builtin_amdgcn_ds_read_tr16_b64_v4i16((LAS s16x4*)(b0 + ks * 10240 + n * 256));
    } else af[ks][i - 10] = *(const LAS s16x8*)(aad + ((i - 10) * 20 + ks) * 1024);
  };
  auto mma1 = [&](int sl, int i) {
    const int m = i / 5, n = i % 5;
    s16x8 bb = __builtin_shufflevector(bl[sl][n], bh[sl][n], 0, 1, 2, 3, 4, 5, 6, 7);
    acc[m][n] = __builtin_amdgcn_mfma_f32_16x16x32_bf16(bb, af[sl][m], acc[m][n], 0, 0, 0);
  };
  __builtin_amdgcn_s_waitcnt(0xC07F);
  __builtin_amdgcn_s_barrier();
  TLS(9);
#pragma unroll
  for (int i = 0; i < 12; ++i) ldop(0, i);
  int cur = 0;
#pragma unroll 1
  for (int kt = 0; kt < 10; ++kt) {
    __builtin_amdgcn_sched_barrier(0);
#pragma unroll
    for (int i = 0; i < 10; ++i) {
      mma1(0, i);
      ldop(1, i); if (i < 2) ldop(1, 10 + i);
      __builtin_amdgcn_sched_barrier(0);
    }
    cur = (cur + 1) & (ONBUF - 1);
    b0 = sbase + cur * OSTAGE + boff0; b1 = sbase + cur * OSTAGE + boff1;
    aad += (kt < 9) ? 2048 : 0;
    __builtin_amdgcn_s_waitcnt(0xC07F);
    if (kt < 9) __builtin_amdgcn_s_barrier();
    __builtin_amdgcn_sched_barrier(0);
#pragma unroll
    for (int i = 0; i < 10; ++i) {
      mma1(1, i);
      ldop(0, i); if (i < 2) ldop(0, 10 + i);
      __builtin_amdgcn_sched_barrier(0);
    }
  }
#pragma unroll
  for (int m = 0; m < 2; ++m)
#pragma unroll
    for (int n = 0; n < 5; ++n) {
      u32x2 o = {pk_bf16(acc[m][n][0], acc[m][n][1]), pk_bf16(acc[m][n][2], acc[m][n][3])};
      *(u32x2*)(out + (size_t)(row0 + wm * 32 + m * 16 + fr) * 1280 + 160 * ct + wn * 80 + n * 16 + 4 * g) = o;
    }
}

struct P { const u16 *x, *w, *wo, *u, *lar, *aim, *cr, *ci, *ldt, *dsk; u16* out; char* ws; };
__global__ __launch_bounds__(512, 2) void kmain(P p) {
  extern __shared__ __attribute__((aligned(16))) char smem[];
  const int b = blockIdx.x, tid = threadIdx.x;
  char* ws = p.ws;
  const rsrc_t rs = __builtin_amdgcn_make_buffer_rsrc((void*)ws, 0, WS_SPAN, 0x00020000);
  bool initdone = false;
  TLS(0);
  if (b >= 248) {
    const int part = b - 248;
    for (int i = tid; i < ZERO_BYTES / 128; i += 512) st16_wt(rs, WS_ZERO + part * (ZERO_BYTES / 8) + i * 16, (u32x4){0, 0, 0, 0});
    asm volatile("s_waitcnt vmcnt(0)" ::: "memory");
    __syncthreads();
    if (tid < 256) __hip_atomic_store((gu64*)(ws + WS_FLAG) + tid * 8 + part, MAGIC, RLX_AGENT);
  }
  const KGP kp = {p.lar, p.aim, p.cr, p.ci, p.ldt};
  int bt0 = -1, bt1 = -1, o_task = -1;
  if (b < 192) {
    const int xcd = b & 7, j = b >> 3;
    const int rt = (xcd >> 1) * 4 + (j & 3), c6 = j >> 2;
    if (c6 < 4) { q_block<true>(p.x, p.w, ws, rs, rt, (xcd & 1) * 4 + c6, smem, initdone); o_task = xcd * 16 + j; }
    else { q_block<false>(p.x, p.w, ws, rs, rt, (xcd & 1) * 2 + (c6 - 4), smem, initdone); bt0 = xcd * 20 + 12 + (j - 16); }
  } else {
    const int ab = b - 192;
    aux_block(p.u, p.wo, ws, rs, ab, smem, initdone);
    { const int xcd = ab & 7, i = ab >> 3; bt0 = xcd * 20 + i; if (i < 4) bt1 = xcd * 20 + 8 + i; }
  }
  TLS(2);
  __syncthreads();
#pragma unroll 1
  for (int it = 0; it < 2; ++it) {
    const int t = it == 0 ? bt0 : bt1;
    if (t >= 0) b_block(kp, ws, rs, p.dsk, p.out, t, smem, initdone);
  }
  TLS(5);
  if (o_task >= 0) o_block(ws, rs, p.out, o_task >> 2, o_task & 3, smem, initdone);
  TLS(8);
}

extern "C" void kernel_launch(void* const* d_in, const int* in_sizes, int n_in, void* d_out, int out_size, void* d_ws, size_t ws_size, hipStream_t stream) {
  constexpr int LDS = O_OFF_W + ONBUF * OSTAGE;
  static_assert(QNBUF * QSTAGE <= LDS && 8 * KG_REG <= LDS && B_LDS <= LDS, "LDS");
  (void)hipFuncSetAttribute((const void*)kmain, hipFuncAttributeMaxDynamicSharedMemorySize, LDS);
  P p = {(const u16*)d_in[0], (const u16*)d_in[1], (const u16*)d_in[2], (const u16*)d_in[3], (const u16*)d_in[4], (const u16*)d_in[5], (const u16*)d_in[6], (const u16*)d_in[7], (const u16*)d_in[8], (const u16*)d_in[9], (u16*)d_out, (char*)d_ws};
  kmain<<<256, 512, LDS, stream>>>(p);
}
```

```cpp
#include <hip/hip_runtime.h>
#include <hip/hip_bf16.h>
#include <stdint.h>
#include <type_traits>
typedef unsigned short u16;
typedef unsigned int u32;
typedef float f32x2 __attribute__((ext_vector_type(2)));
typedef float f32x4 __attribute__((ext_vector_type(4)));
typedef float f32x16 __attribute__((ext_vector_type(16)));
typedef short s16x4 __attribute__((ext_vector_type(4)));
typedef short s16x8 __attribute__((ext_vector_type(8)));
typedef _Float16 h8 __attribute__((ext_vector_type(8)));
typedef _Float16 h2 __attribute__((ext_vector_type(2)));
typedef __bf16 bf16x2 __attribute__((ext_vector_type(2)));
typedef u32 u32x2 __attribute__((ext_vector_type(2)));
typedef u32 u32x4 __attribute__((ext_vector_type(4)));
#define DI __device__ __forceinline__
#define LAS __attribute__((address_space(3)))
#define GAS __attribute__((address_space(1)))
typedef GAS unsigned gu32; typedef GAS unsigned long long gu64;
#define RLX_AGENT __ATOMIC_RELAXED, __HIP_MEMORY_SCOPE_AGENT
DI float bf2f(u16 x) { return __uint_as_float(((u32)x) << 16); }
DI u32 pk_bf16(float a, float b) { bf16x2 r = __builtin_convertvector((f32x2){a, b}, bf16x2); return __builtin_bit_cast(u32, r); }
DI u32 pk_f16(float a, float b) { h2 r = __builtin_convertvector((f32x2){a, b}, h2); return __builtin_bit_cast(u32, r); }
DI float lo16(u32 x) { return __uint_as_float(x << 16); }
DI float hi16(u32 x) { return __uint_as_float(x & 0xffff0000u); }
DI float rbf(float f) { return lo16(pk_bf16(f, 0.f)); }
DI u32 phi_pair(float a, float b) {
  u32 t = pk_bf16(a, b);
  float ta = lo16(t), tb = hi16(t);
  float ea = __builtin_amdgcn_fmed3f(__builtin_amdgcn_exp2f(ta * 1.4426950408889634f), 0.f, 1.f);
  float eb = __builtin_amdgcn_fmed3f(__builtin_amdgcn_exp2f(tb * 1.4426950408889634f), 0.f, 1.f);
  return pk_bf16(ea + fmaxf(ta, 1e-6f), eb + fmaxf(tb, 1e-6f));
}
DI f32x2 cmul(f32x2 x, f32x2 y) {
  f32x2 t, z;
  asm("v_pk_mul_f32 %0, %1, %2 op_sel_hi:[0,1]" : "=v"(t) : "v"(x), "v"(y));
  asm("v_pk_fma_f32 %0, %1, %2, %3 op_sel:[1,1,0] op_sel_hi:[1,0,1] neg_lo:[0,1,0]" : "=v"(z) : "v"(x), "v"(y), "v"(t));
  return z;
}
DI f32x2 cexpP(float ar2, float airev, float P) {
  float e = __builtin_amdgcn_exp2f(ar2 * P);
  float t = __builtin_amdgcn_fractf(airev * P);
  float s = __builtin_amdgcn_sinf(t), c = __builtin_amdgcn_cosf(t);
  f32x2 r = {e * c, e * s};
  return r;
}
constexpr int S = 2048, D = 640, NS = 192;
constexpr u32 WS_PQ = 0;
constexpr u32 WS_UT = 2621440;
constexpr u32 WS_WOP = 2 * 2621440;
constexpr u32 WS_FLAG = 2 * 2621440 + 819200;
constexpr u32 WS_ZERO = WS_FLAG + 16384;
constexpr int KVACC_BYTES = 32 * 1024 * 4;
constexpr int CNT_OFF = KVACC_BYTES;
constexpr int CNT_KV = 0, CNT_T = 8, CNT_PQ = 16, CNT_KG = 32, ZPAD = 40;
constexpr int ZERO_BYTES = KVACC_BYTES + 48 * 128;
constexpr u32 WS_SPAN = 7u << 20;
constexpr unsigned long long MAGIC = 0x9e3779b97f4a7c15ull;
constexpr unsigned SPIN_CAP = 1u << 17;
typedef __amdgpu_buffer_rsrc_t rsrc_t;

DI gu32* cnt_ptr(char* ws, int i) { return (gu32*)(ws + WS_ZERO + CNT_OFF + i * 128); }
DI void wait_init_t0(char* ws, bool& done) {
  if (done) return;
  gu64* f = (gu64*)(ws + WS_FLAG) + blockIdx.x * 8;
  for (unsigned sp = 0; sp < SPIN_CAP; ++sp) {
    bool ok = true;
#pragma unroll
    for (int i = 0; i < 8; ++i) ok &= __hip_atomic_load(f + i, RLX_AGENT) == MAGIC;
    if (ok) break;
    __builtin_amdgcn_s_sleep(2);
  }
#pragma unroll
  for (int i = 0; i < 8; ++i) __hip_atomic_store(f + i, 0ull, RLX_AGENT);
  done = true;
}
DI void wait_cnt_t0(char* ws, int i, unsigned want) {
  gu32* c = cnt_ptr(ws, i);
  if (__hip_atomic_load(c, RLX_AGENT) >= want) return;
  unsigned v0, v1, v2, v3;
#define PLD(v) asm volatile("global_load_dword %0, %1, off sc1" : "=v"(v) : "v"(c) : "memory")
  PLD(v0); __builtin_amdgcn_s_sleep(2); PLD(v1); __builtin_amdgcn_s_sleep(2); PLD(v2); __builtin_amdgcn_s_sleep(2);
  for (unsigned sp = 0; sp < SPIN_CAP; ++sp) {
    PLD(v3); asm volatile("s_waitcnt vmcnt(3)" : "+v"(v0) :: "memory"); if (v0 >= want) break; __builtin_amdgcn_s_sleep(2);
    PLD(v0); asm volatile("s_waitcnt vmcnt(3)" : "+v"(v1) :: "memory"); if (v1 >= want) break; __builtin_amdgcn_s_sleep(2);
    PLD(v1); asm volatile("s_waitcnt vmcnt(3)" : "+v"(v2) :: "memory"); if (v2 >= want) break; __builtin_amdgcn_s_sleep(2);
    PLD(v2); asm volatile("s_waitcnt vmcnt(3)" : "+v"(v3) :: "memory"); if (v3 >= want) break; __builtin_amdgcn_s_sleep(2);
  }
  asm volatile("s_waitcnt vmcnt(0)" : "+v"(v0), "+v"(v1), "+v"(v2), "+v"(v3) :: "memory");
#undef PLD
}
DI void publish(char* ws, int i, bool& initdone) {
  asm volatile("s_waitcnt vmcnt(0)" ::: "memory");
  __syncthreads();
  if (threadIdx.x == 0) { wait_init_t0(ws, initdone); __hip_atomic_fetch_add(cnt_ptr(ws, i), 1u, RLX_AGENT); }
}
DI void publish8(char* ws, int i, bool& initdone) {
  if (threadIdx.x == 0) wait_init_t0(ws, initdone);
  asm volatile("s_waitcnt vmcnt(0)" ::: "memory");
  __syncthreads();
  if (threadIdx.x < 8) __hip_atomic_fetch_add(cnt_ptr(ws, i + threadIdx.x), 1u, RLX_AGENT);
}
DI u32x4 ld16_sc(rsrc_t r, u32 off) { return __builtin_amdgcn_raw_buffer_load_b128(r, off, 0, 16); }
DI u32x2 ld8_sc(const void* p) { unsigned long long v = __hip_atomic_load((gu64*)p, RLX_AGENT); return (u32x2){(u32)v, (u32)(v >> 32)}; }
DI float ld4_sc(const void* p) { return __uint_as_float(__hip_atomic_load((gu32*)p, RLX_AGENT)); }
DI void acquire_all() {
  if (threadIdx.x == 0) { __builtin_amdgcn_fence(__ATOMIC_ACQUIRE, "agent"); asm volatile("s_waitcnt vmcnt(0)" ::: "memory"); }
  __syncthreads();
}
DI void st16_wt(rsrc_t r, u32 off, u32x4 v) { __builtin_amdgcn_raw_buffer_store_b128(v, r, off, 0, 16); }
DI void st8_wt(void* p, u32x2 v) { __hip_atomic_store((gu64*)p, ((unsigned long long)v.y << 32) | v.x, RLX_AGENT); }

#define TLS(i)
constexpr int QSTAGE = 36864, QNBUF = 4;
constexpr int TROW = 272, KG_REG = 14720;

DI f32x2 cm(f32x2 x, f32x2 y) {
  f32x2 xx = {x.x, x.x}, xy = {x.y, x.y}, ys = {-y.y, y.x};
  return __builtin_elementwise_fma(xy, ys, xx * y);
}
DI f32x2 cmc(f32x2 x, f32x2 y) {
  f32x2 xx = {x.x, x.x}, xy = {x.y, x.y}, yc = {y.x, -y.y}, ys = {y.y, y.x};
  return __builtin_elementwise_fma(xy, ys, xx * yc);
}
DI void kgen_chunk(float a2, float av, f32x2 Ein, char* reg, f32x16& kacc) {
  const int lane = threadIdx.x & 63;
  u32* tE = (u32*)(reg) + lane;
  u32* tF = (u32*)(reg + 21 * TROW) + lane;
  constexpr int R = TROW / 4;
  auto stE = [&](int r, f32x2 v) { tE[r * R] = pk_f16(v.x, v.y); };
  auto stF = [&](int r, f32x2 v) { tF[r * R] = pk_f16(v.x, v.y); };
  f32x2 w1 = cexpP(a2, av, 1.f), v1 = cexpP(a2, av, 8.f), B256 = cexpP(a2, av, 256.f);
  f32x2 w2 = cm(w1, w1), w4 = cm(w2, w2);
  f32x2 v2 = cm(v1, v1), v4 = cm(v2, v2), v8 = cm(v4, v4), v16 = cm(v8, v8);
  f32x2 B512 = cm(B256, B256), B1024 = cm(B512, B512), B768 = cm(B512, B256);
  f32x2 E0 = {Ein.x, -Ein.y};
  f32x2 E1 = cmc(E0, w1), E2 = cmc(E0, w2), E3 = cmc(E1, w2), E4 = cmc(E0, w4), E5 = cmc(E1, w4), E6 = cmc(E2, w4), E7 = cmc(E3, w4);
  stE(0, E0); stE(1, E1); stE(2, E2); stE(3, E3); stE(4, E4); stE(5, E5); stE(6, E6); stE(7, E7);
  stE(8, cmc(E0, B256)); stE(9, cmc(E2, B256)); stE(10, cmc(E4, B256)); stE(11, cmc(E6, B256));
  stE(12, cmc(E0, B512)); stE(13, cmc(E4, B512)); stE(14, cmc(E0, B768)); stE(15, cmc(E4, B768));
  f32x2 G0 = cmc(E0, B1024);
  stE(16, G0); stE(17, cmc(G0, B256)); stE(18, cmc(G0, B512)); stE(19, cmc(G0, B768)); stE(20, cmc(G0, B1024));
  f32x2 F[8];
  F[0].x = 1.f; F[0].y = 0.f; F[1] = v1; F[2] = v2; F[3] = cm(v2, v1); F[4] = v4; F[5] = cm(v4, v1); F[6] = cm(v4, v2); F[7] = cm(v4, F[3]);
  f32x2 v24 = cm(v16, v8);
#pragma unroll
  for (int i = 0; i < 8; ++i) {
    stF(i, F[i]);
    stF(8 + i, i == 0 ? v8 : cm(F[i], v8));
    stF(16 + i, i == 0 ? v16 : cm(F[i], v16));
    stF(24 + i, i == 0 ? v24 : cm(F[i], v24));
  }
  __builtin_amdgcn_wave_barrier();
  const int r = lane & 31, hh = lane >> 5;
  const char* pa = reg + (r < 21 ? r : 20) * TROW + 16 * hh;
  const char* pb = reg + (21 + r) * TROW + 16 * hh;
#pragma unroll
  for (int s = 0; s < 8; ++s) {
    h8 a = *(const h8*)(pa + 32 * s);
    h8 b = *(const h8*)(pb + 32 * s);
    kacc = __builtin_amdgcn_mfma_f32_32x32x16_f16(a, b, kacc, 0, 0, 0);
  }
  __builtin_amdgcn_wave_barrier();
}

struct KGP { const u16 *lar, *aim, *cr, *ci, *ldt; };
struct KGL { u16 pcr[3], pci[3], plar, paim, pldt; };
DI void kgen_load(const KGP& p, int d, int c, KGL& l) {
  const int lane = threadIdx.x & 63;
#pragma unroll
  for (int q = 0; q < 3; ++q) { int n = d * NS + 64 * q + lane; l.pcr[q] = p.cr[n]; l.pci[q] = p.ci[n]; }
  l.plar = p.lar[d * NS + 64 * c + lane]; l.paim = p.aim[d * NS + 64 * c + lane];
  l.pldt = p.ldt[d];
}
DI float kgen_task(const KGL& l, int c, char* reg, float* dst) {
  const int lane = threadIdx.x & 63;
  const float dt = rbf(expf(bf2f(l.pldt)));
  float cmax = 0.f;
#pragma unroll
  for (int q = 0; q < 3; ++q) cmax = fmaxf(cmax, fmaxf(fabsf(bf2f(l.pcr[q])), fabsf(bf2f(l.pci[q]))));
  cmax *= dt * 2.f;
#pragma unroll
  for (int o = 32; o >= 1; o >>= 1) cmax = fmaxf(cmax, __shfl_xor(cmax, o));
  int ce = (int)((__float_as_uint(cmax) >> 23) & 0xff);
  ce = ce < 30 ? 30 : (ce > 220 ? 220 : ce);
  const float sc = __uint_as_float((u32)(127 + 125 - ce) << 23);
  const float isc = __uint_as_float((u32)(ce + 2) << 23);
  const float Are = -rbf(expf(bf2f(l.plar)));
  const float ar2 = Are * dt * 1.4426950408889634f, airev = bf2f(l.paim) * dt * 0.15915494309189535f;
  const u16 mcr = c == 0 ? l.pcr[0] : (c == 1 ? l.pcr[1] : l.pcr[2]), mci = c == 0 ? l.pci[0] : (c == 1 ? l.pci[1] : l.pci[2]);
  f32x2 E = {bf2f(mcr) * dt * 2.f * sc, bf2f(mci) * dt * 2.f * sc};
  f32x16 kacc;
#pragma unroll
  for (int i = 0; i < 16; ++i) kacc[i] = 0.f;
  kgen_chunk(ar2, airev, E, reg, kacc);
  const int i = lane & 31, hh = lane >> 5;
#pragma unroll
  for (int rg = 0; rg < 12; ++rg) {
    const int q = rg >> 2, sub = rg & 3;
    int idx; bool ok = true;
    if (q == 0) idx = 8 * i + 4 * hh + sub;
    else if (q == 1) idx = hh == 0 ? 256 + 4 * i + sub : 384 + 64 * (sub >> 1) + 2 * i + (sub & 1);
    else { idx = hh == 0 ? 512 + 32 * sub + i : 640; ok = (hh == 0) || (sub == 0 && i == 0); }
    if (ok) dst[idx] = kacc[rg];
  }
  return isc * (1.f / 128.f);
}

template <bool KV>
DI void q_block(const u16* __restrict__ x, const u16* __restrict__ w, char* __restrict__ ws, rsrc_t rs, int rt, int ct, char* smem, bool& initdone) {
  const int tid = threadIdx.x, lane = tid & 63, wv = __builtin_amdgcn_readfirstlane(tid >> 6);
  const int row0 = rt * 128;
  const int fr = lane & 15, g = lane >> 4;
  if (wv >= 4) {
    const int lw = wv - 4;
    const u16* xs[4]; const u16* ws_[5];
#pragma unroll
    for (int i = 0; i < 4; ++i) { int pc = lw * 4 + i; int row = 8 * pc + (lane >> 3); int ch = (lane & 7) ^ ((row >> 1) & 7); xs[i] = x + (size_t)(row0 + row) * D + 8 * ch; }
#pragma unroll
    for (int i = 0; i < 5; ++i) {
      int pc = lw * 5 + i; int sb = 4 * pc + (lane >> 4); int kb = sb / 10, nb = sb % 10; int row = ((lane >> 1) & 7) ^ (4 * (kb & 1)); int c = nb * 16 + 8 * (lane & 1);
      int gc;
      if (KV) { int wn2 = c / 80, cc = c % 80; gc = cc < 40 ? 640 + 80 * ct + 40 * wn2 + cc : 1280 + 80 * ct + 40 * wn2 + (cc - 40); }
      else gc = 160 * ct + c;
      ws_[i] = w + (size_t)(8 * kb + row) * 1920 + gc;
    }
    auto stage = [&](int kt) {
      char* b = smem + (kt & (QNBUF - 1)) * QSTAGE;
#pragma unroll
      for (int i = 0; i < 4; ++i) __builtin_amdgcn_global_load_lds((const u32*)(xs[i] + kt * 64), (LAS u32*)(b + (lw * 4 + i) * 1024), 16, 0, 0);
#pragma unroll
      for (int i = 0; i < 5; ++i) __builtin_amdgcn_global_load_lds((const u32*)(ws_[i] + (size_t)kt * 64 * 1920), (LAS u32*)(b + 16384 + (lw * 5 + i) * 1024), 16, 0, 0);
    };
#pragma unroll 1
    for (int i = 0; i < QNBUF; ++i) stage(i);
    asm volatile("s_waitcnt vmcnt(%0)" :: "n"((QNBUF - 1) * 9) : "memory");
    __builtin_amdgcn_s_barrier();
#pragma unroll 1
    for (int kt = 0; kt < 6; ++kt) {
      asm volatile("s_waitcnt vmcnt(18)" ::: "memory");
      __builtin_amdgcn_s_barrier();
      stage(kt + QNBUF);
    }
    asm volatile("s_waitcnt vmcnt(18)" ::: "memory"); __builtin_amdgcn_s_barrier();
    asm volatile("s_waitcnt vmcnt(9)" ::: "memory");  __builtin_amdgcn_s_barrier();
    asm volatile("s_waitcnt vmcnt(0)" ::: "memory");  __builtin_amdgcn_s_barrier();
    if (KV) { __builtin_amdgcn_s_barrier(); __builtin_amdgcn_s_barrier(); }
  } else {
    u32 tch = 0;
    {
      const int t = tid;
#pragma unroll
      for (int i = 0; i < 3; ++i) { const int e = i * 256 + t; const int row = e / 6, l6 = e % 6; tch ^= *(const volatile u32*)(x + (size_t)(row0 + row) * D + 256 + l6 * 64); }
#pragma unroll
      for (int i = 0; i < 6; ++i) {
        const int e = i * 256 + t; const int k = 256 + (e >> 2), q = e & 3;
        int gc;
        if (KV) gc = (q < 2 ? 640 : 1280) + 80 * ct + (q & 1) * 79; else gc = 160 * ct + q * 53;
        tch ^= *(const volatile u16*)(w + (size_t)k * 1920 + gc);
      }
    }
    const int wm = wv >> 1, wn = wv & 1;
    f32x4 acc[4][5];
#pragma unroll
    for (int m = 0; m < 4; ++m)
#pragma unroll
      for (int n = 0; n < 5; ++n) acc[m][n] = (f32x4){0, 0, 0, 0};
    const u32 aoff0 = (wm * 64 + fr) * 128 + (((0 + g) ^ (fr >> 1)) * 16);
    const u32 aoff1 = (wm * 64 + fr) * 128 + (((4 + g) ^ (fr >> 1)) * 16);
    const u32 boff0 = 16384 + g * 2560 + wn * 1280 + ((0 ^ (4 * (g & 1))) + (fr >> 2)) * 32 + (lane & 3) * 8;
    const u32 boff1 = 16384 + g * 2560 + wn * 1280 + ((4 ^ (4 * (g & 1))) + (fr >> 2)) * 32 + (lane & 3) * 8;
    const u32 sbase = (u32)(size_t)smem;
    s16x8 af[2][4]; s16x4 bl[2][5], bh[2][5];
    u32 a0 = sbase + aoff0, a1 = sbase + aoff1, b0 = sbase + boff0, b1 = sbase + boff1;
    auto ldop = [&](int ks, int i) {
      if (i < 10) {
        const int n = i >> 1;
        if (i & 1) bh[ks][n] = __builtin_amdgcn_ds_read_tr16_b64_v4i16((LAS s16x4*)(b1 + ks * 10240 + n * 256));
        else       bl[ks][n] = __builtin_amdgcn_ds_read_tr16_b64_v4i16((LAS s16x4*)(b0 + ks * 10240 + n * 256));
      } else af[ks][i - 10] = *(const LAS s16x8*)((ks ? a1 : a0) + (i - 10) * 2048);
    };
    auto mma1 = [&](int sl, int i) {
      const int m = i / 5, n = i % 5;
      s16x8 bb = __builtin_shufflevector(bl[sl][n], bh[sl][n], 0, 1, 2, 3, 4, 5, 6, 7);
      acc[m][n] = KV ? __builtin_amdgcn_mfma_f32_16x16x32_bf16(af[sl][m], bb, acc[m][n], 0, 0, 0)
                     : __builtin_amdgcn_mfma_f32_16x16x32_bf16(bb, af[sl][m], acc[m][n], 0, 0, 0);
    };
    __builtin_amdgcn_s_barrier();
    TLS(15);
#pragma unroll
    for (int i = 0; i < 14; ++i) ldop(0, i);
    int cur = 0;
#pragma unroll 1
    for (int kt = 0; kt < 10; ++kt) {
      __builtin_amdgcn_sched_barrier(0);
#pragma unroll
      for (int i = 0; i < 20; ++i) {
        mma1(0, i);
        if (i < 14) ldop(1, i);
        __builtin_amdgcn_sched_barrier(0);
      }
      cur = (cur + 1) & (QNBUF - 1);
      const u32 nb = sbase + cur * QSTAGE;
      a0 = nb + aoff0; a1 = nb + aoff1; b0 = nb + boff0; b1 = nb + boff1;
      __builtin_amdgcn_s_waitcnt(0xC07F);
      if (kt < 9) __builtin_amdgcn_s_barrier();
      __builtin_amdgcn_sched_barrier(0);
#pragma unroll
      for (int i = 0; i < 20; ++i) {
        mma1(1, i);
        if (i < 14) ldop(0, i);
        __builtin_amdgcn_sched_barrier(0);
      }
    }
    asm volatile("" :: "v"(tch));
    TLS(14);
    if (!KV) {
      char* pq = ws + WS_PQ;
#pragma unroll
      for (int m = 0; m < 4; ++m)
#pragma unroll
        for (int n = 0; n < 5; ++n) {
          u32x2 o = {phi_pair(acc[m][n][0], acc[m][n][1]), phi_pair(acc[m][n][2], acc[m][n][3])};
          const int col = 160 * ct + wn * 80 + n * 16 + 4 * g; const int h = col / 20, q = (col % 20) >> 2;
          st8_wt(pq + 2 * (((size_t)((rt * 8 + wm * 4 + m) * 32 + h) * 5 + q) * 64 + fr * 4), o);
        }
    } else {
      u32x2 pk[4][5];
      const bool isk = fr < 8;
#pragma unroll
      for (int m = 0; m < 4; ++m)
#pragma unroll
        for (int n = 0; n < 5; ++n) {
          if (n <= 2) { pk[m][n].x = phi_pair(acc[m][n][0], acc[m][n][1]); pk[m][n].y = phi_pair(acc[m][n][2], acc[m][n][3]); }
          if (n >= 2) {
            u32 v0 = pk_bf16(acc[m][n][0], acc[m][n][1]), v1 = pk_bf16(acc[m][n][2], acc[m][n][3]);
            if (n > 2) { pk[m][n].x = v0; pk[m][n].y = v1; } else { pk[m][n].x = isk ? pk[m][n].x : v0; pk[m][n].y = isk ? pk[m][n].y : v1; }
          }
        }
      f32x4 kv[10];
#pragma unroll
      for (int i = 0; i < 10; ++i) kv[i] = (f32x4){0, 0, 0, 0};
      const s16x8 ones = {0x3f80, 0x3f80, 0x3f80, 0x3f80, 0x3f80, 0x3f80, 0x3f80, 0x3f80};
#pragma unroll
      for (int u = 0; u < 2; ++u) {
        s16x8 f[5];
#pragma unroll
        for (int n = 0; n < 5; ++n) f[n] = __builtin_bit_cast(s16x8, (u32x4){pk[2 * u][n].x, pk[2 * u][n].y, pk[2 * u + 1][n].x, pk[2 * u + 1][n].y});
        kv[0] = __builtin_amdgcn_mfma_f32_16x16x32_bf16(f[0], f[2], kv[0], 0, 0, 0);
        kv[1] = __builtin_amdgcn_mfma_f32_16x16x32_bf16(f[0], f[3], kv[1], 0, 0, 0);
        kv[2] = __builtin_amdgcn_mfma_f32_16x16x32_bf16(f[1], f[2], kv[2], 0, 0, 0);
        kv[3] = __builtin_amdgcn_mfma_f32_16x16x32_bf16(f[1], f[3], kv[3], 0, 0, 0);
        kv[4] = __builtin_amdgcn_mfma_f32_16x16x32_bf16(f[1], f[4], kv[4], 0, 0, 0);
        kv[5] = __builtin_amdgcn_mfma_f32_16x16x32_bf16(f[2], f[3], kv[5], 0, 0, 0);
        kv[6] = __builtin_amdgcn_mfma_f32_16x16x32_bf16(f[2], f[4], kv[6], 0, 0, 0);
        kv[7] = __builtin_amdgcn_mfma_f32_16x16x32_bf16(f[0], ones, kv[7], 0, 0, 0);
        kv[8] = __builtin_amdgcn_mfma_f32_16x16x32_bf16(f[1], ones, kv[8], 0, 0, 0);
        kv[9] = __builtin_amdgcn_mfma_f32_16x16x32_bf16(f[2], ones, kv[9], 0, 0, 0);
      }
      __builtin_amdgcn_s_barrier();
      f32x4* red = (f32x4*)smem + (wm * 2 + wn) * 640 + lane;
#pragma unroll
      for (int i = 0; i < 10; ++i) red[i * 64] = kv[i];
      __builtin_amdgcn_s_waitcnt(0xC07F);
      __builtin_amdgcn_s_barrier();
    }
  }
  if (KV) {
    if (tid == 0) wait_init_t0(ws, initdone);
    __syncthreads();
    const float* raw = (const float*)smem;
    float* acc = (float*)(ws + WS_ZERO) + (size_t)ct * 4096 + tid;
#pragma unroll
    for (int it = 0; it < 8; ++it) {
      const int e0 = it * 512 + tid;
      const int hl4 = it >> 1, tile = it & 1, lp = (e0 >> 3) & 63, j = e0 & 7;
      const int row = lp & 15, gk = lp >> 4, gg = row >> 2, r = row & 3;
      const int wn = hl4 >> 1, hl = hl4 & 1;
      const int dd = 8 * gk + j;
      if ((tile == 0 || r < 2) && dd < 20) {
        const int d = dd + 20 * hl, dt = d >> 4;
        const int e = (tile == 0 ? 5 * gg + r : 5 * gg + 3 + r) + 20 * hl + 40, et = e >> 4;
        int t, ln;
        if (tile == 0 && r == 3) { t = 7 + dt; ln = ((d & 15) >> 2) * 16; }
        else { t = dt == 0 ? et - 2 : (dt == 1 ? et : et + 2); ln = ((d & 15) >> 2) * 16 + (e & 15); }
        const int idx = (wn * 640 + t * 64 + ln) * 4 + (d & 3);
        const float v = raw[idx] + raw[2 * 640 * 4 + idx];
        __hip_atomic_fetch_add((GAS float*)(acc + it * 512), v, RLX_AGENT);
      }
    }
    TLS(1);
    publish8(ws, CNT_KV, initdone);
  } else {
    TLS(1);
    publish(ws, CNT_PQ + rt, initdone);
  }
}

DI void aux_block(const u16* __restrict__ u, const u16* __restrict__ wo, char* __restrict__ ws, rsrc_t rs, int ab, char* smem, bool& initdone) {
  const int tid = threadIdx.x, lane = tid & 63, wv = __builtin_amdgcn_readfirstlane(tid >> 6);
  char* reg = smem + wv * KG_REG;
  if (wv < 5) {
    const int tile = ab * 5 + wv; const int rb = tile / 10, cb = tile % 10;
    u16* t = (u16*)reg;
#pragma unroll
    for (int i = 0; i < 8; ++i) {
      const int row = i * 8 + (lane >> 3), ch = lane & 7;
      u32x4 v = *(const u32x4*)(u + (size_t)(rb * 64 + row) * D + cb * 64 + ch * 8);
      u32* p = (u32*)(t + row * 66 + ch * 8);
      p[0] = v.x; p[1] = v.y; p[2] = v.z; p[3] = v.w;
    }
    __builtin_amdgcn_wave_barrier();
#pragma unroll
    for (int i = 0; i < 8; ++i) {
      const int c = i * 8 + (lane >> 3), r8 = lane & 7;
      u32 o[4];
#pragma unroll
      for (int k = 0; k < 4; ++k) o[k] = (u32)t[(r8 * 8 + 2 * k) * 66 + c] | ((u32)t[(r8 * 8 + 2 * k + 1) * 66 + c] << 16);
      st16_wt(rs, WS_UT + ((cb * 64 + c) * S + rb * 64 + r8 * 8) * 2, (u32x4){o[0], o[1], o[2], o[3]});
    }
    __builtin_amdgcn_wave_barrier();
  } else {
#pragma unroll
    for (int it = 0; it < 5; ++it) {
      const int uu = (wv - 5) * 64 + lane + 192 * it;
      if (uu < 800) {
        const int U = ab * 800 + uu; const int img = U / 1280, un = U % 1280; const int ct = img / 10, kt = img % 10;
        const int sb = un >> 4, kb = sb / 10, nb = sb % 10, j = ((un >> 1) & 7) ^ (4 * (kb & 1));
        const int T = 2 * kt + (kb >> 2), G = T / 5, tt = T % 5, ii = 8 * tt + j, hh = ii / 5, r = ii % 5;
        const int k = 160 * G + 20 * hh + 5 * (kb & 3) + r;
        st16_wt(rs, WS_WOP + U * 16, *(const u32x4*)(wo + (size_t)k * D + 160 * ct + nb * 16 + 8 * (un & 1)));
      }
    }
  }
  TLS(1);
  publish8(ws, CNT_T, initdone);
}

constexpr int UC_STRIDE = 4160, UCH = 4 * UC_STRIDE, KP_STRIDE = 1280, KPCH = 4 * KP_STRIDE;
constexpr int UC0 = 4 * KG_REG, PART0 = UC0 + 4 * UCH, KC0 = PART0 + 12 * 2592, RED0 = KC0 + 4 * 1312;
constexpr int B_LDS = RED0 + 128;
constexpr int PSPLIT = 22;
DI void b_block(const KGP& kp, char* __restrict__ ws, rsrc_t rs, const u16* __restrict__ dsk_, u16* __restrict__ out, int cb, char* smem, bool& initdone) {
  const int tid = threadIdx.x, lane = tid & 63, wv = __builtin_amdgcn_readfirstlane(tid >> 6), ch = wv >> 1, hf = wv & 1;
  const int d0 = cb * 4;
  float* red = (float*)(smem + RED0);
  float* part = (float*)(smem + PART0);
  KGL l0, l1;
  kgen_load(kp, d0 + wv / 3, wv % 3, l0);
  kgen_load(kp, d0 + (8 + (wv & 3)) / 3, (8 + (wv & 3)) % 3, l1);
  if (tid == 0) { wait_init_t0(ws, initdone); wait_cnt_t0(ws, CNT_T + (blockIdx.x & 7), 64); }
  {
    float ks = kgen_task(l0, wv % 3, smem + wv * KG_REG, part + wv * 648);
    if (lane == 0 && wv % 3 == 0) red[8 + wv / 3] = ks;
  }
  __syncthreads();
  TLS(3);
  if (wv < 4) {
    float ks = kgen_task(l1, (8 + wv) % 3, smem + wv * KG_REG, part + (8 + wv) * 648);
    if (lane == 0 && wv == 1) red[8 + 3] = ks;
  } else {
    const int c = wv - 4;
    char* ur = smem + UC0 + c * UCH;
    u32x4 uv[4];
#pragma unroll
    for (int i = 0; i < 4; ++i) uv[i] = ld16_sc(rs, WS_UT + ((d0 + c) * S + 8 * (lane + 64 * i)) * 2);
    float m = 0.f;
#pragma unroll
    for (int i = 0; i < 4; ++i)
#pragma unroll
      for (int k = 0; k < 4; ++k) m = fmaxf(m, fmaxf(fabsf(lo16(uv[i][k])), fabsf(hi16(uv[i][k]))));
#pragma unroll
    for (int o = 32; o >= 1; o >>= 1) m = fmaxf(m, __shfl_xor(m, o));
    int e = (int)((__float_as_uint(m) >> 23) & 0xff);
    e = e < 30 ? 30 : (e > 220 ? 220 : e);
    const float us = __uint_as_float((u32)(127 + 140 - e) << 23);
    if (lane == 0) red[c] = __uint_as_float((u32)(e - 13) << 23);
#pragma unroll
    for (int i = 0; i < 4; ++i) {
      const int q = lane + 64 * i;
      u32x4 o = {pk_f16(hi16(uv[i].w) * us, lo16(uv[i].w) * us), pk_f16(hi16(uv[i].z) * us, lo16(uv[i].z) * us),
                 pk_f16(hi16(uv[i].y) * us, lo16(uv[i].y) * us), pk_f16(hi16(uv[i].x) * us, lo16(uv[i].x) * us)};
      *(u32x4*)(ur + 2 * (2040 - 8 * q)) = o;
    }
    if (lane < 4) *(u32x4*)(ur + 4096 + 16 * lane) = (u32x4){0, 0, 0, 0};
    __builtin_amdgcn_wave_barrier();
#pragma unroll
    for (int k = 0; k < 5; ++k) {
      const int q = lane + 64 * k;
      if (q < 260) {
        u32x4 a = *(const u32x4*)(ur + 16 * q);
        u32x2 b = *(const u32x2*)(ur + 16 * q + 16);
        u32x4 c1 = {__builtin_amdgcn_alignbit(a.y, a.x, 16), __builtin_amdgcn_alignbit(a.z, a.y, 16), __builtin_amdgcn_alignbit(a.w, a.z, 16), __builtin_amdgcn_alignbit(b.x, a.w, 16)};
        u32x4 c2 = {a.y, a.z, a.w, b.x};
        u32x4 c3 = {c1.y, c1.z, c1.w, __builtin_amdgcn_alignbit(b.y, b.x, 16)};
        *(u32x4*)(ur + 1 * UC_STRIDE + 16 * q) = c1;
        *(u32x4*)(ur + 2 * UC_STRIDE + 16 * q) = c2;
        *(u32x4*)(ur + 3 * UC_STRIDE + 16 * q) = c3;
      }
    }
  }
  __syncthreads();
  TLS(11);
  {
    u16* kc = (u16*)(smem + KC0 + ch * 1312);
    const float* p0 = part + ch * 3 * 648;
#pragma unroll
    for (int i = 0; i < 2; ++i) {
      const int ci = hf * 81 + 64 * i + lane;
      if (lane + 64 * i < 81) {
        f32x4 v = *(const f32x4*)(p0 + ci * 4) + *(const f32x4*)(p0 + 648 + ci * 4) + *(const f32x4*)(p0 + 1296 + ci * 4);
        *(u32x2*)(kc + ci * 4) = (u32x2){pk_f16(v[0] * 128.f, v[1] * 128.f), pk_f16(v[2] * 128.f, v[3] * 128.f)};
      }
    }
  }
  __syncthreads();
  {
    const u16* kc = (const u16*)(smem + KC0 + ch * 1312);
    char* kpl = smem + ch * KPCH;
    if (hf == 1 && lane < 60) *(u32x4*)(kpl + (lane & 3) * KP_STRIDE + (lane >> 2) * 16) = (u32x4){0, 0, 0, 0};
#pragma unroll
    for (int k = 0; k < 2; ++k) {
      const int q = hf + 2 * (lane + 64 * k);
      u32 v[8];
#pragma unroll
      for (int j8 = 0; j8 < 8; ++j8) {
        int j = 8 * q + j8;
        u32 fb = __float_as_uint((float)j); fb += 0x7fffu + ((fb >> 16) & 1u);
        int idx = j < 256 ? j : 256 + (int)((fb >> 16) - 0x4380u);
        v[j8] = kc[idx];
      }
      u32x4 o = {v[0] | (v[1] << 16), v[2] | (v[3] << 16), v[4] | (v[5] << 16), v[6] | (v[7] << 16)};
      *(u32x4*)(kpl + (q & 3) * KP_STRIDE + ((q >> 2) + 15) * 16) = o;
    }
  }
  __syncthreads();
  TLS(12);
  f32x4 acc[4][2];
#pragma unroll
  for (int t = 0; t < 4; ++t) { acc[t][0] = (f32x4){0, 0, 0, 0}; acc[t][1] = (f32x4){0, 0, 0, 0}; }
  const int b = lane & 15, g = lane >> 4;
  char* ureg = smem + UC0 + ch * UCH;
  {
    const int cpy = (3 - b) & 3;
    const char* pa0 = ureg + cpy * UC_STRIDE + 2 * (2044 - 4 * (b >> 2) + 8 * g);
    const char* pb0 = smem + ch * KPCH + g * KP_STRIDE + 16 * (b + 15);
    typedef const volatile LAS u32x2* lp2;
    typedef const volatile LAS u32x4* lp4;
    const u32 abase = (u32)(size_t)(pa0 - 64 * 63 - 32);
    const u32 bbase = (u32)(size_t)(pb0 - 16 * 63);
    constexpr int DP = 2;
    u32x2 Ab[DP + 1][4]; u32x4 Bb[DP + 1][4];
    auto run = [&](auto P0c, auto P1c) {
      constexpr int P0 = decltype(P0c)::value, P1 = decltype(P1c)::value;
      auto issue = [&](int p) {
        const int sl = p % (DP + 1);
        const u32 oa = 64 * (63 - p);
        Ab[sl][0] = *(lp2)(abase + oa + 32); Ab[sl][1] = *(lp2)(abase + oa + 40);
        Ab[sl][2] = *(lp2)(abase + oa);      Ab[sl][3] = *(lp2)(abase + oa + 8);
#pragma unroll
        for (int t = 0; t < 4; ++t) if (t >= (p >> 4)) Bb[sl][t] = *(lp4)(bbase + 16 * (63 - p) + 256 * t);
      };
#pragma unroll
      for (int p = P0; p < P0 + DP; ++p) issue(p);
#pragma unroll
      for (int p = P0; p < P1; ++p) {
        if (p + DP < P1) issue(p + DP);
        __builtin_amdgcn_sched_barrier(0);
        const int sl = p % (DP + 1);
        h8 A0 = __builtin_bit_cast(h8, (u32x4){Ab[sl][0].x, Ab[sl][0].y, Ab[sl][1].x, Ab[sl][1].y});
        h8 A1 = __builtin_bit_cast(h8, (u32x4){Ab[sl][2].x, Ab[sl][2].y, Ab[sl][3].x, Ab[sl][3].y});
#pragma unroll
        for (int t = 0; t < 4; ++t) if (t >= (p >> 4)) {
          h8 B = __builtin_bit_cast(h8, Bb[sl][t]);
          acc[t][0] = __builtin_amdgcn_mfma_f32_16x16x32_f16(A0, B, acc[t][0], 0, 0, 0);
          acc[t][1] = __builtin_amdgcn_mfma_f32_16x16x32_f16(A1, B, acc[t][1], 0, 0, 0);
        }
        __builtin_amdgcn_sched_barrier(0);
      }
    };
    if (hf == 0) run(std::integral_constant<int, 0>{}, std::integral_constant<int, PSPLIT>{});
    else run(std::integral_constant<int, PSPLIT>{}, std::integral_constant<int, 64>{});
  }
  TLS(13);
  f32x4* xch = (f32x4*)(smem + PART0 + ch * 6144) + lane;
  if (hf == 1) {
#pragma unroll
    for (int t = 1; t < 4; ++t) { xch[(2 * (t - 1)) * 64] = acc[t][0]; xch[(2 * (t - 1) + 1) * 64] = acc[t][1]; }
  }
  __syncthreads();
  if (hf == 0) {
    const float ysc = red[8 + ch] * red[ch];
    char* yb = smem + ch * 4096;
#pragma unroll
    for (int t = 0; t < 4; ++t)
#pragma unroll
      for (int r = 0; r < 2; ++r) {
        f32x4 a = acc[t][r];
        if (t > 0) a += xch[(2 * (t - 1) + r) * 64];
        u32x2 o = {pk_bf16(a[0] * ysc, a[1] * ysc), pk_bf16(a[2] * ysc, a[3] * ysc)};
        *(u32x2*)(yb + 2 * (32 * (16 * t + b) + 16 * r + 4 * g)) = o;
      }
  }
  __syncthreads();
  {
    u32x2 yv[4], uu[4]; float iu[4], dk[4];
#pragma unroll
    for (int c = 0; c < 4; ++c) {
      yv[c] = *(const u32x2*)(smem + c * 4096 + 8 * tid);
      uu[c] = *(const u32x2*)(smem + UC0 + c * UCH + 2 * (2044 - 4 * tid));
      iu[c] = red[c]; dk[c] = bf2f(dsk_[d0 + c]);
    }
#pragma unroll
    for (int i = 0; i < 4; ++i) {
      float r[4];
#pragma unroll
      for (int c = 0; c < 4; ++c) {
        u32 yw = yv[c][i >> 1]; float y = (i & 1) ? hi16(yw) : lo16(yw);
        const u32 uw = (i < 2) ? uu[c].y : uu[c].x; const u16 ub = (i & 1) ? (u16)uw : (u16)(uw >> 16); float uf = (float)__builtin_bit_cast(_Float16, ub) * iu[c];
        r[c] = y + rbf(uf * dk[c]);
      }
      u32x2 o = {pk_bf16(r[0], r[1]), pk_bf16(r[2], r[3])};
      *(u32x2*)(out + (size_t)(4 * tid + i) * 1280 + 640 + d0) = o;
    }
  }
  __syncthreads();
}

constexpr int OSTAGE = 20480, ONBUF = 4, O_OFF_W = 81920;
DI void o_block(char* __restrict__ ws, rsrc_t rs, u16* __restrict__ out, int rt, int ct, char* smem, bool& initdone) {
  const int tid = threadIdx.x, lane = tid & 63, wv = __builtin_amdgcn_readfirstlane(tid >> 6);
  const int row0 = rt * 64, fr = lane & 15, g = lane >> 4;
  const int lw = wv - 4;
  if (tid == 0) { wait_init_t0(ws, initdone); wait_cnt_t0(ws, CNT_T + (blockIdx.x & 7), 64); }
  acquire_all();
  const char* wsrc = ws + WS_WOP + (size_t)ct * 10 * OSTAGE + lw * 5120 + lane * 16;
  auto stage = [&](int kt) {
    char* b = smem + O_OFF_W + (kt & (ONBUF - 1)) * OSTAGE + lw * 5120;
#pragma unroll
    for (int i = 0; i < 5; ++i) __builtin_amdgcn_global_load_lds((const u32*)(wsrc + kt * OSTAGE + i * 1024), (LAS u32*)(b + i * 1024), 16, 0, 0);
  };
  if (wv >= 4) {
#pragma unroll 1
    for (int i = 0; i < ONBUF; ++i) stage(i);
  }
  if (tid == 0) wait_cnt_t0(ws, CNT_PQ + (rt >> 1), 4);
  asm volatile("s_waitcnt lgkmcnt(0)" ::: "memory");
  __builtin_amdgcn_s_barrier();
  const char* pq = ws + WS_PQ;
  const char* zp = ws + WS_ZERO + CNT_OFF + ZPAD * 128;
  u32x4 kr[4][2][2]; u32x2 pa[4][4], pb[4][4];
#pragma unroll
  for (int hh = 0; hh < 4; ++hh)
#pragma unroll
    for (int st = 0; st < 4; ++st) {
      const char* p = pq + 2 * (((size_t)((rt * 4 + st) * 32 + 4 * wv + hh) * 5) * 64 + fr * 4);
      pa[hh][st] = ld8_sc(g < 3 ? p + 2 * g * 128 : zp);
      pb[hh][st] = ld8_sc(g < 2 ? p + (2 * g + 1) * 128 : zp);
    }
  if (tid == 0) wait_cnt_t0(ws, CNT_KV + (blockIdx.x & 7), 128);
  TLS(6);
  asm volatile("s_waitcnt lgkmcnt(0)" ::: "memory");
  __builtin_amdgcn_s_barrier();
  TLS(7);
#pragma unroll
  for (int hh = 0; hh < 4; ++hh)
#pragma unroll
    for (int tl = 0; tl < 2; ++tl) {
      kr[hh][tl][0] = ld16_sc(rs, WS_ZERO + (((4 * wv + hh) * 2 + tl) * 64 + lane) * 32);
      kr[hh][tl][1] = ld16_sc(rs, WS_ZERO + (((4 * wv + hh) * 2 + tl) * 64 + lane) * 32 + 16);
    }
  __builtin_amdgcn_sched_barrier(0);
  {
    s16x8 kf[4][2];
#pragma unroll
    for (int hh = 0; hh < 4; ++hh)
#pragma unroll
      for (int tl = 0; tl < 2; ++tl) {
        f32x4 a = __builtin_bit_cast(f32x4, kr[hh][tl][0]), b = __builtin_bit_cast(f32x4, kr[hh][tl][1]);
        kf[hh][tl] = __builtin_bit_cast(s16x8, (u32x4){pk_bf16(a[0], a[1]), pk_bf16(a[2], a[3]), pk_bf16(b[0], b[1]), pk_bf16(b[2], b[3])});
      }
    asm volatile("" :: "v"(kf[3][1]), "v"(pa[3][3]), "v"(pb[3][3]));
    TLS(10);
    const int G = wv >> 1, half = wv & 1;
#pragma unroll
    for (int st = 0; st < 4; ++st) {
      float vals[20];
#pragma unroll
      for (int hh = 0; hh < 4; ++hh) {
        s16x8 pf = __builtin_bit_cast(s16x8, (u32x4){pa[hh][st].x, pa[hh][st].y, pb[hh][st].x, pb[hh][st].y});
        f32x4 z4 = {0, 0, 0, 0};
        f32x4 t0 = __builtin_amdgcn_mfma_f32_16x16x32_bf16(kf[hh][0], pf, z4, 0, 0, 0);
        f32x4 t1 = __builtin_amdgcn_mfma_f32_16x16x32_bf16(kf[hh][1], pf, z4, 0, 0, 0);
        float den = rbf(rbf(t0[3]) + 1e-6f);
        float rd = __builtin_amdgcn_rcpf(den);
        vals[5 * hh + 0] = t0[0] * rd; vals[5 * hh + 1] = t0[1] * rd; vals[5 * hh + 2] = t0[2] * rd;
        vals[5 * hh + 3] = t1[0] * rd; vals[5 * hh + 4] = t1[1] * rd;
      }
#pragma unroll
      for (int i = 0; i < 5; ++i) {
        const int byte = 40 * half + 8 * i;
        const int T = 5 * G + (byte >> 4);
        *(u32x2*)(smem + (st * 20 + T) * 1024 + lane * 16 + (byte & 8)) = (u32x2){pk_bf16(vals[4 * i], vals[4 * i + 1]), pk_bf16(vals[4 * i + 2], vals[4 * i + 3])};
      }
    }
  }
  if (wv >= 4) {
    asm volatile("s_waitcnt vmcnt(0) lgkmcnt(0)" ::: "memory");
    __builtin_amdgcn_s_barrier();
#pragma unroll 1
    for (int kt = 0; kt < 6; ++kt) {
      asm volatile("s_waitcnt vmcnt(10)" ::: "memory");
      __builtin_amdgcn_s_barrier();
      stage(kt + ONBUF);
    }
    asm volatile("s_waitcnt vmcnt(10)" ::: "memory"); __builtin_amdgcn_s_barrier();
    asm volatile("s_waitcnt vmcnt(5)" ::: "memory");  __builtin_amdgcn_s_barrier();
    asm volatile("s_waitcnt vmcnt(0)" ::: "memory");  __builtin_amdgcn_s_barrier();
    return;
  }
  const int wm = wv >> 1, wn = wv & 1;
  f32x4 acc[2][5];
#pragma unroll
  for (int m = 0; m < 2; ++m)
#pragma unroll
    for (int n = 0; n < 5; ++n) acc[m][n] = (f32x4){0, 0, 0, 0};
  const u32 sbase = (u32)(size_t)smem;
  const u32 boff0 = O_OFF_W + g * 2560 + wn * 1280 + ((0 ^ (4 * (g & 1))) + (fr >> 2)) * 32 + (lane & 3) * 8;
  const u32 boff1 = O_OFF_W + g * 2560 + wn * 1280 + ((4 ^ (4 * (g & 1))) + (fr >> 2)) * 32 + (lane & 3) * 8;
  u32 aad = sbase + wm * 2 * 20 * 1024 + lane * 16;
  u32 b0 = sbase + boff0, b1 = sbase + boff1;
  s16x8 af[2][2]; s16x4 bl[2][5], bh[2][5];
  auto ldop = [&](int ks, int i) {
    if (i < 10) {
      const int n = i >> 1;
      if (i & 1) bh[ks][n] = __builtin_amdgcn_ds_read_tr16_b64_v4i16((LAS s16x4*)(b1 + ks * 10240 + n * 256));
      else       bl[ks][n] = __builtin_amdgcn_ds_read_tr16_b64_v4i16((LAS s16x4*)(b0 + ks * 10240 + n * 256));
    } else af[ks][i - 10] = *(const LAS s16x8*)(aad + ((i - 10) * 20 + ks) * 1024);
  };
  auto mma1 = [&](int sl, int i) {
    const int m = i / 5, n = i % 5;
    s16x8 bb = __builtin_shufflevector(bl[sl][n], bh[sl][n], 0, 1, 2, 3, 4, 5, 6, 7);
    acc[m][n] = __builtin_amdgcn_mfma_f32_16x16x32_bf16(bb, af[sl][m], acc[m][n], 0, 0, 0);
  };
  __builtin_amdgcn_s_waitcnt(0xC07F);
  __builtin_amdgcn_s_barrier();
  TLS(9);
#pragma unroll
  for (int i = 0; i < 12; ++i) ldop(0, i);
  int cur = 0;
#pragma unroll 1
  for (int kt = 0; kt < 10; ++kt) {
    __builtin_amdgcn_sched_barrier(0);
#pragma unroll
    for (int i = 0; i < 10; ++i) {
      mma1(0, i);
      ldop(1, i); if (i < 2) ldop(1, 10 + i);
      __builtin_amdgcn_sched_barrier(0);
    }
    cur = (cur + 1) & (ONBUF - 1);
    b0 = sbase + cur * OSTAGE + boff0; b1 = sbase + cur * OSTAGE + boff1;
    aad += (kt < 9) ? 2048 : 0;
    __builtin_amdgcn_s_waitcnt(0xC07F);
    if (kt < 9) __builtin_amdgcn_s_barrier();
    __builtin_amdgcn_sched_barrier(0);
#pragma unroll
    for (int i = 0; i < 10; ++i) {
      mma1(1, i);
      ldop(0, i); if (i < 2) ldop(0, 10 + i);
      __builtin_amdgcn_sched_barrier(0);
    }
  }
#pragma unroll
  for (int m = 0; m < 2; ++m)
#pragma unroll
    for (int n = 0; n < 5; ++n) {
      u32x2 o = {pk_bf16(acc[m][n][0], acc[m][n][1]), pk_bf16(acc[m][n][2], acc[m][n][3])};
      *(u32x2*)(out + (size_t)(row0 + wm * 32 + m * 16 + fr) * 1280 + 160 * ct + wn * 80 + n * 16 + 4 * g) = o;
    }
}

struct P { const u16 *x, *w, *wo, *u, *lar, *aim, *cr, *ci, *ldt, *dsk; u16* out; char* ws; };
__global__ __launch_bounds__(512, 2) void kmain(P p) {
  extern __shared__ __attribute__((aligned(16))) char smem[];
  const int b = blockIdx.x, tid = threadIdx.x;
  char* ws = p.ws;
  const rsrc_t rs = __builtin_amdgcn_make_buffer_rsrc((void*)ws, 0, WS_SPAN, 0x00020000);
  bool initdone = false;
  TLS(0);
  if (b >= 248) {
    const int part = b - 248;
    for (int i = tid; i < ZERO_BYTES / 128; i += 512) st16_wt(rs, WS_ZERO + part * (ZERO_BYTES / 8) + i * 16, (u32x4){0, 0, 0, 0});
    asm volatile("s_waitcnt vmcnt(0)" ::: "memory");
    __syncthreads();
    if (tid < 256) __hip_atomic_store((gu64*)(ws + WS_FLAG) + tid * 8 + part, MAGIC, RLX_AGENT);
  }
  const KGP kp = {p.lar, p.aim, p.cr, p.ci, p.ldt};
  int bt0 = -1, bt1 = -1, o_task = -1;
  if (b < 192) {
    const int xcd = b & 7, j = b >> 3;
    const int rt = (xcd >> 1) * 4 + (j & 3), c6 = j >> 2;
    if (c6 < 4) { q_block<true>(p.x, p.w, ws, rs, rt, (xcd & 1) * 4 + c6, smem, initdone); o_task = xcd * 16 + j; }
    else { q_block<false>(p.x, p.w, ws, rs, rt, (xcd & 1) * 2 + (c6 - 4), smem, initdone); bt0 = xcd * 20 + 12 + (j - 16); }
  } else {
    const int ab = b - 192;
    aux_block(p.u, p.wo, ws, rs, ab, smem, initdone);
    { const int xcd = ab & 7, i = ab >> 3; bt0 = xcd * 20 + i; if (i < 4) bt1 = xcd * 20 + 8 + i; }
  }
  TLS(2);
  __syncthreads();
#pragma unroll 1
  for (int it = 0; it < 2; ++it) {
    const int t = it == 0 ? bt0 : bt1;
    if (t >= 0) b_block(kp, ws, rs, p.dsk, p.out, t, smem, initdone);
  }
  TLS(5);
  if (o_task >= 0) o_block(ws, rs, p.out, o_task >> 2, o_task & 3, smem, initdone);
  TLS(8);
}

extern "C" void kernel_launch(void* const* d_in, const int* in_sizes, int n_in, void* d_out, int out_size, void* d_ws, size_t ws_size, hipStream_t stream) {
  constexpr int LDS = O_OFF_W + ONBUF * OSTAGE;
  static_assert(QNBUF * QSTAGE <= LDS && 8 * KG_REG <= LDS && B_LDS <= LDS, "LDS");
  (void)hipFuncSetAttribute((const void*)kmain, hipFuncAttributeMaxDynamicSharedMemorySize, LDS);
  P p = {(const u16*)d_in[0], (const u16*)d_in[1], (const u16*)d_in[2], (const u16*)d_in[3], (const u16*)d_in[4], (const u16*)d_in[5], (const u16*)d_in[6], (const u16*)d_in[7], (const u16*)d_in[8], (const u16*)d_in[9], (u16*)d_out, (char*)d_ws};
  kmain<<<256, 512, LDS, stream>>>(p);
}
```

```cpp
#include <hip/hip_runtime.h>
#include <hip/hip_bf16.h>
#include <stdint.h>
#include <type_traits>
typedef unsigned short u16;
typedef unsigned int u32;
typedef float f32x2 __attribute__((ext_vector_type(2)));
typedef float f32x4 __attribute__((ext_vector_type(4)));
typedef float f32x16 __attribute__((ext_vector_type(16)));
typedef short s16x4 __attribute__((ext_vector_type(4)));
typedef short s16x8 __attribute__((ext_vector_type(8)));
typedef _Float16 h8 __attribute__((ext_vector_type(8)));
typedef _Float16 h2 __attribute__((ext_vector_type(2)));
typedef __bf16 bf16x2 __attribute__((ext_vector_type(2)));
typedef u32 u32x2 __attribute__((ext_vector_type(2)));
typedef u32 u32x4 __attribute__((ext_vector_type(4)));
#define DI __device__ __forceinline__
#define LAS __attribute__((address_space(3)))
#define GAS __attribute__((address_space(1)))
typedef GAS unsigned gu32; typedef GAS unsigned long long gu64;
#define RLX_AGENT __ATOMIC_RELAXED, __HIP_MEMORY_SCOPE_AGENT
DI float bf2f(u16 x) { return __uint_as_float(((u32)x) << 16); }
DI u32 pk_bf16(float a, float b) { bf16x2 r = __builtin_convertvector((f32x2){a, b}, bf16x2); return __builtin_bit_cast(u32, r); }
DI u32 pk_f16(float a, float b) { h2 r = __builtin_convertvector((f32x2){a, b}, h2); return __builtin_bit_cast(u32, r); }
DI float lo16(u32 x) { return __uint_as_float(x << 16); }
DI float hi16(u32 x) { return __uint_as_float(x & 0xffff0000u); }
DI float rbf(float f) { return lo16(pk_bf16(f, 0.f)); }
DI u32 phi_pair(float a, float b) {
  u32 t = pk_bf16(a, b);
  float ta = lo16(t), tb = hi16(t);
  float ea = __builtin_amdgcn_fmed3f(__builtin_amdgcn_exp2f(ta * 1.4426950408889634f), 0.f, 1.f);
  float eb = __builtin_amdgcn_fmed3f(__builtin_amdgcn_exp2f(tb * 1.4426950408889634f), 0.f, 1.f);
  return pk_bf16(ea + fmaxf(ta, 1e-6f), eb + fmaxf(tb, 1e-6f));
}
DI f32x2 cmul(f32x2 x, f32x2 y) {
  f32x2 t, z;
  asm("v_pk_mul_f32 %0, %1, %2 op_sel_hi:[0,1]" : "=v"(t) : "v"(x), "v"(y));
  asm("v_pk_fma_f32 %0, %1, %2, %3 op_sel:[1,1,0] op_sel_hi:[1,0,1] neg_lo:[0,1,0]" : "=v"(z) : "v"(x), "v"(y), "v"(t));
  return z;
}
DI f32x2 cexpP(float ar2, float airev, float P) {
  float e = __builtin_amdgcn_exp2f(ar2 * P);
  float t = __builtin_amdgcn_fractf(airev * P);
  float s = __builtin_amdgcn_sinf(t), c = __builtin_amdgcn_cosf(t);
  f32x2 r = {e * c, e * s};
  return r;
}
constexpr int S = 2048, D = 640, NS = 192;
constexpr u32 WS_PQ = 0;
constexpr u32 WS_UT = 2621440;
constexpr u32 WS_WOP = 2 * 2621440;
constexpr u32 WS_FLAG = 2 * 2621440 + 819200;
constexpr u32 WS_ZERO = WS_FLAG + 16384;
constexpr int KVACC_BYTES = 32 * 1024 * 4;
constexpr int CNT_OFF = KVACC_BYTES;
constexpr int CNT_KV = 0, CNT_T = 8, CNT_PQ = 16, CNT_KG = 32, ZPAD = 40;
constexpr int ZERO_BYTES = KVACC_BYTES + 48 * 128;
constexpr u32 WS_SPAN = 7u << 20;
constexpr unsigned long long MAGIC = 0x9e3779b97f4a7c15ull;
constexpr unsigned SPIN_CAP = 1u << 17;
typedef __amdgpu_buffer_rsrc_t rsrc_t;

DI gu32* cnt_ptr(char* ws, int i) { return (gu32*)(ws + WS_ZERO + CNT_OFF + i * 128); }
DI void wait_init_t0(char* ws, bool& done) {
  if (done) return;
  gu64* f = (gu64*)(ws + WS_FLAG) + blockIdx.x * 8;
  for (unsigned sp = 0; sp < SPIN_CAP; ++sp) {
    bool ok = true;
#pragma unroll
    for (int i = 0; i < 8; ++i) ok &= __hip_atomic_load(f + i, RLX_AGENT) == MAGIC;
    if (ok) break;
    __builtin_amdgcn_s_sleep(2);
  }
#pragma unroll
  for (int i = 0; i < 8; ++i) __hip_atomic_store(f + i, 0ull, RLX_AGENT);
  done = true;
}
DI void wait_cnt_t0(char* ws, int i, unsigned want) {
  gu32* c = cnt_ptr(ws, i);
  if (__hip_atomic_load(c, RLX_AGENT) >= want) return;
  unsigned v0, v1, v2, v3;
#define PLD(v) asm volatile("global_load_dword %0, %1, off sc1" : "=v"(v) : "v"(c) : "memory")
  PLD(v0); __builtin_amdgcn_s_sleep(2); PLD(v1); __builtin_amdgcn_s_sleep(2); PLD(v2); __builtin_amdgcn_s_sleep(2);
  for (unsigned sp = 0; sp < SPIN_CAP; ++sp) {
    PLD(v3); asm volatile("s_waitcnt vmcnt(3)" : "+v"(v0) :: "memory"); if (v0 >= want) break; __builtin_amdgcn_s_sleep(2);
    PLD(v0); asm volatile("s_waitcnt vmcnt(3)" : "+v"(v1) :: "memory"); if (v1 >= want) break; __builtin_amdgcn_s_sleep(2);
    PLD(v1); asm volatile("s_waitcnt vmcnt(3)" : "+v"(v2) :: "memory"); if (v2 >= want) break; __builtin_amdgcn_s_sleep(2);
    PLD(v2); asm volatile("s_waitcnt vmcnt(3)" : "+v"(v3) :: "memory"); if (v3 >= want) break; __builtin_amdgcn_s_sleep(2);
  }
  asm volatile("s_waitcnt vmcnt(0)" : "+v"(v0), "+v"(v1), "+v"(v2), "+v"(v3) :: "memory");
#undef PLD
}
DI void publish(char* ws, int i, bool& initdone) {
  asm volatile("s_waitcnt vmcnt(0)" ::: "memory");
  __syncthreads();
  if (threadIdx.x == 0) { wait_init_t0(ws, initdone); __hip_atomic_fetch_add(cnt_ptr(ws, i), 1u, RLX_AGENT); }
}
DI void publish8(char* ws, int i, bool& initdone) {
  if (threadIdx.x == 0) wait_init_t0(ws, initdone);
  asm volatile("s_waitcnt vmcnt(0)" ::: "memory");
  __syncthreads();
  if (threadIdx.x < 8) __hip_atomic_fetch_add(cnt_ptr(ws, i + threadIdx.x), 1u, RLX_AGENT);
}
DI u32x4 ld16_sc(rsrc_t r, u32 off) { return __builtin_amdgcn_raw_buffer_load_b128(r, off, 0, 16); }
DI u32x2 ld8_sc(const void* p) { unsigned long long v = __hip_atomic_load((gu64*)p, RLX_AGENT); return (u32x2){(u32)v, (u32)(v >> 32)}; }
DI float ld4_sc(const void* p) { return __uint_as_float(__hip_atomic_load((gu32*)p, RLX_AGENT)); }
DI void acquire_all() {
  if (threadIdx.x == 0) { __builtin_amdgcn_fence(__ATOMIC_ACQUIRE, "agent"); asm volatile("s_waitcnt vmcnt(0)" ::: "memory"); }
  __syncthreads();
}
DI void st16_wt(rsrc_t r, u32 off, u32x4 v) { __builtin_amdgcn_raw_buffer_store_b128(v, r, off, 0, 16); }
DI void st8_wt(void* p, u32x2 v) { __hip_atomic_store((gu64*)p, ((unsigned long long)v.y << 32) | v.x, RLX_AGENT); }

#define TLS(i)
constexpr int QSTAGE = 36864, QNBUF = 4;
constexpr int TROW = 272, KG_REG = 14720;

DI f32x2 cm(f32x2 x, f32x2 y) {
  f32x2 xx = {x.x, x.x}, xy = {x.y, x.y}, ys = {-y.y, y.x};
  return __builtin_elementwise_fma(xy, ys, xx * y);
}
DI f32x2 cmc(f32x2 x, f32x2 y) {
  f32x2 xx = {x.x, x.x}, xy = {x.y, x.y}, yc = {y.x, -y.y}, ys = {y.y, y.x};
  return __builtin_elementwise_fma(xy, ys, xx * yc);
}
DI void kgen_chunk(float a2, float av, f32x2 Ein, char* reg, f32x16& kacc) {
  const int lane = threadIdx.x & 63;
  u32* tE = (u32*)(reg) + lane;
  u32* tF = (u32*)(reg + 21 * TROW) + lane;
  constexpr int R = TROW / 4;
  auto stE = [&](int r, f32x2 v) { tE[r * R] = pk_f16(v.x, v.y); };
  auto stF = [&](int r, f32x2 v) { tF[r * R] = pk_f16(v.x, v.y); };
  f32x2 w1 = cexpP(a2, av, 1.f), v1 = cexpP(a2, av, 8.f), B256 = cexpP(a2, av, 256.f);
  f32x2 w2 = cm(w1, w1), w4 = cm(w2, w2);
  f32x2 v2 = cm(v1, v1), v4 = cm(v2, v2), v8 = cm(v4, v4), v16 = cm(v8, v8);
  f32x2 B512 = cm(B256, B256), B1024 = cm(B512, B512), B768 = cm(B512, B256);
  f32x2 E0 = {Ein.x, -Ein.y};
  f32x2 E1 = cmc(E0, w1), E2 = cmc(E0, w2), E3 = cmc(E1, w2), E4 = cmc(E0, w4), E5 = cmc(E1, w4), E6 = cmc(E2, w4), E7 = cmc(E3, w4);
  stE(0, E0); stE(1, E1); stE(2, E2); stE(3, E3); stE(4, E4); stE(5, E5); stE(6, E6); stE(7, E7);
  stE(8, cmc(E0, B256)); stE(9, cmc(E2, B256)); stE(10, cmc(E4, B256)); stE(11, cmc(E6, B256));
  stE(12, cmc(E0, B512)); stE(13, cmc(E4, B512)); stE(14, cmc(E0, B768)); stE(15, cmc(E4, B768));
  f32x2 G0 = cmc(E0, B1024);
  stE(16, G0); stE(17, cmc(G0, B256)); stE(18, cmc(G0, B512)); stE(19, cmc(G0, B768)); stE(20, cmc(G0, B1024));
  f32x2 F[8];
  F[0].x = 1.f; F[0].y = 0.f; F[1] = v1; F[2] = v2; F[3] = cm(v2, v1); F[4] = v4; F[5] = cm(v4, v1); F[6] = cm(v4, v2); F[7] = cm(v4, F[3]);
  f32x2 v24 = cm(v16, v8);
#pragma unroll
  for (int i = 0; i < 8; ++i) {
    stF(i, F[i]);
    stF(8 + i, i == 0 ? v8 : cm(F[i], v8));
    stF(16 + i, i == 0 ? v16 : cm(F[i], v16));
    stF(24 + i, i == 0 ? v24 : cm(F[i], v24));
  }
  __builtin_amdgcn_wave_barrier();
  const int r = lane & 31, hh = lane >> 5;
  const char* pa = reg + (r < 21 ? r : 20) * TROW + 16 * hh;
  const char* pb = reg + (21 + r) * TROW + 16 * hh;
#pragma unroll
  for (int s = 0; s < 8; ++s) {
    h8 a = *(const h8*)(pa + 32 * s);
    h8 b = *(const h8*)(pb + 32 * s);
    kacc = __builtin_amdgcn_mfma_f32_32x32x16_f16(a, b, kacc, 0, 0, 0);
  }
  __builtin_amdgcn_wave_barrier();
}

struct KGP { const u16 *lar, *aim, *cr, *ci, *ldt; };
struct KGL { u16 pcr[3], pci[3], plar, paim, pldt; };
DI void kgen_load(const KGP& p, int d, int c, KGL& l) {
  const int lane = threadIdx.x & 63;
#pragma unroll
  for (int q = 0; q < 3; ++q) { int n = d * NS + 64 * q + lane; l.pcr[q] = p.cr[n]; l.pci[q] = p.ci[n]; }
  l.plar = p.lar[d * NS + 64 * c + lane]; l.paim = p.aim[d * NS + 64 * c + lane];
  l.pldt = p.ldt[d];
}
DI float kgen_task(const KGL& l, int c, char* reg, float* dst) {
  const int lane = threadIdx.x & 63;
  const float dt = rbf(expf(bf2f(l.pldt)));
  float cmax = 0.f;
#pragma unroll
  for (int q = 0; q < 3; ++q) cmax = fmaxf(cmax, fmaxf(fabsf(bf2f(l.pcr[q])), fabsf(bf2f(l.pci[q]))));
  cmax *= dt * 2.f;
#pragma unroll
  for (int o = 32; o >= 1; o >>= 1) cmax = fmaxf(cmax, __shfl_xor(cmax, o));
  int ce = (int)((__float_as_uint(cmax) >> 23) & 0xff);
  ce = ce < 30 ? 30 : (ce > 220 ? 220 : ce);
  const float sc = __uint_as_float((u32)(127 + 125 - ce) << 23);
  const float isc = __uint_as_float((u32)(ce + 2) << 23);
  const float Are = -rbf(expf(bf2f(l.plar)));
  const float ar2 = Are * dt * 1.4426950408889634f, airev = bf2f(l.paim) * dt * 0.15915494309189535f;
  const u16 mcr = c == 0 ? l.pcr[0] : (c == 1 ? l.pcr[1] : l.pcr[2]), mci = c == 0 ? l.pci[0] : (c == 1 ? l.pci[1] : l.pci[2]);
  f32x2 E = {bf2f(mcr) * dt * 2.f * sc, bf2f(mci) * dt * 2.f * sc};
  f32x16 kacc;
#pragma unroll
  for (int i = 0; i < 16; ++i) kacc[i] = 0.f;
  kgen_chunk(ar2, airev, E, reg, kacc);
  const int i = lane & 31, hh = lane >> 5;
#pragma unroll
  for (int rg = 0; rg < 12; ++rg) {
    const int q = rg >> 2, sub = rg & 3;
    int idx; bool ok = true;
    if (q == 0) idx = 8 * i + 4 * hh + sub;
    else if (q == 1) idx = hh == 0 ? 256 + 4 * i + sub : 384 + 64 * (sub >> 1) + 2 * i + (sub & 1);
    else { idx = hh == 0 ? 512 + 32 * sub + i : 640; ok = (hh == 0) || (sub == 0 && i == 0); }
    if (ok) dst[idx] = kacc[rg];
  }
  return isc * (1.f / 128.f);
}

template <bool KV>
DI void q_block(const u16* __restrict__ x, const u16* __restrict__ w, char* __restrict__ ws, rsrc_t rs, int rt, int ct, char* smem, bool& initdone) {
  const int tid = threadIdx.x, lane = tid & 63, wv = __builtin_amdgcn_readfirstlane(tid >> 6);
  const int row0 = rt * 128;
  const int fr = lane & 15, g = lane >> 4;
  if (wv >= 4) {
    const int lw = wv - 4;
    const u16* xs[4]; const u16* ws_[5];
#pragma unroll
    for (int i = 0; i < 4; ++i) { int pc = lw * 4 + i; int row = 8 * pc + (lane >> 3); int ch = (lane & 7) ^ ((row >> 1) & 7); xs[i] = x + (size_t)(row0 + row) * D + 8 * ch; }
#pragma unroll
    for (int i = 0; i < 5; ++i) {
      int pc = lw * 5 + i; int sb = 4 * pc + (lane >> 4); int kb = sb / 10, nb = sb % 10; int row = ((lane >> 1) & 7) ^ (4 * (kb & 1)); int c = nb * 16 + 8 * (lane & 1);
      int gc;
      if (KV) { int wn2 = c / 80, cc = c % 80; gc = cc < 40 ? 640 + 80 * ct + 40 * wn2 + cc : 1280 + 80 * ct + 40 * wn2 + (cc - 40); }
      else gc = 160 * ct + c;
      ws_[i] = w + (size_t)(8 * kb + row) * 1920 + gc;
    }
    auto stage = [&](int kt) {
      char* b = smem + (kt & (QNBUF - 1)) * QSTAGE;
#pragma unroll
      for (int i = 0; i < 4; ++i) __builtin_amdgcn_global_load_lds((const u32*)(xs[i] + kt * 64), (LAS u32*)(b + (lw * 4 + i) * 1024), 16, 0, 0);
#pragma unroll
      for (int i = 0; i < 5; ++i) __builtin_amdgcn_global_load_lds((const u32*)(ws_[i] + (size_t)kt * 64 * 1920), (LAS u32*)(b + 16384 + (lw * 5 + i) * 1024), 16, 0, 0);
    };
#pragma unroll 1
    for (int i = 0; i < QNBUF; ++i) stage(i);
    asm volatile("s_waitcnt vmcnt(%0)" :: "n"((QNBUF - 1) * 9) : "memory");
    __builtin_amdgcn_s_barrier();
#pragma unroll 1
    for (int kt = 0; kt < 6; ++kt) {
      asm volatile("s_waitcnt vmcnt(18)" ::: "memory");
      __builtin_amdgcn_s_barrier();
      stage(kt + QNBUF);
    }
    asm volatile("s_waitcnt vmcnt(18)" ::: "memory"); __builtin_amdgcn_s_barrier();
    asm volatile("s_waitcnt vmcnt(9)" ::: "memory");  __builtin_amdgcn_s_barrier();
    asm volatile("s_waitcnt vmcnt(0)" ::: "memory");  __builtin_amdgcn_s_barrier();
    if (KV) { __builtin_amdgcn_s_barrier(); __builtin_amdgcn_s_barrier(); }
  } else {
    const int wm = wv >> 1, wn = wv & 1;
    f32x4 acc[4][5];
#pragma unroll
    for (int m = 0; m < 4; ++m)
#pragma unroll
      for (int n = 0; n < 5; ++n) acc[m][n] = (f32x4){0, 0, 0, 0};
    const u32 aoff0 = (wm * 64 + fr) * 128 + (((0 + g) ^ (fr >> 1)) * 16);
    const u32 aoff1 = (wm * 64 + fr) * 128 + (((4 + g) ^ (fr >> 1)) * 16);
    const u32 boff0 = 16384 + g * 2560 + wn * 1280 + ((0 ^ (4 * (g & 1))) + (fr >> 2)) * 32 + (lane & 3) * 8;
    const u32 boff1 = 16384 + g * 2560 + wn * 1280 + ((4 ^ (4 * (g & 1))) + (fr >> 2)) * 32 + (lane & 3) * 8;
    const u32 sbase = (u32)(size_t)smem;
    s16x8 af[2][4]; s16x4 bl[2][5], bh[2][5];
    u32 a0 = sbase + aoff0, a1 = sbase + aoff1, b0 = sbase + boff0, b1 = sbase + boff1;
    auto ldop = [&](int ks, int i) {
      if (i < 10) {
        const int n = i >> 1;
        if (i & 1) bh[ks][n] = __builtin_amdgcn_ds_read_tr16_b64_v4i16((LAS s16x4*)(b1 + ks * 10240 + n * 256));
        else       bl[ks][n] = __builtin_amdgcn_ds_read_tr16_b64_v4i16((LAS s16x4*)(b0 + ks * 10240 + n * 256));
      } else af[ks][i - 10] = *(const LAS s16x8*)((ks ? a1 : a0) + (i - 10) * 2048);
    };
    auto mma1 = [&](int sl, int i) {
      const int m = i / 5, n = i % 5;
      s16x8 bb = __builtin_shufflevector(bl[sl][n], bh[sl][n], 0, 1, 2, 3, 4, 5, 6, 7);
      acc[m][n] = KV ? __builtin_amdgcn_mfma_f32_16x16x32_bf16(af[sl][m], bb, acc[m][n], 0, 0, 0)
                     : __builtin_amdgcn_mfma_f32_16x16x32_bf16(bb, af[sl][m], acc[m][n], 0, 0, 0);
    };
    __builtin_amdgcn_s_barrier();
    TLS(15);
#pragma unroll
    for (int i = 0; i < 14; ++i) ldop(0, i);
    int cur = 0;
#pragma unroll 1
    for (int kt = 0; kt < 10; ++kt) {
      __builtin_amdgcn_sched_barrier(0);
#pragma unroll
      for (int i = 0; i < 20; ++i) {
        mma1(0, i);
        if (i < 14) ldop(1, i);
        __builtin_amdgcn_sched_barrier(0);
      }
      cur = (cur + 1) & (QNBUF - 1);
      const u32 nb = sbase + cur * QSTAGE;
      a0 = nb + aoff0; a1 = nb + aoff1; b0 = nb + boff0; b1 = nb + boff1;
      __builtin_amdgcn_s_waitcnt(0xC07F);
      if (kt < 9) __builtin_amdgcn_s_barrier();
      __builtin_amdgcn_sched_barrier(0);
#pragma unroll
      for (int i = 0; i < 20; ++i) {
        mma1(1, i);
        if (i < 14) ldop(0, i);
        __builtin_amdgcn_sched_barrier(0);
      }
    }
    TLS(14);
    if (!KV) {
      char* pq = ws + WS_PQ;
#pragma unroll
      for (int m = 0; m < 4; ++m)
#pragma unroll
        for (int n = 0; n < 5; ++n) {
          u32x2 o = {phi_pair(acc[m][n][0], acc[m][n][1]), phi_pair(acc[m][n][2], acc[m][n][3])};
          const int col = 160 * ct + wn * 80 + n * 16 + 4 * g; const int h = col / 20, q = (col % 20) >> 2;
          st8_wt(pq + 2 * (((size_t)((rt * 8 + wm * 4 + m) * 32 + h) * 5 + q) * 64 + fr * 4), o);
        }
    } else {
      u32x2 pk[4][5];
      const bool isk = fr < 8;
#pragma unroll
      for (int m = 0; m < 4; ++m)
#pragma unroll
        for (int n = 0; n < 5; ++n) {
          if (n <= 2) { pk[m][n].x = phi_pair(acc[m][n][0], acc[m][n][1]); pk[m][n].y = phi_pair(acc[m][n][2], acc[m][n][3]); }
          if (n >= 2) {
            u32 v0 = pk_bf16(acc[m][n][0], acc[m][n][1]), v1 = pk_bf16(acc[m][n][2], acc[m][n][3]);
            if (n > 2) { pk[m][n].x = v0; pk[m][n].y = v1; } else { pk[m][n].x = isk ? pk[m][n].x : v0; pk[m][n].y = isk ? pk[m][n].y : v1; }
          }
        }
      f32x4 kv[10];
#pragma unroll
      for (int i = 0; i < 10; ++i) kv[i] = (f32x4){0, 0, 0, 0};
      const s16x8 ones = {0x3f80, 0x3f80, 0x3f80, 0x3f80, 0x3f80, 0x3f80, 0x3f80, 0x3f80};
#pragma unroll
      for (int u = 0; u < 2; ++u) {
        s16x8 f[5];
#pragma unroll
        for (int n = 0; n < 5; ++n) f[n] = __builtin_bit_cast(s16x8, (u32x4){pk[2 * u][n].x, pk[2 * u][n].y, pk[2 * u + 1][n].x, pk[2 * u + 1][n].y});
        kv[0] = __builtin_amdgcn_mfma_f32_16x16x32_bf16(f[0], f[2], kv[0], 0, 0, 0);
        kv[1] = __builtin_amdgcn_mfma_f32_16x16x32_bf16(f[0], f[3], kv[1], 0, 0, 0);
        kv[2] = __builtin_amdgcn_mfma_f32_16x16x32_bf16(f[1], f[2], kv[2], 0, 0, 0);
        kv[3] = __builtin_amdgcn_mfma_f32_16x16x32_bf16(f[1], f[3], kv[3], 0, 0, 0);
        kv[4] = __builtin_amdgcn_mfma_f32_16x16x32_bf16(f[1], f[4], kv[4], 0, 0, 0);
        kv[5] = __builtin_amdgcn_mfma_f32_16x16x32_bf16(f[2], f[3], kv[5], 0, 0, 0);
        kv[6] = __builtin_amdgcn_mfma_f32_16x16x32_bf16(f[2], f[4], kv[6], 0, 0, 0);
        kv[7] = __builtin_amdgcn_mfma_f32_16x16x32_bf16(f[0], ones, kv[7], 0, 0, 0);
        kv[8] = __builtin_amdgcn_mfma_f32_16x16x32_bf16(f[1], ones, kv[8], 0, 0, 0);
        kv[9] = __builtin_amdgcn_mfma_f32_16x16x32_bf16(f[2], ones, kv[9], 0, 0, 0);
      }
      __builtin_amdgcn_s_barrier();
      f32x4* red = (f32x4*)smem + (wm * 2 + wn) * 640 + lane;
#pragma unroll
      for (int i = 0; i < 10; ++i) red[i * 64] = kv[i];
      __builtin_amdgcn_s_waitcnt(0xC07F);
      __builtin_amdgcn_s_barrier();
    }
  }
  if (KV) {
    if (tid == 0) wait_init_t0(ws, initdone);
    __syncthreads();
    const float* raw = (const float*)smem;
    float* acc = (float*)(ws + WS_ZERO) + (size_t)ct * 4096 + tid;
#pragma unroll
    for (int it = 0; it < 8; ++it) {
      const int e0 = it * 512 + tid;
      const int hl4 = it >> 1, tile = it & 1, lp = (e0 >> 3) & 63, j = e0 & 7;
      const int row = lp & 15, gk = lp >> 4, gg = row >> 2, r = row & 3;
      const int wn = hl4 >> 1, hl = hl4 & 1;
      const int dd = 8 * gk + j;
      if ((tile == 0 || r < 2) && dd < 20) {
        const int d = dd + 20 * hl, dt = d >> 4;
        const int e = (tile == 0 ? 5 * gg + r : 5 * gg + 3 + r) + 20 * hl + 40, et = e >> 4;
        int t, ln;
        if (tile == 0 && r == 3) { t = 7 + dt; ln = ((d & 15) >> 2) * 16; }
        else { t = dt == 0 ? et - 2 : (dt == 1 ? et : et + 2); ln = ((d & 15) >> 2) * 16 + (e & 15); }
        const int idx = (wn * 640 + t * 64 + ln) * 4 + (d & 3);
        const float v = raw[idx] + raw[2 * 640 * 4 + idx];
        __hip_atomic_fetch_add((GAS float*)(acc + it * 512), v, RLX_AGENT);
      }
    }
    TLS(1);
    publish8(ws, CNT_KV, initdone);
  } else {
    TLS(1);
    publish(ws, CNT_PQ + rt, initdone);
  }
}

DI void aux_block(const u16* __restrict__ u, const u16* __restrict__ wo, char* __restrict__ ws, rsrc_t rs, int ab, char* smem, bool& initdone) {
  const int tid = threadIdx.x, lane = tid & 63, wv = __builtin_amdgcn_readfirstlane(tid >> 6);
  char* reg = smem + wv * KG_REG;
  if (wv < 5) {
    const int tile = ab * 5 + wv; const int rb = tile / 10, cb = tile % 10;
    u16* t = (u16*)reg;
#pragma unroll
    for (int i = 0; i < 8; ++i) {
      const int row = i * 8 + (lane >> 3), ch = lane & 7;
      u32x4 v = *(const u32x4*)(u + (size_t)(rb * 64 + row) * D + cb * 64 + ch * 8);
      u32* p = (u32*)(t + row * 66 + ch * 8);
      p[0] = v.x; p[1] = v.y; p[2] = v.z; p[3] = v.w;
    }
    __builtin_amdgcn_wave_barrier();
#pragma unroll
    for (int i = 0; i < 8; ++i) {
      const int c = i * 8 + (lane >> 3), r8 = lane & 7;
      u32 o[4];
#pragma unroll
      for (int k = 0; k < 4; ++k) o[k] = (u32)t[(r8 * 8 + 2 * k) * 66 + c] | ((u32)t[(r8 * 8 + 2 * k + 1) * 66 + c] << 16);
      st16_wt(rs, WS_UT + ((cb * 64 + c) * S + rb * 64 + r8 * 8) * 2, (u32x4){o[0], o[1], o[2], o[3]});
    }
    __builtin_amdgcn_wave_barrier();
  } else {
#pragma unroll
    for (int it = 0; it < 5; ++it) {
      const int uu = (wv - 5) * 64 + lane + 192 * it;
      if (uu < 800) {
        const int U = ab * 800 + uu; const int img = U / 1280, un = U % 1280; const int ct = img / 10, kt = img % 10;
        const int sb = un >> 4, kb = sb / 10, nb = sb % 10, j = ((un >> 1) & 7) ^ (4 * (kb & 1));
        const int T = 2 * kt + (kb >> 2), G = T / 5, tt = T % 5, ii = 8 * tt + j, hh = ii / 5, r = ii % 5;
        const int k = 160 * G + 20 * hh + 5 * (kb & 3) + r;
        st16_wt(rs, WS_WOP + U * 16, *(const u32x4*)(wo + (size_t)k * D + 160 * ct + nb * 16 + 8 * (un & 1)));
      }
    }
  }
  TLS(1);
  publish8(ws, CNT_T, initdone);
}

constexpr int UC_STRIDE = 4160, UCH = 4 * UC_STRIDE, KP_STRIDE = 1280, KPCH = 4 * KP_STRIDE;
constexpr int UC0 = 4 * KG_REG, PART0 = UC0 + 4 * UCH, KC0 = PART0 + 12 * 2592, RED0 = KC0 + 4 * 1312;
constexpr int B_LDS = RED0 + 128;
constexpr int PSPLIT = 22;
DI void b_block(const KGP& kp, char* __restrict__ ws, rsrc_t rs, const u16* __restrict__ dsk_, u16* __restrict__ out, int cb, char* smem, bool& initdone) {
  const int tid = threadIdx.x, lane = tid & 63, wv = __builtin_amdgcn_readfirstlane(tid >> 6), ch = wv >> 1, hf = wv & 1;
  const int d0 = cb * 4;
  float* red = (float*)(smem + RED0);
  float* part = (float*)(smem + PART0);
  KGL l0, l1;
  kgen_load(kp, d0 + wv / 3, wv % 3, l0);
  kgen_load(kp, d0 + (8 + (wv & 3)) / 3, (8 + (wv & 3)) % 3, l1);
  if (tid == 0) { wait_init_t0(ws, initdone); wait_cnt_t0(ws, CNT_T + (blockIdx.x & 7), 64); }
  {
    float ks = kgen_task(l0, wv % 3, smem + wv * KG_REG, part + wv * 648);
    if (lane == 0 && wv % 3 == 0) red[8 + wv / 3] = ks;
  }
  __syncthreads();
  TLS(3);
  if (wv < 4) {
    float ks = kgen_task(l1, (8 + wv) % 3, smem + wv * KG_REG, part + (8 + wv) * 648);
    if (lane == 0 && wv == 1) red[8 + 3] = ks;
  } else {
    const int c = wv - 4;
    char* ur = smem + UC0 + c * UCH;
    u32x4 uv[4];
#pragma unroll
    for (int i = 0; i < 4; ++i) uv[i] = ld16_sc(rs, WS_UT + ((d0 + c) * S + 8 * (lane + 64 * i)) * 2);
    float m = 0.f;
#pragma unroll
    for (int i = 0; i < 4; ++i)
#pragma unroll
      for (int k = 0; k < 4; ++k) m = fmaxf(m, fmaxf(fabsf(lo16(uv[i][k])), fabsf(hi16(uv[i][k]))));
#pragma unroll
    for (int o = 32; o >= 1; o >>= 1) m = fmaxf(m, __shfl_xor(m, o));
    int e = (int)((__float_as_uint(m) >> 23) & 0xff);
    e = e < 30 ? 30 : (e > 220 ? 220 : e);
    const float us = __uint_as_float((u32)(127 + 140 - e) << 23);
    if (lane == 0) red[c] = __uint_as_float((u32)(e - 13) << 23);
#pragma unroll
    for (int i = 0; i < 4; ++i) {
      const int q = lane + 64 * i;
      u32x4 o = {pk_f16(hi16(uv[i].w) * us, lo16(uv[i].w) * us), pk_f16(hi16(uv[i].z) * us, lo16(uv[i].z) * us),
                 pk_f16(hi16(uv[i].y) * us, lo16(uv[i].y) * us), pk_f16(hi16(uv[i].x) * us, lo16(uv[i].x) * us)};
      *(u32x4*)(ur + 2 * (2040 - 8 * q)) = o;
    }
    if (lane < 4) *(u32x4*)(ur + 4096 + 16 * lane) = (u32x4){0, 0, 0, 0};
    __builtin_amdgcn_wave_barrier();
#pragma unroll
    for (int k = 0; k < 5; ++k) {
      const int q = lane + 64 * k;
      if (q < 260) {
        u32x4 a = *(const u32x4*)(ur + 16 * q);
        u32x2 b = *(const u32x2*)(ur + 16 * q + 16);
        u32x4 c1 = {__builtin_amdgcn_alignbit(a.y, a.x, 16), __builtin_amdgcn_alignbit(a.z, a.y, 16), __builtin_amdgcn_alignbit(a.w, a.z, 16), __builtin_amdgcn_alignbit(b.x, a.w, 16)};
        u32x4 c2 = {a.y, a.z, a.w, b.x};
        u32x4 c3 = {c1.y, c1.z, c1.w, __builtin_amdgcn_alignbit(b.y, b.x, 16)};
        *(u32x4*)(ur + 1 * UC_STRIDE + 16 * q) = c1;
        *(u32x4*)(ur + 2 * UC_STRIDE + 16 * q) = c2;
        *(u32x4*)(ur + 3 * UC_STRIDE + 16 * q) = c3;
      }
    }
  }
  __syncthreads();
  TLS(11);
  {
    u16* kc = (u16*)(smem + KC0 + ch * 1312);
    const float* p0 = part + ch * 3 * 648;
#pragma unroll
    for (int i = 0; i < 2; ++i) {
      const int ci = hf * 81 + 64 * i + lane;
      if (lane + 64 * i < 81) {
        f32x4 v = *(const f32x4*)(p0 + ci * 4) + *(const f32x4*)(p0 + 648 + ci * 4) + *(const f32x4*)(p0 + 1296 + ci * 4);
        *(u32x2*)(kc + ci * 4) = (u32x2){pk_f16(v[0] * 128.f, v[1] * 128.f), pk_f16(v[2] * 128.f, v[3] * 128.f)};
      }
    }
  }
  __syncthreads();
  {
    const u16* kc = (const u16*)(smem + KC0 + ch * 1312);
    char* kpl = smem + ch * KPCH;
    if (hf == 1 && lane < 60) *(u32x4*)(kpl + (lane & 3) * KP_STRIDE + (lane >> 2) * 16) = (u32x4){0, 0, 0, 0};
#pragma unroll
    for (int k = 0; k < 2; ++k) {
      const int q = hf + 2 * (lane + 64 * k);
      u32 v[8];
#pragma unroll
      for (int j8 = 0; j8 < 8; ++j8) {
        int j = 8 * q + j8;
        u32 fb = __float_as_uint((float)j); fb += 0x7fffu + ((fb >> 16) & 1u);
        int idx = j < 256 ? j : 256 + (int)((fb >> 16) - 0x4380u);
        v[j8] = kc[idx];
      }
      u32x4 o = {v[0] | (v[1] << 16), v[2] | (v[3] << 16), v[4] | (v[5] << 16), v[6] | (v[7] << 16)};
      *(u32x4*)(kpl + (q & 3) * KP_STRIDE + ((q >> 2) + 15) * 16) = o;
    }
  }
  __syncthreads();
  TLS(12);
  f32x4 acc[4][2];
#pragma unroll
  for (int t = 0; t < 4; ++t) { acc[t][0] = (f32x4){0, 0, 0, 0}; acc[t][1] = (f32x4){0, 0, 0, 0}; }
  const int b = lane & 15, g = lane >> 4;
  char* ureg = smem + UC0 + ch * UCH;
  {
    const int cpy = (3 - b) & 3;
    const char* pa0 = ureg + cpy * UC_STRIDE + 2 * (2044 - 4 * (b >> 2) + 8 * g);
    const char* pb0 = smem + ch * KPCH + g * KP_STRIDE + 16 * (b + 15);
    typedef const volatile LAS u32x2* lp2;
    typedef const volatile LAS u32x4* lp4;
    const u32 abase = (u32)(size_t)(pa0 - 64 * 63 - 32);
    const u32 bbase = (u32)(size_t)(pb0 - 16 * 63);
    constexpr int DP = 2;
    u32x2 Ab[DP + 1][4]; u32x4 Bb[DP + 1][4];
    auto run = [&](auto P0c, auto P1c) {
      constexpr int P0 = decltype(P0c)::value, P1 = decltype(P1c)::value;
      auto issue = [&](int p) {
        const int sl = p % (DP + 1);
        const u32 oa = 64 * (63 - p);
        Ab[sl][0] = *(lp2)(abase + oa + 32); Ab[sl][1] = *(lp2)(abase + oa + 40);
        Ab[sl][2] = *(lp2)(abase + oa);      Ab[sl][3] = *(lp2)(abase + oa + 8);
#pragma unroll
        for (int t = 0; t < 4; ++t) if (t >= (p >> 4)) Bb[sl][t] = *(lp4)(bbase + 16 * (63 - p) + 256 * t);
      };
#pragma unroll
      for (int p = P0; p < P0 + DP; ++p) issue(p);
#pragma unroll
      for (int p = P0; p < P1; ++p) {
        if (p + DP < P1) issue(p + DP);
        __builtin_amdgcn_sched_barrier(0);
        const int sl = p % (DP + 1);
        h8 A0 = __builtin_bit_cast(h8, (u32x4){Ab[sl][0].x, Ab[sl][0].y, Ab[sl][1].x, Ab[sl][1].y});
        h8 A1 = __builtin_bit_cast(h8, (u32x4){Ab[sl][2].x, Ab[sl][2].y, Ab[sl][3].x, Ab[sl][3].y});
#pragma unroll
        for (int t = 0; t < 4; ++t) if (t >= (p >> 4)) {
          h8 B = __builtin_bit_cast(h8, Bb[sl][t]);
          acc[t][0] = __builtin_amdgcn_mfma_f32_16x16x32_f16(A0, B, acc[t][0], 0, 0, 0);
          acc[t][1] = __builtin_amdgcn_mfma_f32_16x16x32_f16(A1, B, acc[t][1], 0, 0, 0);
        }
        __builtin_amdgcn_sched_barrier(0);
      }
    };
    if (hf == 0) run(std::integral_constant<int, 0>{}, std::integral_constant<int, PSPLIT>{});
    else run(std::integral_constant<int, PSPLIT>{}, std::integral_constant<int, 64>{});
  }
  TLS(13);
  f32x4* xch = (f32x4*)(smem + PART0 + ch * 6144) + lane;
  if (hf == 1) {
#pragma unroll
    for (int t = 1; t < 4; ++t) { xch[(2 * (t - 1)) * 64] = acc[t][0]; xch[(2 * (t - 1) + 1) * 64] = acc[t][1]; }
  }
  __syncthreads();
  if (hf == 0) {
    const float ysc = red[8 + ch] * red[ch];
    char* yb = smem + ch * 4096;
#pragma unroll
    for (int t = 0; t < 4; ++t)
#pragma unroll
      for (int r = 0; r < 2; ++r) {
        f32x4 a = acc[t][r];
        if (t > 0) a += xch[(2 * (t - 1) + r) * 64];
        u32x2 o = {pk_bf16(a[0] * ysc, a[1] * ysc), pk_bf16(a[2] * ysc, a[3] * ysc)};
        *(u32x2*)(yb + 2 * (32 * (16 * t + b) + 16 * r + 4 * g)) = o;
      }
  }
  __syncthreads();
  {
    u32x2 yv[4], uu[4]; float iu[4], dk[4];
#pragma unroll
    for (int c = 0; c < 4; ++c) {
      yv[c] = *(const u32x2*)(smem + c * 4096 + 8 * tid);
      uu[c] = *(const u32x2*)(smem + UC0 + c * UCH + 2 * (2044 - 4 * tid));
      iu[c] = red[c]; dk[c] = bf2f(dsk_[d0 + c]);
    }
#pragma unroll
    for (int i = 0; i < 4; ++i) {
      float r[4];
#pragma unroll
      for (int c = 0; c < 4; ++c) {
        u32 yw = yv[c][i >> 1]; float y = (i & 1) ? hi16(yw) : lo16(yw);
        const u32 uw = (i < 2) ? uu[c].y : uu[c].x; const u16 ub = (i & 1) ? (u16)uw : (u16)(uw >> 16); float uf = (float)__builtin_bit_cast(_Float16, ub) * iu[c];
        r[c] = y + rbf(uf * dk[c]);
      }
      u32x2 o = {pk_bf16(r[0], r[1]), pk_bf16(r[2], r[3])};
      *(u32x2*)(out + (size_t)(4 * tid + i) * 1280 + 640 + d0) = o;
    }
  }
  __syncthreads();
}

constexpr int OSTAGE = 20480, ONBUF = 4, O_OFF_W = 81920;
DI void o_block(char* __restrict__ ws, rsrc_t rs, u16* __restrict__ out, int rt, int ct, char* smem, bool& initdone) {
  const int tid = threadIdx.x, lane = tid & 63, wv = __builtin_amdgcn_readfirstlane(tid >> 6);
  const int row0 = rt * 64, fr = lane & 15, g = lane >> 4;
  const int lw = wv - 4;
  if (tid == 0) { wait_init_t0(ws, initdone); wait_cnt_t0(ws, CNT_T + (blockIdx.x & 7), 64); }
  acquire_all();
  const char* wsrc = ws + WS_WOP + (size_t)ct * 10 * OSTAGE + lw * 5120 + lane * 16;
  auto stage = [&](int kt) {
    char* b = smem + O_OFF_W + (kt & (ONBUF - 1)) * OSTAGE + lw * 5120;
#pragma unroll
    for (int i = 0; i < 5; ++i) __builtin_amdgcn_global_load_lds((const u32*)(wsrc + kt * OSTAGE + i * 1024), (LAS u32*)(b + i * 1024), 16, 0, 0);
  };
  if (wv >= 4) {
#pragma unroll 1
    for (int i = 0; i < ONBUF; ++i) stage(i);
  }
  if (tid == 0) wait_cnt_t0(ws, CNT_PQ + (rt >> 1), 4);
  asm volatile("s_waitcnt lgkmcnt(0)" ::: "memory");
  __builtin_amdgcn_s_barrier();
  const char* pq = ws + WS_PQ;
  const char* zp = ws + WS_ZERO + CNT_OFF + ZPAD * 128;
  u32x4 kr[4][2][2]; u32x2 pa[4][4], pb[4][4];
#pragma unroll
  for (int hh = 0; hh < 4; ++hh)
#pragma unroll
    for (int st = 0; st < 4; ++st) {
      const char* p = pq + 2 * (((size_t)((rt * 4 + st) * 32 + 4 * wv + hh) * 5) * 64 + fr * 4);
      pa[hh][st] = ld8_sc(g < 3 ? p + 2 * g * 128 : zp);
      pb[hh][st] = ld8_sc(g < 2 ? p + (2 * g + 1) * 128 : zp);
    }
  if (tid == 0) wait_cnt_t0(ws, CNT_KV + (blockIdx.x & 7), 128);
  TLS(6);
  asm volatile("s_waitcnt lgkmcnt(0)" ::: "memory");
  __builtin_amdgcn_s_barrier();
  TLS(7);
#pragma unroll
  for (int hh = 0; hh < 4; ++hh)
#pragma unroll
    for (int tl = 0; tl < 2; ++tl) {
      kr[hh][tl][0] = ld16_sc(rs, WS_ZERO + (((4 * wv + hh) * 2 + tl) * 64 + lane) * 32);
      kr[hh][tl][1] = ld16_sc(rs, WS_ZERO + (((4 * wv + hh) * 2 + tl) * 64 + lane) * 32 + 16);
    }
  __builtin_amdgcn_sched_barrier(0);
  {
    s16x8 kf[4][2];
#pragma unroll
    for (int hh = 0; hh < 4; ++hh)
#pragma unroll
      for (int tl = 0; tl < 2; ++tl) {
        f32x4 a = __builtin_bit_cast(f32x4, kr[hh][tl][0]), b = __builtin_bit_cast(f32x4, kr[hh][tl][1]);
        kf[hh][tl] = __builtin_bit_cast(s16x8, (u32x4){pk_bf16(a[0], a[1]), pk_bf16(a[2], a[3]), pk_bf16(b[0], b[1]), pk_bf16(b[2], b[3])});
      }
    asm volatile("" :: "v"(kf[3][1]), "v"(pa[3][3]), "v"(pb[3][3]));
    TLS(10);
    const int G = wv >> 1, half = wv & 1;
#pragma unroll
    for (int st = 0; st < 4; ++st) {
      float vals[20];
#pragma unroll
      for (int hh = 0; hh < 4; ++hh) {
        s16x8 pf = __builtin_bit_cast(s16x8, (u32x4){pa[hh][st].x, pa[hh][st].y, pb[hh][st].x, pb[hh][st].y});
        f32x4 z4 = {0, 0, 0, 0};
        f32x4 t0 = __builtin_amdgcn_mfma_f32_16x16x32_bf16(kf[hh][0], pf, z4, 0, 0, 0);
        f32x4 t1 = __builtin_amdgcn_mfma_f32_16x16x32_bf16(kf[hh][1], pf, z4, 0, 0, 0);
        float den = rbf(rbf(t0[3]) + 1e-6f);
        float rd = __builtin_amdgcn_rcpf(den);
        vals[5 * hh + 0] = t0[0] * rd; vals[5 * hh + 1] = t0[1] * rd; vals[5 * hh + 2] = t0[2] * rd;
        vals[5 * hh + 3] = t1[0] * rd; vals[5 * hh + 4] = t1[1] * rd;
      }
#pragma unroll
      for (int i = 0; i < 5; ++i) {
        const int byte = 40 * half + 8 * i;
        const int T = 5 * G + (byte >> 4);
        *(u32x2*)(smem + (st * 20 + T) * 1024 + lane * 16 + (byte & 8)) = (u32x2){pk_bf16(vals[4 * i], vals[4 * i + 1]), pk_bf16(vals[4 * i + 2], vals[4 * i + 3])};
      }
    }
  }
  if (wv >= 4) {
    asm volatile("s_waitcnt vmcnt(0) lgkmcnt(0)" ::: "memory");
    __builtin_amdgcn_s_barrier();
#pragma unroll 1
    for (int kt = 0; kt < 6; ++kt) {
      asm volatile("s_waitcnt vmcnt(10)" ::: "memory");
      __builtin_amdgcn_s_barrier();
      stage(kt + ONBUF);
    }
    asm volatile("s_waitcnt vmcnt(10)" ::: "memory"); __builtin_amdgcn_s_barrier();
    asm volatile("s_waitcnt vmcnt(5)" ::: "memory");  __builtin_amdgcn_s_barrier();
    asm volatile("s_waitcnt vmcnt(0)" ::: "memory");  __builtin_amdgcn_s_barrier();
    return;
  }
  const int wm = wv >> 1, wn = wv & 1;
  f32x4 acc[2][5];
#pragma unroll
  for (int m = 0; m < 2; ++m)
#pragma unroll
    for (int n = 0; n < 5; ++n) acc[m][n] = (f32x4){0, 0, 0, 0};
  const u32 sbase = (u32)(size_t)smem;
  const u32 boff0 = O_OFF_W + g * 2560 + wn * 1280 + ((0 ^ (4 * (g & 1))) + (fr >> 2)) * 32 + (lane & 3) * 8;
  const u32 boff1 = O_OFF_W + g * 2560 + wn * 1280 + ((4 ^ (4 * (g & 1))) + (fr >> 2)) * 32 + (lane & 3) * 8;
  u32 aad = sbase + wm * 2 * 20 * 1024 + lane * 16;
  u32 b0 = sbase + boff0, b1 = sbase + boff1;
  s16x8 af[2][2]; s16x4 bl[2][5], bh[2][5];
  auto ldop = [&](int ks, int i) {
    if (i < 10) {
      const int n = i >> 1;
      if (i & 1) bh[ks][n] = __builtin_amdgcn_ds_read_tr16_b64_v4i16((LAS s16x4*)(b1 + ks * 10240 + n * 256));
      else       bl[ks][n] = __builtin_amdgcn_ds_read_tr16_b64_v4i16((LAS s16x4*)(b0 + ks * 10240 + n * 256));
    } else af[ks][i - 10] = *(const LAS s16x8*)(aad + ((i - 10) * 20 + ks) * 1024);
  };
  auto mma1 = [&](int sl, int i) {
    const int m = i / 5, n = i % 5;
    s16x8 bb = __builtin_shufflevector(bl[sl][n], bh[sl][n], 0, 1, 2, 3, 4, 5, 6, 7);
    acc[m][n] = __builtin_amdgcn_mfma_f32_16x16x32_bf16(bb, af[sl][m], acc[m][n], 0, 0, 0);
  };
  __builtin_amdgcn_s_waitcnt(0xC07F);
  __builtin_amdgcn_s_barrier();
  TLS(9);
#pragma unroll
  for (int i = 0; i < 12; ++i) ldop(0, i);
  int cur = 0;
#pragma unroll 1
  for (int kt = 0; kt < 10; ++kt) {
    __builtin_amdgcn_sched_barrier(0);
#pragma unroll
    for (int i = 0; i < 10; ++i) {
      mma1(0, i);
      ldop(1, i); if (i < 2) ldop(1, 10 + i);
      __builtin_amdgcn_sched_barrier(0);
    }
    cur = (cur + 1) & (ONBUF - 1);
    b0 = sbase + cur * OSTAGE + boff0; b1 = sbase + cur * OSTAGE + boff1;
    aad += (kt < 9) ? 2048 : 0;
    __builtin_amdgcn_s_waitcnt(0xC07F);
    if (kt < 9) __builtin_amdgcn_s_barrier();
    __builtin_amdgcn_sched_barrier(0);
#pragma unroll
    for (int i = 0; i < 10; ++i) {
      mma1(1, i);
      ldop(0, i); if (i < 2) ldop(0, 10 + i);
      __builtin_amdgcn_sched_barrier(0);
    }
  }
#pragma unroll
  for (int m = 0; m < 2; ++m)
#pragma unroll
    for (int n = 0; n < 5; ++n) {
      u32x2 o = {pk_bf16(acc[m][n][0], acc[m][n][1]), pk_bf16(acc[m][n][2], acc[m][n][3])};
      *(u32x2*)(out + (size_t)(row0 + wm * 32 + m * 16 + fr) * 1280 + 160 * ct + wn * 80 + n * 16 + 4 * g) = o;
    }
}

struct P { const u16 *x, *w, *wo, *u, *lar, *aim, *cr, *ci, *ldt, *dsk; u16* out; char* ws; };
__global__ __launch_bounds__(512, 2) void kmain(P p) {
  extern __shared__ __attribute__((aligned(16))) char smem[];
  const int b = blockIdx.x, tid = threadIdx.x;
  char* ws = p.ws;
  const rsrc_t rs = __builtin_amdgcn_make_buffer_rsrc((void*)ws, 0, WS_SPAN, 0x00020000);
  bool initdone = false;
  TLS(0);
  if (b >= 248) {
    const int part = b - 248;
    for (int i = tid; i < ZERO_BYTES / 128; i += 512) st16_wt(rs, WS_ZERO + part * (ZERO_BYTES / 8) + i * 16, (u32x4){0, 0, 0, 0});
    asm volatile("s_waitcnt vmcnt(0)" ::: "memory");
    __syncthreads();
    if (tid < 256) __hip_atomic_store((gu64*)(ws + WS_FLAG) + tid * 8 + part, MAGIC, RLX_AGENT);
  }
  const KGP kp = {p.lar, p.aim, p.cr, p.ci, p.ldt};
  int bt0 = -1, bt1 = -1, o_task = -1;
  if (b < 192) {
    const int xcd = b & 7, j = b >> 3;
    const int rt = (xcd >> 1) * 4 + (j & 3), c6 = j >> 2;
    if (c6 < 4) { q_block<true>(p.x, p.w, ws, rs, rt, (xcd & 1) * 4 + c6, smem, initdone); if (j < 12) o_task = xcd * 16 + j; else bt0 = xcd * 20 + 16 + (j - 12); }
    else { q_block<false>(p.x, p.w, ws, rs, rt, (xcd & 1) * 2 + (c6 - 4), smem, initdone); bt0 = xcd * 20 + 8 + (j - 16); }
  } else {
    const int ab = b - 192;
    aux_block(p.u, p.wo, ws, rs, ab, smem, initdone);
    { const int xcd = ab & 7, i = ab >> 3; bt0 = xcd * 20 + i; if (i < 4) o_task = xcd * 16 + 12 + i; }
  }
  TLS(2);
  __syncthreads();
#pragma unroll 1
  for (int it = 0; it < 2; ++it) {
    const int t = it == 0 ? bt0 : bt1;
    if (t >= 0) b_block(kp, ws, rs, p.dsk, p.out, t, smem, initdone);
  }
  TLS(5);
  if (o_task >= 0) o_block(ws, rs, p.out, o_task >> 2, o_task & 3, smem, initdone);
  TLS(8);
}

extern "C" void kernel_launch(void* const* d_in, const int* in_sizes, int n_in, void* d_out, int out_size, void* d_ws, size_t ws_size, hipStream_t stream) {
  constexpr int LDS = O_OFF_W + ONBUF * OSTAGE;
  static_assert(QNBUF * QSTAGE <= LDS && 8 * KG_REG <= LDS && B_LDS <= LDS, "LDS");
  (void)hipFuncSetAttribute((const void*)kmain, hipFuncAttributeMaxDynamicSharedMemorySize, LDS);
  P p = {(const u16*)d_in[0], (const u16*)d_in[1], (const u16*)d_in[2], (const u16*)d_in[3], (const u16*)d_in[4], (const u16*)d_in[5], (const u16*)d_in[6], (const u16*)d_in[7], (const u16*)d_in[8], (const u16*)d_in[9], (u16*)d_out, (char*)d_ws};
  kmain<<<256, 512, LDS, stream>>>(p);
}
```

```cpp
#include <hip/hip_runtime.h>
#include <hip/hip_bf16.h>
#include <stdint.h>
#include <type_traits>
typedef unsigned short u16;
typedef unsigned int u32;
typedef float f32x2 __attribute__((ext_vector_type(2)));
typedef float f32x4 __attribute__((ext_vector_type(4)));
typedef float f32x16 __attribute__((ext_vector_type(16)));
typedef short s16x4 __attribute__((ext_vector_type(4)));
typedef short s16x8 __attribute__((ext_vector_type(8)));
typedef _Float16 h8 __attribute__((ext_vector_type(8)));
typedef _Float16 h2 __attribute__((ext_vector_type(2)));
typedef __bf16 bf16x2 __attribute__((ext_vector_type(2)));
typedef u32 u32x2 __attribute__((ext_vector_type(2)));
typedef u32 u32x4 __attribute__((ext_vector_type(4)));
#define DI __device__ __forceinline__
#define LAS __attribute__((address_space(3)))
#define GAS __attribute__((address_space(1)))
typedef GAS unsigned gu32; typedef GAS unsigned long long gu64;
#define RLX_AGENT __ATOMIC_RELAXED, __HIP_MEMORY_SCOPE_AGENT
DI float bf2f(u16 x) { return __uint_as_float(((u32)x) << 16); }
DI u32 pk_bf16(float a, float b) { bf16x2 r = __builtin_convertvector((f32x2){a, b}, bf16x2); return __builtin_bit_cast(u32, r); }
DI u32 pk_f16(float a, float b) { h2 r = __builtin_convertvector((f32x2){a, b}, h2); return __builtin_bit_cast(u32, r); }
DI float lo16(u32 x) { return __uint_as_float(x << 16); }
DI float hi16(u32 x) { return __uint_as_float(x & 0xffff0000u); }
DI float rbf(float f) { return lo16(pk_bf16(f, 0.f)); }
DI u32 phi_pair(float a, float b) {
  u32 t = pk_bf16(a, b);
  float ta = lo16(t), tb = hi16(t);
  float ea = __builtin_amdgcn_fmed3f(__builtin_amdgcn_exp2f(ta * 1.4426950408889634f), 0.f, 1.f);
  float eb = __builtin_amdgcn_fmed3f(__builtin_amdgcn_exp2f(tb * 1.4426950408889634f), 0.f, 1.f);
  return pk_bf16(ea + fmaxf(ta, 1e-6f), eb + fmaxf(tb, 1e-6f));
}
DI f32x2 cmul(f32x2 x, f32x2 y) {
  f32x2 t, z;
  asm("v_pk_mul_f32 %0, %1, %2 op_sel_hi:[0,1]" : "=v"(t) : "v"(x), "v"(y));
  asm("v_pk_fma_f32 %0, %1, %2, %3 op_sel:[1,1,0] op_sel_hi:[1,0,1] neg_lo:[0,1,0]" : "=v"(z) : "v"(x), "v"(y), "v"(t));
  return z;
}
DI f32x2 cexpP(float ar2, float airev, float P) {
  float e = __builtin_amdgcn_exp2f(ar2 * P);
  float t = __builtin_amdgcn_fractf(airev * P);
  float s = __builtin_amdgcn_sinf(t), c = __builtin_amdgcn_cosf(t);
  f32x2 r = {e * c, e * s};
  return r;
}
constexpr int S = 2048, D = 640, NS = 192;
constexpr u32 WS_PQ = 0;
constexpr u32 WS_UT = 2621440;
constexpr u32 WS_WOP = 2 * 2621440;
constexpr u32 WS_FLAG = 2 * 2621440 + 819200;
constexpr u32 WS_ZERO = WS_FLAG + 16384;
constexpr int KVACC_BYTES = 32 * 1024 * 4;
constexpr int CNT_OFF = KVACC_BYTES;
constexpr int CNT_KV = 0, CNT_T = 8, CNT_PQ = 16, CNT_KG = 32, ZPAD = 40;
constexpr int ZERO_BYTES = KVACC_BYTES + 48 * 128;
constexpr u32 WS_SPAN = 7u << 20;
constexpr unsigned long long MAGIC = 0x9e3779b97f4a7c15ull;
constexpr unsigned SPIN_CAP = 1u << 17;
typedef __amdgpu_buffer_rsrc_t rsrc_t;

DI gu32* cnt_ptr(char* ws, int i) { return (gu32*)(ws + WS_ZERO + CNT_OFF + i * 128); }
DI void wait_init_t0(char* ws, bool& done) {
  if (done) return;
  gu64* f = (gu64*)(ws + WS_FLAG) + blockIdx.x * 8;
  for (unsigned sp = 0; sp < SPIN_CAP; ++sp) {
    bool ok = true;
#pragma unroll
    for (int i = 0; i < 8; ++i) ok &= __hip_atomic_load(f + i, RLX_AGENT) == MAGIC;
    if (ok) break;
    __builtin_amdgcn_s_sleep(2);
  }
#pragma unroll
  for (int i = 0; i < 8; ++i) __hip_atomic_store(f + i, 0ull, RLX_AGENT);
  done = true;
}
DI void wait_cnt_t0(char* ws, int i, unsigned want) {
  gu32* c = cnt_ptr(ws, i);
  if (__hip_atomic_load(c, RLX_AGENT) >= want) return;
  unsigned v0, v1, v2, v3;
#define PLD(v) asm volatile("global_load_dword %0, %1, off sc1" : "=v"(v) : "v"(c) : "memory")
  PLD(v0); __builtin_amdgcn_s_sleep(2); PLD(v1); __builtin_amdgcn_s_sleep(2); PLD(v2); __builtin_amdgcn_s_sleep(2);
  for (unsigned sp = 0; sp < SPIN_CAP; ++sp) {
    PLD(v3); asm volatile("s_waitcnt vmcnt(3)" : "+v"(v0) :: "memory"); if (v0 >= want) break; __builtin_amdgcn_s_sleep(2);
    PLD(v0); asm volatile("s_waitcnt vmcnt(3)" : "+v"(v1) :: "memory"); if (v1 >= want) break; __builtin_amdgcn_s_sleep(2);
    PLD(v1); asm volatile("s_waitcnt vmcnt(3)" : "+v"(v2) :: "memory"); if (v2 >= want) break; __builtin_amdgcn_s_sleep(2);
    PLD(v2); asm volatile("s_waitcnt vmcnt(3)" : "+v"(v3) :: "memory"); if (v3 >= want) break; __builtin_amdgcn_s_sleep(2);
  }
  asm volatile("s_waitcnt vmcnt(0)" : "+v"(v0), "+v"(v1), "+v"(v2), "+v"(v3) :: "memory");
#undef PLD
}
DI void publish(char* ws, int i, bool& initdone) {
  asm volatile("s_waitcnt vmcnt(0)" ::: "memory");
  __syncthreads();
  if (threadIdx.x == 0) { wait_init_t0(ws, initdone); __hip_atomic_fetch_add(cnt_ptr(ws, i), 1u, RLX_AGENT); }
}
DI void publish8(char* ws, int i, bool& initdone) {
  if (threadIdx.x == 0) wait_init_t0(ws, initdone);
  asm volatile("s_waitcnt vmcnt(0)" ::: "memory");
  __syncthreads();
  if (threadIdx.x < 8) __hip_atomic_fetch_add(cnt_ptr(ws, i + threadIdx.x), 1u, RLX_AGENT);
}
DI u32x4 ld16_sc(rsrc_t r, u32 off) { return __builtin_amdgcn_raw_buffer_load_b128(r, off, 0, 16); }
DI u32x2 ld8_sc(const void* p) { unsigned long long v = __hip_atomic_load((gu64*)p, RLX_AGENT); return (u32x2){(u32)v, (u32)(v >> 32)}; }
DI float ld4_sc(const void* p) { return __uint_as_float(__hip_atomic_load((gu32*)p, RLX_AGENT)); }
DI void acquire_all() {
  if (threadIdx.x == 0) { __builtin_amdgcn_fence(__ATOMIC_ACQUIRE, "agent"); asm volatile("s_waitcnt vmcnt(0)" ::: "memory"); }
  __syncthreads();
}
DI void st16_wt(rsrc_t r, u32 off, u32x4 v) { __builtin_amdgcn_raw_buffer_store_b128(v, r, off, 0, 16); }
DI void st8_wt(void* p, u32x2 v) { __hip_atomic_store((gu64*)p, ((unsigned long long)v.y << 32) | v.x, RLX_AGENT); }

#define TLS(i)
constexpr int QSTAGE = 36864, QNBUF = 4;
constexpr int TROW = 272, KG_REG = 14720;

DI f32x2 cm(f32x2 x, f32x2 y) {
  f32x2 xx = {x.x, x.x}, xy = {x.y, x.y}, ys = {-y.y, y.x};
  return __builtin_elementwise_fma(xy, ys, xx * y);
}
DI f32x2 cmc(f32x2 x, f32x2 y) {
  f32x2 xx = {x.x, x.x}, xy = {x.y, x.y}, yc = {y.x, -y.y}, ys = {y.y, y.x};
  return __builtin_elementwise_fma(xy, ys, xx * yc);
}
DI void kgen_chunk(float a2, float av, f32x2 Ein, char* reg, f32x16& kacc) {
  const int lane = threadIdx.x & 63;
  u32* tE = (u32*)(reg) + lane;
  u32* tF = (u32*)(reg + 21 * TROW) + lane;
  constexpr int R = TROW / 4;
  auto stE = [&](int r, f32x2 v) { tE[r * R] = pk_f16(v.x, v.y); };
  auto stF = [&](int r, f32x2 v) { tF[r * R] = pk_f16(v.x, v.y); };
  f32x2 w1 = cexpP(a2, av, 1.f), v1 = cexpP(a2, av, 8.f), B256 = cexpP(a2, av, 256.f);
  f32x2 w2 = cm(w1, w1), w4 = cm(w2, w2);
  f32x2 v2 = cm(v1, v1), v4 = cm(v2, v2), v8 = cm(v4, v4), v16 = cm(v8, v8);
  f32x2 B512 = cm(B256, B256), B1024 = cm(B512, B512), B768 = cm(B512, B256);
  f32x2 E0 = {Ein.x, -Ein.y};
  f32x2 E1 = cmc(E0, w1), E2 = cmc(E0, w2), E3 = cmc(E1, w2), E4 = cmc(E0, w4), E5 = cmc(E1, w4), E6 = cmc(E2, w4), E7 = cmc(E3, w4);
  stE(0, E0); stE(1, E1); stE(2, E2); stE(3, E3); stE(4, E4); stE(5, E5); stE(6, E6); stE(7, E7);
  stE(8, cmc(E0, B256)); stE(9, cmc(E2, B256)); stE(10, cmc(E4, B256)); stE(11, cmc(E6, B256));
  stE(12, cmc(E0, B512)); stE(13, cmc(E4, B512)); stE(14, cmc(E0, B768)); stE(15, cmc(E4, B768));
  f32x2 G0 = cmc(E0, B1024);
  stE(16, G0); stE(17, cmc(G0, B256)); stE(18, cmc(G0, B512)); stE(19, cmc(G0, B768)); stE(20, cmc(G0, B1024));
  f32x2 F[8];
  F[0].x = 1.f; F[0].y = 0.f; F[1] = v1; F[2] = v2; F[3] = cm(v2, v1); F[4] = v4; F[5] = cm(v4, v1); F[6] = cm(v4, v2); F[7] = cm(v4, F[3]);
  f32x2 v24 = cm(v16, v8);
#pragma unroll
  for (int i = 0; i < 8; ++i) {
    stF(i, F[i]);
    stF(8 + i, i == 0 ? v8 : cm(F[i], v8));
    stF(16 + i, i == 0 ? v16 : cm(F[i], v16));
    stF(24 + i, i == 0 ? v24 : cm(F[i], v24));
  }
  __builtin_amdgcn_wave_barrier();
  const int r = lane & 31, hh = lane >> 5;
  const char* pa = reg + (r < 21 ? r : 20) * TROW + 16 * hh;
  const char* pb = reg + (21 + r) * TROW + 16 * hh;
#pragma unroll
  for (int s = 0; s < 8; ++s) {
    h8 a = *(const h8*)(pa + 32 * s);
    h8 b = *(const h8*)(pb + 32 * s);
    kacc = __builtin_amdgcn_mfma_f32_32x32x16_f16(a, b, kacc, 0, 0, 0);
  }
  __builtin_amdgcn_wave_barrier();
}

struct KGP { const u16 *lar, *aim, *cr, *ci, *ldt; };
struct KGL { u16 pcr[3], pci[3], plar, paim, pldt; };
DI void kgen_load(const KGP& p, int d, int c, KGL& l) {
  const int lane = threadIdx.x & 63;
#pragma unroll
  for (int q = 0; q < 3; ++q) { int n = d * NS + 64 * q + lane; l.pcr[q] = p.cr[n]; l.pci[q] = p.ci[n]; }
  l.plar = p.lar[d * NS + 64 * c + lane]; l.paim = p.aim[d * NS + 64 * c + lane];
  l.pldt = p.ldt[d];
}
template <class HOOK> DI float kgen_task(const KGL& l, int c, char* reg, float* dst, HOOK hook) {
  const int lane = threadIdx.x & 63;
  const float dt = rbf(expf(bf2f(l.pldt)));
  float cmax = 0.f;
#pragma unroll
  for (int q = 0; q < 3; ++q) cmax = fmaxf(cmax, fmaxf(fabsf(bf2f(l.pcr[q])), fabsf(bf2f(l.pci[q]))));
  cmax *= dt * 2.f;
#pragma unroll
  for (int o = 32; o >= 1; o >>= 1) cmax = fmaxf(cmax, __shfl_xor(cmax, o));
  int ce = (int)((__float_as_uint(cmax) >> 23) & 0xff);
  ce = ce < 30 ? 30 : (ce > 220 ? 220 : ce);
  const float sc = __uint_as_float((u32)(127 + 125 - ce) << 23);
  const float isc = __uint_as_float((u32)(ce + 2) << 23);
  const float Are = -rbf(expf(bf2f(l.plar)));
  const float ar2 = Are * dt * 1.4426950408889634f, airev = bf2f(l.paim) * dt * 0.15915494309189535f;
  const u16 mcr = c == 0 ? l.pcr[0] : (c == 1 ? l.pcr[1] : l.pcr[2]), mci = c == 0 ? l.pci[0] : (c == 1 ? l.pci[1] : l.pci[2]);
  f32x2 E = {bf2f(mcr) * dt * 2.f * sc, bf2f(mci) * dt * 2.f * sc};
  f32x16 kacc;
#pragma unroll
  for (int i = 0; i < 16; ++i) kacc[i] = 0.f;
  hook();
  kgen_chunk(ar2, airev, E, reg, kacc);
  const int i = lane & 31, hh = lane >> 5;
#pragma unroll
  for (int rg = 0; rg < 12; ++rg) {
    const int q = rg >> 2, sub = rg & 3;
    int idx; bool ok = true;
    if (q == 0) idx = 8 * i + 4 * hh + sub;
    else if (q == 1) idx = hh == 0 ? 256 + 4 * i + sub : 384 + 64 * (sub >> 1) + 2 * i + (sub & 1);
    else { idx = hh == 0 ? 512 + 32 * sub + i : 640; ok = (hh == 0) || (sub == 0 && i == 0); }
    if (ok) dst[idx] = kacc[rg];
  }
  return isc * (1.f / 128.f);
}

template <bool KV>
DI void q_block(const u16* __restrict__ x, const u16* __restrict__ w, char* __restrict__ ws, rsrc_t rs, int rt, int ct, char* smem, bool& initdone) {
  const int tid = threadIdx.x, lane = tid & 63, wv = __builtin_amdgcn_readfirstlane(tid >> 6);
  const int row0 = rt * 128;
  const int fr = lane & 15, g = lane >> 4;
  if (wv >= 4) {
    const int lw = wv - 4;
    const u16* xs[4]; const u16* ws_[5];
#pragma unroll
    for (int i = 0; i < 4; ++i) { int pc = lw * 4 + i; int row = 8 * pc + (lane >> 3); int ch = (lane & 7) ^ ((row >> 1) & 7); xs[i] = x + (size_t)(row0 + row) * D + 8 * ch; }
#pragma unroll
    for (int i = 0; i < 5; ++i) {
      int pc = lw * 5 + i; int sb = 4 * pc + (lane >> 4); int kb = sb / 10, nb = sb % 10; int row = ((lane >> 1) & 7) ^ (4 * (kb & 1)); int c = nb * 16 + 8 * (lane & 1);
      int gc;
      if (KV) { int wn2 = c / 80, cc = c % 80; gc = cc < 40 ? 640 + 80 * ct + 40 * wn2 + cc : 1280 + 80 * ct + 40 * wn2 + (cc - 40); }
      else gc = 160 * ct + c;
      ws_[i] = w + (size_t)(8 * kb + row) * 1920 + gc;
    }
    auto stage = [&](int kt) {
      char* b = smem + (kt & (QNBUF - 1)) * QSTAGE;
#pragma unroll
      for (int i = 0; i < 4; ++i) __builtin_amdgcn_global_load_lds((const u32*)(xs[i] + kt * 64), (LAS u32*)(b + (lw * 4 + i) * 1024), 16, 0, 0);
#pragma unroll
      for (int i = 0; i < 5; ++i) __builtin_amdgcn_global_load_lds((const u32*)(ws_[i] + (size_t)kt * 64 * 1920), (LAS u32*)(b + 16384 + (lw * 5 + i) * 1024), 16, 0, 0);
    };
#pragma unroll 1
    for (int i = 0; i < QNBUF; ++i) stage(i);
    asm volatile("s_waitcnt vmcnt(%0)" :: "n"((QNBUF - 1) * 9) : "memory");
    __builtin_amdgcn_s_barrier();
#pragma unroll 1
    for (int kt = 0; kt < 6; ++kt) {
      asm volatile("s_waitcnt vmcnt(18)" ::: "memory");
      __builtin_amdgcn_s_barrier();
      stage(kt + QNBUF);
    }
    asm volatile("s_waitcnt vmcnt(18)" ::: "memory"); __builtin_amdgcn_s_barrier();
    asm volatile("s_waitcnt vmcnt(9)" ::: "memory");  __builtin_amdgcn_s_barrier();
    asm volatile("s_waitcnt vmcnt(0)" ::: "memory");  __builtin_amdgcn_s_barrier();
    if (KV) { __builtin_amdgcn_s_barrier(); __builtin_amdgcn_s_barrier(); }
  } else {
    const int wm = wv >> 1, wn = wv & 1;
    f32x4 acc[4][5];
#pragma unroll
    for (int m = 0; m < 4; ++m)
#pragma unroll
      for (int n = 0; n < 5; ++n) acc[m][n] = (f32x4){0, 0, 0, 0};
    const u32 aoff0 = (wm * 64 + fr) * 128 + (((0 + g) ^ (fr >> 1)) * 16);
    const u32 aoff1 = (wm * 64 + fr) * 128 + (((4 + g) ^ (fr >> 1)) * 16);
    const u32 boff0 = 16384 + g * 2560 + wn * 1280 + ((0 ^ (4 * (g & 1))) + (fr >> 2)) * 32 + (lane & 3) * 8;
    const u32 boff1 = 16384 + g * 2560 + wn * 1280 + ((4 ^ (4 * (g & 1))) + (fr >> 2)) * 32 + (lane & 3) * 8;
    const u32 sbase = (u32)(size_t)smem;
    s16x8 af[2][4]; s16x4 bl[2][5], bh[2][5];
    u32 a0 = sbase + aoff0, a1 = sbase + aoff1, b0 = sbase + boff0, b1 = sbase + boff1;
    auto ldop = [&](int ks, int i) {
      if (i < 10) {
        const int n = i >> 1;
        if (i & 1) bh[ks][n] = __builtin_amdgcn_ds_read_tr16_b64_v4i16((LAS s16x4*)(b1 + ks * 10240 + n * 256));
        else       bl[ks][n] = __builtin_amdgcn_ds_read_tr16_b64_v4i16((LAS s16x4*)(b0 + ks * 10240 + n * 256));
      } else af[ks][i - 10] = *(const LAS s16x8*)((ks ? a1 : a0) + (i - 10) * 2048);
    };
    auto mma1 = [&](int sl, int i) {
      const int m = i / 5, n = i % 5;
      s16x8 bb = __builtin_shufflevector(bl[sl][n], bh[sl][n], 0, 1, 2, 3, 4, 5, 6, 7);
      acc[m][n] = KV ? __builtin_amdgcn_mfma_f32_16x16x32_bf16(af[sl][m], bb, acc[m][n], 0, 0, 0)
                     : __builtin_amdgcn_mfma_f32_16x16x32_bf16(bb, af[sl][m], acc[m][n], 0, 0, 0);
    };
    __builtin_amdgcn_s_barrier();
    TLS(15);
#pragma unroll
    for (int i = 0; i < 14; ++i) ldop(0, i);
    int cur = 0;
    unsigned long long fl = MAGIC;
    gu64* fp = (gu64*)(ws + WS_FLAG) + blockIdx.x * 8 + (lane & 7);
#pragma unroll 1
    for (int kt = 0; kt < 10; ++kt) {
      if (kt == 6 && wv == 0) asm volatile("global_load_dwordx2 %0, %1, off sc1" : "=v"(fl) : "v"(fp) : "memory");
      __builtin_amdgcn_sched_barrier(0);
#pragma unroll
      for (int i = 0; i < 20; ++i) {
        mma1(0, i);
        if (i < 14) ldop(1, i);
        __builtin_amdgcn_sched_barrier(0);
      }
      cur = (cur + 1) & (QNBUF - 1);
      const u32 nb = sbase + cur * QSTAGE;
      a0 = nb + aoff0; a1 = nb + aoff1; b0 = nb + boff0; b1 = nb + boff1;
      __builtin_amdgcn_s_waitcnt(0xC07F);
      if (kt < 9) __builtin_amdgcn_s_barrier();
      __builtin_amdgcn_sched_barrier(0);
#pragma unroll
      for (int i = 0; i < 20; ++i) {
        mma1(1, i);
        if (i < 14) ldop(0, i);
        __builtin_amdgcn_sched_barrier(0);
      }
    }
    if (wv == 0) {
      asm volatile("s_waitcnt vmcnt(0)" : "+v"(fl) :: "memory");
      if (__all(fl == MAGIC)) { if (lane < 8) __hip_atomic_store(fp, 0ull, RLX_AGENT); }
      else if (lane == 0) { bool d = false; wait_init_t0(ws, d); }
    }
    TLS(14);
    if (!KV) {
      char* pq = ws + WS_PQ;
#pragma unroll
      for (int m = 0; m < 4; ++m)
#pragma unroll
        for (int n = 0; n < 5; ++n) {
          u32x2 o = {phi_pair(acc[m][n][0], acc[m][n][1]), phi_pair(acc[m][n][2], acc[m][n][3])};
          const int col = 160 * ct + wn * 80 + n * 16 + 4 * g; const int h = col / 20, q = (col % 20) >> 2;
          st8_wt(pq + 2 * (((size_t)((rt * 8 + wm * 4 + m) * 32 + h) * 5 + q) * 64 + fr * 4), o);
        }
    } else {
      u32x2 pk[4][5];
      const bool isk = fr < 8;
#pragma unroll
      for (int m = 0; m < 4; ++m)
#pragma unroll
        for (int n = 0; n < 5; ++n) {
          if (n <= 2) { pk[m][n].x = phi_pair(acc[m][n][0], acc[m][n][1]); pk[m][n].y = phi_pair(acc[m][n][2], acc[m][n][3]); }
          if (n >= 2) {
            u32 v0 = pk_bf16(acc[m][n][0], acc[m][n][1]), v1 = pk_bf16(acc[m][n][2], acc[m][n][3]);
            if (n > 2) { pk[m][n].x = v0; pk[m][n].y = v1; } else { pk[m][n].x = isk ? pk[m][n].x : v0; pk[m][n].y = isk ? pk[m][n].y : v1; }
          }
        }
      f32x4 kv[10];
#pragma unroll
      for (int i = 0; i < 10; ++i) kv[i] = (f32x4){0, 0, 0, 0};
      const s16x8 ones = {0x3f80, 0x3f80, 0x3f80, 0x3f80, 0x3f80, 0x3f80, 0x3f80, 0x3f80};
#pragma unroll
      for (int u = 0; u < 2; ++u) {
        s16x8 f[5];
#pragma unroll
        for (int n = 0; n < 5; ++n) f[n] = __builtin_bit_cast(s16x8, (u32x4){pk[2 * u][n].x, pk[2 * u][n].y, pk[2 * u + 1][n].x, pk[2 * u + 1][n].y});
        kv[0] = __builtin_amdgcn_mfma_f32_16x16x32_bf16(f[0], f[2], kv[0], 0, 0, 0);
        kv[1] = __builtin_amdgcn_mfma_f32_16x16x32_bf16(f[0], f[3], kv[1], 0, 0, 0);
        kv[2] = __builtin_amdgcn_mfma_f32_16x16x32_bf16(f[1], f[2], kv[2], 0, 0, 0);
        kv[3] = __builtin_amdgcn_mfma_f32_16x16x32_bf16(f[1], f[3], kv[3], 0, 0, 0);
        kv[4] = __builtin_amdgcn_mfma_f32_16x16x32_bf16(f[1], f[4], kv[4], 0, 0, 0);
        kv[5] = __builtin_amdgcn_mfma_f32_16x16x32_bf16(f[2], f[3], kv[5], 0, 0, 0);
        kv[6] = __builtin_amdgcn_mfma_f32_16x16x32_bf16(f[2], f[4], kv[6], 0, 0, 0);
        kv[7] = __builtin_amdgcn_mfma_f32_16x16x32_bf16(f[0], ones, kv[7], 0, 0, 0);
        kv[8] = __builtin_amdgcn_mfma_f32_16x16x32_bf16(f[1], ones, kv[8], 0, 0, 0);
        kv[9] = __builtin_amdgcn_mfma_f32_16x16x32_bf16(f[2], ones, kv[9], 0, 0, 0);
      }
      __builtin_amdgcn_s_barrier();
      f32x4* red = (f32x4*)smem + (wm * 2 + wn) * 640 + lane;
#pragma unroll
      for (int i = 0; i < 10; ++i) red[i * 64] = kv[i];
      __builtin_amdgcn_s_waitcnt(0xC07F);
      __builtin_amdgcn_s_barrier();
    }
  }
  initdone = true;
  if (KV) {
    const float* raw = (const float*)smem;
    float* acc = (float*)(ws + WS_ZERO) + (size_t)ct * 4096 + tid;
#pragma unroll
    for (int it = 0; it < 8; ++it) {
      const int e0 = it * 512 + tid;
      const int hl4 = it >> 1, tile = it & 1, lp = (e0 >> 3) & 63, j = e0 & 7;
      const int row = lp & 15, gk = lp >> 4, gg = row >> 2, r = row & 3;
      const int wn = hl4 >> 1, hl = hl4 & 1;
      const int dd = 8 * gk + j;
      if ((tile == 0 || r < 2) && dd < 20) {
        const int d = dd + 20 * hl, dt = d >> 4;
        const int e = (tile == 0 ? 5 * gg + r : 5 * gg + 3 + r) + 20 * hl + 40, et = e >> 4;
        int t, ln;
        if (tile == 0 && r == 3) { t = 7 + dt; ln = ((d & 15) >> 2) * 16; }
        else { t = dt == 0 ? et - 2 : (dt == 1 ? et : et + 2); ln = ((d & 15) >> 2) * 16 + (e & 15); }
        const int idx = (wn * 640 + t * 64 + ln) * 4 + (d & 3);
        const float v = raw[idx] + raw[2 * 640 * 4 + idx];
        __hip_atomic_fetch_add((GAS float*)(acc + it * 512), v, RLX_AGENT);
      }
    }
    TLS(1);
    publish8(ws, CNT_KV, initdone);
  } else {
    TLS(1);
    publish(ws, CNT_PQ + rt, initdone);
  }
}

DI void aux_block(const u16* __restrict__ u, const u16* __restrict__ wo, char* __restrict__ ws, rsrc_t rs, int ab, char* smem, bool& initdone) {
  const int tid = threadIdx.x, lane = tid & 63, wv = __builtin_amdgcn_readfirstlane(tid >> 6);
  char* reg = smem + wv * KG_REG;
  if (wv < 5) {
    const int tile = ab * 5 + wv; const int rb = tile / 10, cb = tile % 10;
    u16* t = (u16*)reg;
#pragma unroll
    for (int i = 0; i < 8; ++i) {
      const int row = i * 8 + (lane >> 3), ch = lane & 7;
      u32x4 v = *(const u32x4*)(u + (size_t)(rb * 64 + row) * D + cb * 64 + ch * 8);
      u32* p = (u32*)(t + row * 66 + ch * 8);
      p[0] = v.x; p[1] = v.y; p[2] = v.z; p[3] = v.w;
    }
    __builtin_amdgcn_wave_barrier();
#pragma unroll
    for (int i = 0; i < 8; ++i) {
      const int c = i * 8 + (lane >> 3), r8 = lane & 7;
      u32 o[4];
#pragma unroll
      for (int k = 0; k < 4; ++k) o[k] = (u32)t[(r8 * 8 + 2 * k) * 66 + c] | ((u32)t[(r8 * 8 + 2 * k + 1) * 66 + c] << 16);
      st16_wt(rs, WS_UT + ((cb * 64 + c) * S + rb * 64 + r8 * 8) * 2, (u32x4){o[0], o[1], o[2], o[3]});
    }
    __builtin_amdgcn_wave_barrier();
  } else {
#pragma unroll
    for (int it = 0; it < 5; ++it) {
      const int uu = (wv - 5) * 64 + lane + 192 * it;
      if (uu < 800) {
        const int U = ab * 800 + uu; const int img = U / 1280, un = U % 1280; const int ct = img / 10, kt = img % 10;
        const int sb = un >> 4, kb = sb / 10, nb = sb % 10, j = ((un >> 1) & 7) ^ (4 * (kb & 1));
        const int T = 2 * kt + (kb >> 2), G = T / 5, tt = T % 5, ii = 8 * tt + j, hh = ii / 5, r = ii % 5;
        const int k = 160 * G + 20 * hh + 5 * (kb & 3) + r;
        st16_wt(rs, WS_WOP + U * 16, *(const u32x4*)(wo + (size_t)k * D + 160 * ct + nb * 16 + 8 * (un & 1)));
      }
    }
  }
  TLS(1);
  publish8(ws, CNT_T, initdone);
}

constexpr int UC_STRIDE = 4160, UCH = 4 * UC_STRIDE, KP_STRIDE = 1280, KPCH = 4 * KP_STRIDE;
constexpr int UC0 = 4 * KG_REG, PART0 = UC0 + 4 * UCH, KC0 = PART0 + 12 * 2592, RED0 = KC0 + 4 * 1312;
constexpr int B_LDS = RED0 + 128;
constexpr int PSPLIT = 22;
DI void b_block(const KGP& kp, char* __restrict__ ws, rsrc_t rs, const u16* __restrict__ dsk_, u16* __restrict__ out, int cb, char* smem, bool& initdone) {
  const int tid = threadIdx.x, lane = tid & 63, wv = __builtin_amdgcn_readfirstlane(tid >> 6), ch = wv >> 1, hf = wv & 1;
  const int d0 = cb * 4;
  float* red = (float*)(smem + RED0);
  float* part = (float*)(smem + PART0);
  KGL l0, l1;
  kgen_load(kp, d0 + wv / 3, wv % 3, l0);
  kgen_load(kp, d0 + (8 + (wv & 3)) / 3, (8 + (wv & 3)) % 3, l1);
  float dk[4];
#pragma unroll
  for (int c = 0; c < 4; ++c) dk[c] = bf2f(dsk_[d0 + c]);
  u32x4 uv[4];
  unsigned tc = 0;
  gu32* tcp = cnt_ptr(ws, CNT_T + (blockIdx.x & 7));
  if (wv >= 4) asm volatile("global_load_dword %0, %1, off sc1" : "=v"(tc) : "v"(tcp) : "memory");
  {
    float ks = kgen_task(l0, wv % 3, smem + wv * KG_REG, part + wv * 648, [&]() {
      if (wv >= 4) {
        asm volatile("s_waitcnt vmcnt(0)" : "+v"(tc) :: "memory");
        if (tc < 64) { if (lane == 0) wait_cnt_t0(ws, CNT_T + (blockIdx.x & 7), 64); __builtin_amdgcn_wave_barrier(); }
#pragma unroll
        for (int i = 0; i < 4; ++i) uv[i] = ld16_sc(rs, WS_UT + ((d0 + wv - 4) * S + 8 * (lane + 64 * i)) * 2);
      }
    });
    if (lane == 0 && wv % 3 == 0) red[8 + wv / 3] = ks;
  }
  __syncthreads();
  TLS(3);
  if (wv < 4) {
    float ks = kgen_task(l1, (8 + wv) % 3, smem + wv * KG_REG, part + (8 + wv) * 648, []() {});
    if (lane == 0 && wv == 1) red[8 + 3] = ks;
  } else {
    const int c = wv - 4;
    char* ur = smem + UC0 + c * UCH;
    float m = 0.f;
#pragma unroll
    for (int i = 0; i < 4; ++i)
#pragma unroll
      for (int k = 0; k < 4; ++k) m = fmaxf(m, fmaxf(fabsf(lo16(uv[i][k])), fabsf(hi16(uv[i][k]))));
#pragma unroll
    for (int o = 32; o >= 1; o >>= 1) m = fmaxf(m, __shfl_xor(m, o));
    int e = (int)((__float_as_uint(m) >> 23) & 0xff);
    e = e < 30 ? 30 : (e > 220 ? 220 : e);
    const float us = __uint_as_float((u32)(127 + 140 - e) << 23);
    if (lane == 0) red[c] = __uint_as_float((u32)(e - 13) << 23);
#pragma unroll
    for (int i = 0; i < 4; ++i) {
      const int q = lane + 64 * i;
      u32x4 o = {pk_f16(hi16(uv[i].w) * us, lo16(uv[i].w) * us), pk_f16(hi16(uv[i].z) * us, lo16(uv[i].z) * us),
                 pk_f16(hi16(uv[i].y) * us, lo16(uv[i].y) * us), pk_f16(hi16(uv[i].x) * us, lo16(uv[i].x) * us)};
      *(u32x4*)(ur + 2 * (2040 - 8 * q)) = o;
    }
    if (lane < 4) *(u32x4*)(ur + 4096 + 16 * lane) = (u32x4){0, 0, 0, 0};
    __builtin_amdgcn_wave_barrier();
#pragma unroll
    for (int k = 0; k < 5; ++k) {
      const int q = lane + 64 * k;
      if (q < 260) {
        u32x4 a = *(const u32x4*)(ur + 16 * q);
        u32x2 b = *(const u32x2*)(ur + 16 * q + 16);
        u32x4 c1 = {__builtin_amdgcn_alignbit(a.y, a.x, 16), __builtin_amdgcn_alignbit(a.z, a.y, 16), __builtin_amdgcn_alignbit(a.w, a.z, 16), __builtin_amdgcn_alignbit(b.x, a.w, 16)};
        u32x4 c2 = {a.y, a.z, a.w, b.x};
        u32x4 c3 = {c1.y, c1.z, c1.w, __builtin_amdgcn_alignbit(b.y, b.x, 16)};
        *(u32x4*)(ur + 1 * UC_STRIDE + 16 * q) = c1;
        *(u32x4*)(ur + 2 * UC_STRIDE + 16 * q) = c2;
        *(u32x4*)(ur + 3 * UC_STRIDE + 16 * q) = c3;
      }
    }
  }
  __syncthreads();
  TLS(11);
  {
    u16* kc = (u16*)(smem + KC0 + ch * 1312);
    const float* p0 = part + ch * 3 * 648;
#pragma unroll
    for (int i = 0; i < 2; ++i) {
      const int ci = hf * 81 + 64 * i + lane;
      if (lane + 64 * i < 81) {
        f32x4 v = *(const f32x4*)(p0 + ci * 4) + *(const f32x4*)(p0 + 648 + ci * 4) + *(const f32x4*)(p0 + 1296 + ci * 4);
        *(u32x2*)(kc + ci * 4) = (u32x2){pk_f16(v[0] * 128.f, v[1] * 128.f), pk_f16(v[2] * 128.f, v[3] * 128.f)};
      }
    }
  }
  __syncthreads();
  {
    const u16* kc = (const u16*)(smem + KC0 + ch * 1312);
    char* kpl = smem + ch * KPCH;
    if (hf == 1 && lane < 60) *(u32x4*)(kpl + (lane & 3) * KP_STRIDE + (lane >> 2) * 16) = (u32x4){0, 0, 0, 0};
#pragma unroll
    for (int k = 0; k < 2; ++k) {
      const int q = hf + 2 * (lane + 64 * k);
      u32 v[8];
#pragma unroll
      for (int j8 = 0; j8 < 8; ++j8) {
        int j = 8 * q + j8;
        u32 fb = __float_as_uint((float)j); fb += 0x7fffu + ((fb >> 16) & 1u);
        int idx = j < 256 ? j : 256 + (int)((fb >> 16) - 0x4380u);
        v[j8] = kc[idx];
      }
      u32x4 o = {v[0] | (v[1] << 16), v[2] | (v[3] << 16), v[4] | (v[5] << 16), v[6] | (v[7] << 16)};
      *(u32x4*)(kpl + (q & 3) * KP_STRIDE + ((q >> 2) + 15) * 16) = o;
    }
  }
  __syncthreads();
  TLS(12);
  f32x4 acc[4][2];
#pragma unroll
  for (int t = 0; t < 4; ++t) { acc[t][0] = (f32x4){0, 0, 0, 0}; acc[t][1] = (f32x4){0, 0, 0, 0}; }
  const int b = lane & 15, g = lane >> 4;
  char* ureg = smem + UC0 + ch * UCH;
  {
    const int cpy = (3 - b) & 3;
    const char* pa0 = ureg + cpy * UC_STRIDE + 2 * (2044 - 4 * (b >> 2) + 8 * g);
    const char* pb0 = smem + ch * KPCH + g * KP_STRIDE + 16 * (b + 15);
    typedef const volatile LAS u32x2* lp2;
    typedef const volatile LAS u32x4* lp4;
    const u32 abase = (u32)(size_t)(pa0 - 64 * 63 - 32);
    const u32 bbase = (u32)(size_t)(pb0 - 16 * 63);
    constexpr int DP = 2;
    u32x2 Ab[DP + 1][4]; u32x4 Bb[DP + 1][4];
    auto run = [&](auto P0c, auto P1c) {
      constexpr int P0 = decltype(P0c)::value, P1 = decltype(P1c)::value;
      auto issue = [&](int p) {
        const int sl = p % (DP + 1);
        const u32 oa = 64 * (63 - p);
        Ab[sl][0] = *(lp2)(abase + oa + 32); Ab[sl][1] = *(lp2)(abase + oa + 40);
        Ab[sl][2] = *(lp2)(abase + oa);      Ab[sl][3] = *(lp2)(abase + oa + 8);
#pragma unroll
        for (int t = 0; t < 4; ++t) if (t >= (p >> 4)) Bb[sl][t] = *(lp4)(bbase + 16 * (63 - p) + 256 * t);
      };
#pragma unroll
      for (int p = P0; p < P0 + DP; ++p) issue(p);
#pragma unroll
      for (int p = P0; p < P1; ++p) {
        if (p + DP < P1) issue(p + DP);
        __builtin_amdgcn_sched_barrier(0);
        const int sl = p % (DP + 1);
        h8 A0 = __builtin_bit_cast(h8, (u32x4){Ab[sl][0].x, Ab[sl][0].y, Ab[sl][1].x, Ab[sl][1].y});
        h8 A1 = __builtin_bit_cast(h8, (u32x4){Ab[sl][2].x, Ab[sl][2].y, Ab[sl][3].x, Ab[sl][3].y});
#pragma unroll
        for (int t = 0; t < 4; ++t) if (t >= (p >> 4)) {
          h8 B = __builtin_bit_cast(h8, Bb[sl][t]);
          acc[t][0] = __builtin_amdgcn_mfma_f32_16x16x32_f16(A0, B, acc[t][0], 0, 0, 0);
          acc[t][1] = __builtin_amdgcn_mfma_f32_16x16x32_f16(A1, B, acc[t][1], 0, 0, 0);
        }
        __builtin_amdgcn_sched_barrier(0);
      }
    };
    if (hf == 0) run(std::integral_constant<int, 0>{}, std::integral_constant<int, PSPLIT>{});
    else run(std::integral_constant<int, PSPLIT>{}, std::integral_constant<int, 64>{});
  }
  TLS(13);
  f32x4* xch = (f32x4*)(smem + PART0 + ch * 6144) + lane;
  if (hf == 1) { xch[0 * 64] = acc[1][0]; xch[1 * 64] = acc[1][1]; }
  else { xch[2 * 64] = acc[2][0]; xch[3 * 64] = acc[2][1]; xch[4 * 64] = acc[3][0]; xch[5 * 64] = acc[3][1]; }
  __syncthreads();
  {
    const float ysc = red[8 + ch] * red[ch];
    char* yb = smem + ch * 4096;
    f32x4 a[2][2];
    if (hf == 0) { a[0][0] = acc[0][0]; a[0][1] = acc[0][1]; a[1][0] = acc[1][0] + xch[0 * 64]; a[1][1] = acc[1][1] + xch[1 * 64]; }
    else { a[0][0] = acc[2][0] + xch[2 * 64]; a[0][1] = acc[2][1] + xch[3 * 64]; a[1][0] = acc[3][0] + xch[4 * 64]; a[1][1] = acc[3][1] + xch[5 * 64]; }
#pragma unroll
    for (int t = 0; t < 2; ++t)
#pragma unroll
      for (int r = 0; r < 2; ++r) {
        u32x2 o = {pk_bf16(a[t][r][0] * ysc, a[t][r][1] * ysc), pk_bf16(a[t][r][2] * ysc, a[t][r][3] * ysc)};
        *(u32x2*)(yb + 2 * (32 * (16 * (2 * hf + t) + b) + 16 * r + 4 * g)) = o;
      }
  }
  __syncthreads();
  {
    u32x2 yv[4], uu[4]; float iu[4];
#pragma unroll
    for (int c = 0; c < 4; ++c) {
      yv[c] = *(const u32x2*)(smem + c * 4096 + 8 * tid);
      uu[c] = *(const u32x2*)(smem + UC0 + c * UCH + 2 * (2044 - 4 * tid));
      iu[c] = red[c];
    }
#pragma unroll
    for (int i = 0; i < 4; ++i) {
      float r[4];
#pragma unroll
      for (int c = 0; c < 4; ++c) {
        u32 yw = yv[c][i >> 1]; float y = (i & 1) ? hi16(yw) : lo16(yw);
        const u32 uw = (i < 2) ? uu[c].y : uu[c].x; const u16 ub = (i & 1) ? (u16)uw : (u16)(uw >> 16); float uf = (float)__builtin_bit_cast(_Float16, ub) * iu[c];
        r[c] = y + rbf(uf * dk[c]);
      }
      u32x2 o = {pk_bf16(r[0], r[1]), pk_bf16(r[2], r[3])};
      *(u32x2*)(out + (size_t)(4 * tid + i) * 1280 + 640 + d0) = o;
    }
  }
  __builtin_amdgcn_s_waitcnt(0xC07F);
  __builtin_amdgcn_s_barrier();
}

constexpr int OSTAGE = 20480, ONBUF = 4, O_OFF_W = 81920;
DI void o_block(char* __restrict__ ws, rsrc_t rs, u16* __restrict__ out, int rt, int ct, char* smem, bool& initdone) {
  const int tid = threadIdx.x, lane = tid & 63, wv = __builtin_amdgcn_readfirstlane(tid >> 6);
  const int row0 = rt * 64, fr = lane & 15, g = lane >> 4;
  const int lw = wv - 4;
  if (tid == 0) { wait_init_t0(ws, initdone); wait_cnt_t0(ws, CNT_T + (blockIdx.x & 7), 64); }
  acquire_all();
  const char* wsrc = ws + WS_WOP + (size_t)ct * 10 * OSTAGE + lw * 5120 + lane * 16;
  auto stage = [&](int kt) {
    char* b = smem + O_OFF_W + (kt & (ONBUF - 1)) * OSTAGE + lw * 5120;
#pragma unroll
    for (int i = 0; i < 5; ++i) __builtin_amdgcn_global_load_lds((const u32*)(wsrc + kt * OSTAGE + i * 1024), (LAS u32*)(b + i * 1024), 16, 0, 0);
  };
  if (wv >= 4) {
#pragma unroll 1
    for (int i = 0; i < ONBUF; ++i) stage(i);
  }
  if (tid == 0) wait_cnt_t0(ws, CNT_PQ + (rt >> 1), 4);
  asm volatile("s_waitcnt lgkmcnt(0)" ::: "memory");
  __builtin_amdgcn_s_barrier();
  const char* pq = ws + WS_PQ;
  const char* zp = ws + WS_ZERO + CNT_OFF + ZPAD * 128;
  u32x4 kr[4][2][2]; u32x2 pa[4][4], pb[4][4];
#pragma unroll
  for (int hh = 0; hh < 4; ++hh)
#pragma unroll
    for (int st = 0; st < 4; ++st) {
      const char* p = pq + 2 * (((size_t)((rt * 4 + st) * 32 + 4 * wv + hh) * 5) * 64 + fr * 4);
      pa[hh][st] = ld8_sc(g < 3 ? p + 2 * g * 128 : zp);
      pb[hh][st] = ld8_sc(g < 2 ? p + (2 * g + 1) * 128 : zp);
    }
  if (tid == 0) wait_cnt_t0(ws, CNT_KV + (blockIdx.x & 7), 128);
  TLS(6);
  asm volatile("s_waitcnt lgkmcnt(0)" ::: "memory");
  __builtin_amdgcn_s_barrier();
  TLS(7);
#pragma unroll
  for (int hh = 0; hh < 4; ++hh)
#pragma unroll
    for (int tl = 0; tl < 2; ++tl) {
      kr[hh][tl][0] = ld16_sc(rs, WS_ZERO + (((4 * wv + hh) * 2 + tl) * 64 + lane) * 32);
      kr[hh][tl][1] = ld16_sc(rs, WS_ZERO + (((4 * wv + hh) * 2 + tl) * 64 + lane) * 32 + 16);
    }
  __builtin_amdgcn_sched_barrier(0);
  {
    s16x8 kf[4][2];
#pragma unroll
    for (int hh = 0; hh < 4; ++hh)
#pragma unroll
      for (int tl = 0; tl < 2; ++tl) {
        f32x4 a = __builtin_bit_cast(f32x4, kr[hh][tl][0]), b = __builtin_bit_cast(f32x4, kr[hh][tl][1]);
        kf[hh][tl] = __builtin_bit_cast(s16x8, (u32x4){pk_bf16(a[0], a[1]), pk_bf16(a[2], a[3]), pk_bf16(b[0], b[1]), pk_bf16(b[2], b[3])});
      }
    asm volatile("" :: "v"(kf[3][1]), "v"(pa[3][3]), "v"(pb[3][3]));
    TLS(10);
    const int G = wv >> 1, half = wv & 1;
#pragma unroll
    for (int st = 0; st < 4; ++st) {
      float vals[20];
#pragma unroll
      for (int hh = 0; hh < 4; ++hh) {
        s16x8 pf = __builtin_bit_cast(s16x8, (u32x4){pa[hh][st].x, pa[hh][st].y, pb[hh][st].x, pb[hh][st].y});
        f32x4 z4 = {0, 0, 0, 0};
        f32x4 t0 = __builtin_amdgcn_mfma_f32_16x16x32_bf16(kf[hh][0], pf, z4, 0, 0, 0);
        f32x4 t1 = __builtin_amdgcn_mfma_f32_16x16x32_bf16(kf[hh][1], pf, z4, 0, 0, 0);
        float den = rbf(rbf(t0[3]) + 1e-6f);
        float rd = __builtin_amdgcn_rcpf(den);
        vals[5 * hh + 0] = t0[0] * rd; vals[5 * hh + 1] = t0[1] * rd; vals[5 * hh + 2] = t0[2] * rd;
        vals[5 * hh + 3] = t1[0] * rd; vals[5 * hh + 4] = t1[1] * rd;
      }
#pragma unroll
      for (int i = 0; i < 5; ++i) {
        const int byte = 40 * half + 8 * i;
        const int T = 5 * G + (byte >> 4);
        *(u32x2*)(smem + (st * 20 + T) * 1024 + lane * 16 + (byte & 8)) = (u32x2){pk_bf16(vals[4 * i], vals[4 * i + 1]), pk_bf16(vals[4 * i + 2], vals[4 * i + 3])};
      }
    }
  }
  if (wv >= 4) {
    asm volatile("s_waitcnt vmcnt(0) lgkmcnt(0)" ::: "memory");
    __builtin_amdgcn_s_barrier();
#pragma unroll 1
    for (int kt = 0; kt < 6; ++kt) {
      asm volatile("s_waitcnt vmcnt(10)" ::: "memory");
      __builtin_amdgcn_s_barrier();
      stage(kt + ONBUF);
    }
    asm volatile("s_waitcnt vmcnt(10)" ::: "memory"); __builtin_amdgcn_s_barrier();
    asm volatile("s_waitcnt vmcnt(5)" ::: "memory");  __builtin_amdgcn_s_barrier();
    asm volatile("s_waitcnt vmcnt(0)" ::: "memory");  __builtin_amdgcn_s_barrier();
    return;
  }
  const int wm = wv >> 1, wn = wv & 1;
  f32x4 acc[2][5];
#pragma unroll
  for (int m = 0; m < 2; ++m)
#pragma unroll
    for (int n = 0; n < 5; ++n) acc[m][n] = (f32x4){0, 0, 0, 0};
  const u32 sbase = (u32)(size_t)smem;
  const u32 boff0 = O_OFF_W + g * 2560 + wn * 1280 + ((0 ^ (4 * (g & 1))) + (fr >> 2)) * 32 + (lane & 3) * 8;
  const u32 boff1 = O_OFF_W + g * 2560 + wn * 1280 + ((4 ^ (4 * (g & 1))) + (fr >> 2)) * 32 + (lane & 3) * 8;
  u32 aad = sbase + wm * 2 * 20 * 1024 + lane * 16;
  u32 b0 = sbase + boff0, b1 = sbase + boff1;
  s16x8 af[2][2]; s16x4 bl[2][5], bh[2][5];
  auto ldop = [&](int ks, int i) {
    if (i < 10) {
      const int n = i >> 1;
      if (i & 1) bh[ks][n] = __builtin_amdgcn_ds_read_tr16_b64_v4i16((LAS s16x4*)(b1 + ks * 10240 + n * 256));
      else       bl[ks][n] = __builtin_amdgcn_ds_read_tr16_b64_v4i16((LAS s16x4*)(b0 + ks * 10240 + n * 256));
    } else af[ks][i - 10] = *(const LAS s16x8*)(aad + ((i - 10) * 20 + ks) * 1024);
  };
  auto mma1 = [&](int sl, int i) {
    const int m = i / 5, n = i % 5;
    s16x8 bb = __builtin_shufflevector(bl[sl][n], bh[sl][n], 0, 1, 2, 3, 4, 5, 6, 7);
    acc[m][n] = __builtin_amdgcn_mfma_f32_16x16x32_bf16(bb, af[sl][m], acc[m][n], 0, 0, 0);
  };
  __builtin_amdgcn_s_waitcnt(0xC07F);
  __builtin_amdgcn_s_barrier();
  TLS(9);
#pragma unroll
  for (int i = 0; i < 12; ++i) ldop(0, i);
  int cur = 0;
#pragma unroll 1
  for (int kt = 0; kt < 10; ++kt) {
    __builtin_amdgcn_sched_barrier(0);
#pragma unroll
    for (int i = 0; i < 10; ++i) {
      mma1(0, i);
      ldop(1, i); if (i < 2) ldop(1, 10 + i);
      __builtin_amdgcn_sched_barrier(0);
    }
    cur = (cur + 1) & (ONBUF - 1);
    b0 = sbase + cur * OSTAGE + boff0; b1 = sbase + cur * OSTAGE + boff1;
    aad += (kt < 9) ? 2048 : 0;
    __builtin_amdgcn_s_waitcnt(0xC07F);
    if (kt < 9) __builtin_amdgcn_s_barrier();
    __builtin_amdgcn_sched_barrier(0);
#pragma unroll
    for (int i = 0; i < 10; ++i) {
      mma1(1, i);
      ldop(0, i); if (i < 2) ldop(0, 10 + i);
      __builtin_amdgcn_sched_barrier(0);
    }
  }
#pragma unroll
  for (int m = 0; m < 2; ++m)
#pragma unroll
    for (int n = 0; n < 5; ++n) {
      u32x2 o = {pk_bf16(acc[m][n][0], acc[m][n][1]), pk_bf16(acc[m][n][2], acc[m][n][3])};
      *(u32x2*)(out + (size_t)(row0 + wm * 32 + m * 16 + fr) * 1280 + 160 * ct + wn * 80 + n * 16 + 4 * g) = o;
    }
}

struct P { const u16 *x, *w, *wo, *u, *lar, *aim, *cr, *ci, *ldt, *dsk; u16* out; char* ws; };
__global__ __launch_bounds__(512, 2) void kmain(P p) {
  extern __shared__ __attribute__((aligned(16))) char smem[];
  const int b = blockIdx.x, tid = threadIdx.x;
  char* ws = p.ws;
  const rsrc_t rs = __builtin_amdgcn_make_buffer_rsrc((void*)ws, 0, WS_SPAN, 0x00020000);
  bool initdone = false;
  TLS(0);
  if (b >= 248) {
    const int part = b - 248;
    for (int i = tid; i < ZERO_BYTES / 128; i += 512) st16_wt(rs, WS_ZERO + part * (ZERO_BYTES / 8) + i * 16, (u32x4){0, 0, 0, 0});
    asm volatile("s_waitcnt vmcnt(0)" ::: "memory");
    __syncthreads();
    if (tid < 256) __hip_atomic_store((gu64*)(ws + WS_FLAG) + tid * 8 + part, MAGIC, RLX_AGENT);
  }
  const KGP kp = {p.lar, p.aim, p.cr, p.ci, p.ldt};
  int bt0 = -1, bt1 = -1, o_task = -1;
  if (b < 192) {
    const int xcd = b & 7, j = b >> 3;
    const int rt = (xcd >> 1) * 4 + (j & 3), c6 = j >> 2;
    if (c6 < 4) { q_block<true>(p.x, p.w, ws, rs, rt, (xcd & 1) * 4 + c6, smem, initdone); if (j < 12) o_task = xcd * 16 + j; else bt0 = xcd * 20 + 16 + (j - 12); }
    else { q_block<false>(p.x, p.w, ws, rs, rt, (xcd & 1) * 2 + (c6 - 4), smem, initdone); bt0 = xcd * 20 + 8 + (j - 16); }
  } else {
    const int ab = b - 192;
    aux_block(p.u, p.wo, ws, rs, ab, smem, initdone);
    { const int xcd = ab & 7, i = ab >> 3; bt0 = xcd * 20 + i; if (i < 4) o_task = xcd * 16 + 12 + i; }
  }
  TLS(2);
  __syncthreads();
#pragma unroll 1
  for (int it = 0; it < 2; ++it) {
    const int t = it == 0 ? bt0 : bt1;
    if (t >= 0) b_block(kp, ws, rs, p.dsk, p.out, t, smem, initdone);
  }
  TLS(5);
  if (o_task >= 0) o_block(ws, rs, p.out, o_task >> 2, o_task & 3, smem, initdone);
  TLS(8);
}

extern "C" void kernel_launch(void* const* d_in, const int* in_sizes, int n_in, void* d_out, int out_size, void* d_ws, size_t ws_size, hipStream_t stream) {
  constexpr int LDS = O_OFF_W + ONBUF * OSTAGE;
  static_assert(QNBUF * QSTAGE <= LDS && 8 * KG_REG <= LDS && B_LDS <= LDS, "LDS");
  (void)hipFuncSetAttribute((const void*)kmain, hipFuncAttributeMaxDynamicSharedMemorySize, LDS);
  P p = {(const u16*)d_in[0], (const u16*)d_in[1], (const u16*)d_in[2], (const u16*)d_in[3], (const u16*)d_in[4], (const u16*)d_in[5], (const u16*)d_in[6], (const u16*)d_in[7], (const u16*)d_in[8], (const u16*)d_in[9], (u16*)d_out, (char*)d_ws};
  kmain<<<256, 512, LDS, stream>>>(p);
}
```

```cpp
#include <hip/hip_runtime.h>
#include <hip/hip_bf16.h>
#include <stdint.h>
#include <type_traits>
typedef unsigned short u16;
typedef unsigned int u32;
typedef float f32x2 __attribute__((ext_vector_type(2)));
typedef float f32x4 __attribute__((ext_vector_type(4)));
typedef float f32x16 __attribute__((ext_vector_type(16)));
typedef short s16x4 __attribute__((ext_vector_type(4)));
typedef short s16x8 __attribute__((ext_vector_type(8)));
typedef _Float16 h8 __attribute__((ext_vector_type(8)));
typedef _Float16 h2 __attribute__((ext_vector_type(2)));
typedef __bf16 bf16x2 __attribute__((ext_vector_type(2)));
typedef u32 u32x2 __attribute__((ext_vector_type(2)));
typedef u32 u32x4 __attribute__((ext_vector_type(4)));
#define DI __device__ __forceinline__
#define LAS __attribute__((address_space(3)))
#define GAS __attribute__((address_space(1)))
typedef GAS unsigned gu32; typedef GAS unsigned long long gu64;
#define RLX_AGENT __ATOMIC_RELAXED, __HIP_MEMORY_SCOPE_AGENT
DI float bf2f(u16 x) { return __uint_as_float(((u32)x) << 16); }
DI u32 pk_bf16(float a, float b) { bf16x2 r = __builtin_convertvector((f32x2){a, b}, bf16x2); return __builtin_bit_cast(u32, r); }
DI u32 pk_f16(float a, float b) { h2 r = __builtin_convertvector((f32x2){a, b}, h2); return __builtin_bit_cast(u32, r); }
DI float lo16(u32 x) { return __uint_as_float(x << 16); }
DI float hi16(u32 x) { return __uint_as_float(x & 0xffff0000u); }
DI float rbf(float f) { return lo16(pk_bf16(f, 0.f)); }
DI u32 phi_pair(float a, float b) {
  u32 t = pk_bf16(a, b);
  float ta = lo16(t), tb = hi16(t);
  float ea = __builtin_amdgcn_fmed3f(__builtin_amdgcn_exp2f(ta * 1.4426950408889634f), 0.f, 1.f);
  float eb = __builtin_amdgcn_fmed3f(__builtin_amdgcn_exp2f(tb * 1.4426950408889634f), 0.f, 1.f);
  return pk_bf16(ea + fmaxf(ta, 1e-6f), eb + fmaxf(tb, 1e-6f));
}
DI f32x2 cmul(f32x2 x, f32x2 y) {
  f32x2 t, z;
  asm("v_pk_mul_f32 %0, %1, %2 op_sel_hi:[0,1]" : "=v"(t) : "v"(x), "v"(y));
  asm("v_pk_fma_f32 %0, %1, %2, %3 op_sel:[1,1,0] op_sel_hi:[1,0,1] neg_lo:[0,1,0]" : "=v"(z) : "v"(x), "v"(y), "v"(t));
  return z;
}
DI f32x2 cexpP(float ar2, float airev, float P) {
  float e = __builtin_amdgcn_exp2f(ar2 * P);
  float t = __builtin_amdgcn_fractf(airev * P);
  float s = __builtin_amdgcn_sinf(t), c = __builtin_amdgcn_cosf(t);
  f32x2 r = {e * c, e * s};
  return r;
}
constexpr int S = 2048, D = 640, NS = 192;
constexpr u32 WS_PQ = 0;
constexpr u32 WS_UT = 2621440;
constexpr u32 WS_WOP = 2 * 2621440;
constexpr u32 WS_FLAG = 2 * 2621440 + 819200;
constexpr u32 WS_ZERO = WS_FLAG + 16384;
constexpr int KVACC_BYTES = 32 * 1024 * 4;
constexpr int CNT_OFF = KVACC_BYTES;
constexpr int CNT_KV = 0, CNT_T = 8, CNT_PQ = 16, CNT_KG = 32, ZPAD = 40;
constexpr int ZERO_BYTES = KVACC_BYTES + 48 * 128;
constexpr u32 WS_SPAN = 7u << 20;
constexpr unsigned long long MAGIC = 0x9e3779b97f4a7c15ull;
constexpr unsigned SPIN_CAP = 1u << 17;
typedef __amdgpu_buffer_rsrc_t rsrc_t;

DI gu32* cnt_ptr(char* ws, int i) { return (gu32*)(ws + WS_ZERO + CNT_OFF + i * 128); }
DI void wait_init_t0(char* ws, bool& done) {
  if (done) return;
  gu64* f = (gu64*)(ws + WS_FLAG) + blockIdx.x * 8;
  for (unsigned sp = 0; sp < SPIN_CAP; ++sp) {
    bool ok = true;
#pragma unroll
    for (int i = 0; i < 8; ++i) ok &= __hip_atomic_load(f + i, RLX_AGENT) == MAGIC;
    if (ok) break;
    __builtin_amdgcn_s_sleep(2);
  }
#pragma unroll
  for (int i = 0; i < 8; ++i) __hip_atomic_store(f + i, 0ull, RLX_AGENT);
  done = true;
}
DI void wait_cnt_t0(char* ws, int i, unsigned want) {
  gu32* c = cnt_ptr(ws, i);
  if (__hip_atomic_load(c, RLX_AGENT) >= want) return;
  unsigned v0, v1, v2, v3;
#define PLD(v) asm volatile("global_load_dword %0, %1, off sc1" : "=v"(v) : "v"(c) : "memory")
  PLD(v0); __builtin_amdgcn_s_sleep(2); PLD(v1); __builtin_amdgcn_s_sleep(2); PLD(v2); __builtin_amdgcn_s_sleep(2);
  for (unsigned sp = 0; sp < SPIN_CAP; ++sp) {
    PLD(v3); asm volatile("s_waitcnt vmcnt(3)" : "+v"(v0) :: "memory"); if (v0 >= want) break; __builtin_amdgcn_s_sleep(2);
    PLD(v0); asm volatile("s_waitcnt vmcnt(3)" : "+v"(v1) :: "memory"); if (v1 >= want) break; __builtin_amdgcn_s_sleep(2);
    PLD(v1); asm volatile("s_waitcnt vmcnt(3)" : "+v"(v2) :: "memory"); if (v2 >= want) break; __builtin_amdgcn_s_sleep(2);
    PLD(v2); asm volatile("s_waitcnt vmcnt(3)" : "+v"(v3) :: "memory"); if (v3 >= want) break; __builtin_amdgcn_s_sleep(2);
  }
  asm volatile("s_waitcnt vmcnt(0)" : "+v"(v0), "+v"(v1), "+v"(v2), "+v"(v3) :: "memory");
#undef PLD
}
DI void publish(char* ws, int i, bool& initdone) {
  asm volatile("s_waitcnt vmcnt(0)" ::: "memory");
  __syncthreads();
  if (threadIdx.x == 0) { wait_init_t0(ws, initdone); __hip_atomic_fetch_add(cnt_ptr(ws, i), 1u, RLX_AGENT); }
}
DI void publish8(char* ws, int i, bool& initdone) {
  if (threadIdx.x == 0) wait_init_t0(ws, initdone);
  asm volatile("s_waitcnt vmcnt(0)" ::: "memory");
  __syncthreads();
  if (threadIdx.x < 8) __hip_atomic_fetch_add(cnt_ptr(ws, i + threadIdx.x), 1u, RLX_AGENT);
}
DI u32x4 ld16_sc(rsrc_t r, u32 off) { return __builtin_amdgcn_raw_buffer_load_b128(r, off, 0, 16); }
DI u32x2 ld8_sc(const void* p) { unsigned long long v = __hip_atomic_load((gu64*)p, RLX_AGENT); return (u32x2){(u32)v, (u32)(v >> 32)}; }
DI float ld4_sc(const void* p) { return __uint_as_float(__hip_atomic_load((gu32*)p, RLX_AGENT)); }
DI void acquire_all() {
  if (threadIdx.x == 0) { __builtin_amdgcn_fence(__ATOMIC_ACQUIRE, "agent"); asm volatile("s_waitcnt vmcnt(0)" ::: "memory"); }
  __syncthreads();
}
DI void st16_wt(rsrc_t r, u32 off, u32x4 v) { __builtin_amdgcn_raw_buffer_store_b128(v, r, off, 0, 16); }
DI void st8_wt(void* p, u32x2 v) { __hip_atomic_store((gu64*)p, ((unsigned long long)v.y << 32) | v.x, RLX_AGENT); }

#define TLS(i)
constexpr int QSTAGE = 36864, QNBUF = 4;
constexpr int TROW = 272, KG_REG = 14720;

DI f32x2 cm(f32x2 x, f32x2 y) {
  f32x2 xx = {x.x, x.x}, xy = {x.y, x.y}, ys = {-y.y, y.x};
  return __builtin_elementwise_fma(xy, ys, xx * y);
}
DI f32x2 cmc(f32x2 x, f32x2 y) {
  f32x2 xx = {x.x, x.x}, xy = {x.y, x.y}, yc = {y.x, -y.y}, ys = {y.y, y.x};
  return __builtin_elementwise_fma(xy, ys, xx * yc);
}
DI void kgen_chunk(float a2, float av, f32x2 Ein, char* reg, f32x16& kacc) {
  const int lane = threadIdx.x & 63;
  u32* tE = (u32*)(reg) + lane;
  u32* tF = (u32*)(reg + 21 * TROW) + lane;
  constexpr int R = TROW / 4;
  auto stE = [&](int r, f32x2 v) { tE[r * R] = pk_f16(v.x, v.y); };
  auto stF = [&](int r, f32x2 v) { tF[r * R] = pk_f16(v.x, v.y); };
  f32x2 w1 = cexpP(a2, av, 1.f), v1 = cexpP(a2, av, 8.f), B256 = cexpP(a2, av, 256.f);
  f32x2 w2 = cm(w1, w1), w4 = cm(w2, w2);
  f32x2 v2 = cm(v1, v1), v4 = cm(v2, v2), v8 = cm(v4, v4), v16 = cm(v8, v8);
  f32x2 B512 = cm(B256, B256), B1024 = cm(B512, B512), B768 = cm(B512, B256);
  f32x2 E0 = {Ein.x, -Ein.y};
  f32x2 E1 = cmc(E0, w1), E2 = cmc(E0, w2), E3 = cmc(E1, w2), E4 = cmc(E0, w4), E5 = cmc(E1, w4), E6 = cmc(E2, w4), E7 = cmc(E3, w4);
  stE(0, E0); stE(1, E1); stE(2, E2); stE(3, E3); stE(4, E4); stE(5, E5); stE(6, E6); stE(7, E7);
  stE(8, cmc(E0, B256)); stE(9, cmc(E2, B256)); stE(10, cmc(E4, B256)); stE(11, cmc(E6, B256));
  stE(12, cmc(E0, B512)); stE(13, cmc(E4, B512)); stE(14, cmc(E0, B768)); stE(15, cmc(E4, B768));
  f32x2 G0 = cmc(E0, B1024);
  stE(16, G0); stE(17, cmc(G0, B256)); stE(18, cmc(G0, B512)); stE(19, cmc(G0, B768)); stE(20, cmc(G0, B1024));
  f32x2 F[8];
  F[0].x = 1.f; F[0].y = 0.f; F[1] = v1; F[2] = v2; F[3] = cm(v2, v1); F[4] = v4; F[5] = cm(v4, v1); F[6] = cm(v4, v2); F[7] = cm(v4, F[3]);
  f32x2 v24 = cm(v16, v8);
#pragma unroll
  for (int i = 0; i < 8; ++i) {
    stF(i, F[i]);
    stF(8 + i, i == 0 ? v8 : cm(F[i], v8));
    stF(16 + i, i == 0 ? v16 : cm(F[i], v16));
    stF(24 + i, i == 0 ? v24 : cm(F[i], v24));
  }
  __builtin_amdgcn_wave_barrier();
  const int r = lane & 31, hh = lane >> 5;
  const char* pa = reg + (r < 21 ? r : 20) * TROW + 16 * hh;
  const char* pb = reg + (21 + r) * TROW + 16 * hh;
#pragma unroll
  for (int s = 0; s < 8; ++s) {
    h8 a = *(const h8*)(pa + 32 * s);
    h8 b = *(const h8*)(pb + 32 * s);
    kacc = __builtin_amdgcn_mfma_f32_32x32x16_f16(a, b, kacc, 0, 0, 0);
  }
  __builtin_amdgcn_wave_barrier();
}

struct KGP { const u16 *lar, *aim, *cr, *ci, *ldt; };
struct KGL { u16 pcr[3], pci[3], plar, paim, pldt; };
DI void kgen_load(const KGP& p, int d, int c, KGL& l) {
  const int lane = threadIdx.x & 63;
#pragma unroll
  for (int q = 0; q < 3; ++q) { int n = d * NS + 64 * q + lane; l.pcr[q] = p.cr[n]; l.pci[q] = p.ci[n]; }
  l.plar = p.lar[d * NS + 64 * c + lane]; l.paim = p.aim[d * NS + 64 * c + lane];
  l.pldt = p.ldt[d];
}
template <class HOOK> DI float kgen_task(const KGL& l, int c, char* reg, float* dst, HOOK hook) {
  const int lane = threadIdx.x & 63;
  const float dt = rbf(expf(bf2f(l.pldt)));
  float cmax = 0.f;
#pragma unroll
  for (int q = 0; q < 3; ++q) cmax = fmaxf(cmax, fmaxf(fabsf(bf2f(l.pcr[q])), fabsf(bf2f(l.pci[q]))));
  cmax *= dt * 2.f;
#pragma unroll
  for (int o = 32; o >= 1; o >>= 1) cmax = fmaxf(cmax, __shfl_xor(cmax, o));
  int ce = (int)((__float_as_uint(cmax) >> 23) & 0xff);
  ce = ce < 30 ? 30 : (ce > 220 ? 220 : ce);
  const float sc = __uint_as_float((u32)(127 + 125 - ce) << 23);
  const float isc = __uint_as_float((u32)(ce + 2) << 23);
  const float Are = -rbf(expf(bf2f(l.plar)));
  const float ar2 = Are * dt * 1.4426950408889634f, airev = bf2f(l.paim) * dt * 0.15915494309189535f;
  const u16 mcr = c == 0 ? l.pcr[0] : (c == 1 ? l.pcr[1] : l.pcr[2]), mci = c == 0 ? l.pci[0] : (c == 1 ? l.pci[1] : l.pci[2]);
  f32x2 E = {bf2f(mcr) * dt * 2.f * sc, bf2f(mci) * dt * 2.f * sc};
  f32x16 kacc;
#pragma unroll
  for (int i = 0; i < 16; ++i) kacc[i] = 0.f;
  hook();
  kgen_chunk(ar2, airev, E, reg, kacc);
  const int i = lane & 31, hh = lane >> 5;
#pragma unroll
  for (int rg = 0; rg < 12; ++rg) {
    const int q = rg >> 2, sub = rg & 3;
    int idx; bool ok = true;
    if (q == 0) idx = 8 * i + 4 * hh + sub;
    else if (q == 1) idx = hh == 0 ? 256 + 4 * i + sub : 384 + 64 * (sub >> 1) + 2 * i + (sub & 1);
    else { idx = hh == 0 ? 512 + 32 * sub + i : 640; ok = (hh == 0) || (sub == 0 && i == 0); }
    if (ok) dst[idx] = kacc[rg];
  }
  return isc * (1.f / 128.f);
}

template <bool KV>
DI void q_block(const u16* __restrict__ x, const u16* __restrict__ w, char* __restrict__ ws, rsrc_t rs, int rt, int ct, char* smem, bool& initdone) {
  const int tid = threadIdx.x, lane = tid & 63, wv = __builtin_amdgcn_readfirstlane(tid >> 6);
  const int row0 = rt * 128;
  const int fr = lane & 15, g = lane >> 4;
  if (wv >= 4) {
    const int lw = wv - 4;
    const u16* xs[4]; const u16* ws_[5];
#pragma unroll
    for (int i = 0; i < 4; ++i) { int pc = lw * 4 + i; int row = 8 * pc + (lane >> 3); int ch = (lane & 7) ^ ((row >> 1) & 7); xs[i] = x + (size_t)(row0 + row) * D + 8 * ch; }
#pragma unroll
    for (int i = 0; i < 5; ++i) {
      int pc = lw * 5 + i; int sb = 4 * pc + (lane >> 4); int kb = sb / 10, nb = sb % 10; int row = ((lane >> 1) & 7) ^ (4 * (kb & 1)); int c = nb * 16 + 8 * (lane & 1);
      int gc;
      if (KV) { int wn2 = c / 80, cc = c % 80; gc = cc < 40 ? 640 + 80 * ct + 40 * wn2 + cc : 1280 + 80 * ct + 40 * wn2 + (cc - 40); }
      else gc = 160 * ct + c;
      ws_[i] = w + (size_t)(8 * kb + row) * 1920 + gc;
    }
    auto stage = [&](int kt) {
      char* b = smem + (kt & (QNBUF - 1)) * QSTAGE;
#pragma unroll
      for (int i = 0; i < 4; ++i) __builtin_amdgcn_global_load_lds((const u32*)(xs[i] + kt * 64), (LAS u32*)(b + (lw * 4 + i) * 1024), 16, 0, 0);
#pragma unroll
      for (int i = 0; i < 5; ++i) __builtin_amdgcn_global_load_lds((const u32*)(ws_[i] + (size_t)kt * 64 * 1920), (LAS u32*)(b + 16384 + (lw * 5 + i) * 1024), 16, 0, 0);
    };
#pragma unroll 1
    for (int i = 0; i < QNBUF; ++i) stage(i);
    asm volatile("s_waitcnt vmcnt(%0)" :: "n"((QNBUF - 1) * 9) : "memory");
    __builtin_amdgcn_s_barrier();
#pragma unroll 1
    for (int kt = 0; kt < 6; ++kt) {
      asm volatile("s_waitcnt vmcnt(18)" ::: "memory");
      __builtin_amdgcn_s_barrier();
      stage(kt + QNBUF);
    }
    asm volatile("s_waitcnt vmcnt(18)" ::: "memory"); __builtin_amdgcn_s_barrier();
    asm volatile("s_waitcnt vmcnt(9)" ::: "memory");  __builtin_amdgcn_s_barrier();
    asm volatile("s_waitcnt vmcnt(0)" ::: "memory");  __builtin_amdgcn_s_barrier();
    if (KV) { __builtin_amdgcn_s_barrier(); __builtin_amdgcn_s_barrier(); }
  } else {
    const int wm = wv >> 1, wn = wv & 1;
    f32x4 acc[4][5];
#pragma unroll
    for (int m = 0; m < 4; ++m)
#pragma unroll
      for (int n = 0; n < 5; ++n) acc[m][n] = (f32x4){0, 0, 0, 0};
    const u32 aoff0 = (wm * 64 + fr) * 128 + (((0 + g) ^ (fr >> 1)) * 16);
    const u32 aoff1 = (wm * 64 + fr) * 128 + (((4 + g) ^ (fr >> 1)) * 16);
    const u32 boff0 = 16384 + g * 2560 + wn * 1280 + ((0 ^ (4 * (g & 1))) + (fr >> 2)) * 32 + (lane & 3) * 8;
    const u32 boff1 = 16384 + g * 2560 + wn * 1280 + ((4 ^ (4 * (g & 1))) + (fr >> 2)) * 32 + (lane & 3) * 8;
    const u32 sbase = (u32)(size_t)smem;
    s16x8 af[2][4]; s16x4 bl[2][5], bh[2][5];
    u32 a0 = sbase + aoff0, a1 = sbase + aoff1, b0 = sbase + boff0, b1 = sbase + boff1;
    auto ldop = [&](int ks, int i) {
      if (i < 10) {
        const int n = i >> 1;
        if (i & 1) bh[ks][n] = __builtin_amdgcn_ds_read_tr16_b64_v4i16((LAS s16x4*)(b1 + ks * 10240 + n * 256));
        else       bl[ks][n] = __builtin_amdgcn_ds_read_tr16_b64_v4i16((LAS s16x4*)(b0 + ks * 10240 + n * 256));
      } else af[ks][i - 10] = *(const LAS s16x8*)((ks ? a1 : a0) + (i - 10) * 2048);
    };
    auto mma1 = [&](int sl, int i) {
      const int m = i / 5, n = i % 5;
      s16x8 bb = __builtin_shufflevector(bl[sl][n], bh[sl][n], 0, 1, 2, 3, 4, 5, 6, 7);
      acc[m][n] = KV ? __builtin_amdgcn_mfma_f32_16x16x32_bf16(af[sl][m], bb, acc[m][n], 0, 0, 0)
                     : __builtin_amdgcn_mfma_f32_16x16x32_bf16(bb, af[sl][m], acc[m][n], 0, 0, 0);
    };
    __builtin_amdgcn_s_barrier();
    TLS(15);
#pragma unroll
    for (int i = 0; i < 14; ++i) ldop(0, i);
    int cur = 0;
    unsigned long long fl = MAGIC;
    gu64* fp = (gu64*)(ws + WS_FLAG) + blockIdx.x * 8 + (lane & 7);
#pragma unroll 1
    for (int kt = 0; kt < 10; ++kt) {
      if (kt == 6 && wv == 0) asm volatile("global_load_dwordx2 %0, %1, off sc1" : "=v"(fl) : "v"(fp) : "memory");
      __builtin_amdgcn_sched_barrier(0);
#pragma unroll
      for (int i = 0; i < 20; ++i) {
        mma1(0, i);
        if (i < 14) ldop(1, i);
        __builtin_amdgcn_sched_barrier(0);
      }
      cur = (cur + 1) & (QNBUF - 1);
      const u32 nb = sbase + cur * QSTAGE;
      a0 = nb + aoff0; a1 = nb + aoff1; b0 = nb + boff0; b1 = nb + boff1;
      __builtin_amdgcn_s_waitcnt(0xC07F);
      if (kt < 9) __builtin_amdgcn_s_barrier();
      __builtin_amdgcn_sched_barrier(0);
#pragma unroll
      for (int i = 0; i < 20; ++i) {
        mma1(1, i);
        if (i < 14) ldop(0, i);
        __builtin_amdgcn_sched_barrier(0);
      }
    }
    if (wv == 0) {
      asm volatile("s_waitcnt vmcnt(0)" : "+v"(fl) :: "memory");
      if (__all(fl == MAGIC)) { if (lane < 8) __hip_atomic_store(fp, 0ull, RLX_AGENT); }
      else if (lane == 0) { bool d = false; wait_init_t0(ws, d); }
    }
    TLS(14);
    if (!KV) {
      char* pq = ws + WS_PQ;
#pragma unroll
      for (int m = 0; m < 4; ++m)
#pragma unroll
        for (int n = 0; n < 5; ++n) {
          u32x2 o = {phi_pair(acc[m][n][0], acc[m][n][1]), phi_pair(acc[m][n][2], acc[m][n][3])};
          const int col = 160 * ct + wn * 80 + n * 16 + 4 * g; const int h = col / 20, q = (col % 20) >> 2;
          st8_wt(pq + 2 * (((size_t)((rt * 8 + wm * 4 + m) * 32 + h) * 5 + q) * 64 + fr * 4), o);
        }
    } else {
      u32x2 pk[4][5];
      const bool isk = fr < 8;
#pragma unroll
      for (int m = 0; m < 4; ++m)
#pragma unroll
        for (int n = 0; n < 5; ++n) {
          if (n <= 2) { pk[m][n].x = phi_pair(acc[m][n][0], acc[m][n][1]); pk[m][n].y = phi_pair(acc[m][n][2], acc[m][n][3]); }
          if (n >= 2) {
            u32 v0 = pk_bf16(acc[m][n][0], acc[m][n][1]), v1 = pk_bf16(acc[m][n][2], acc[m][n][3]);
            if (n > 2) { pk[m][n].x = v0; pk[m][n].y = v1; } else { pk[m][n].x = isk ? pk[m][n].x : v0; pk[m][n].y = isk ? pk[m][n].y : v1; }
          }
        }
      f32x4 kv[10];
#pragma unroll
      for (int i = 0; i < 10; ++i) kv[i] = (f32x4){0, 0, 0, 0};
      const s16x8 ones = {0x3f80, 0x3f80, 0x3f80, 0x3f80, 0x3f80, 0x3f80, 0x3f80, 0x3f80};
#pragma unroll
      for (int u = 0; u < 2; ++u) {
        s16x8 f[5];
#pragma unroll
        for (int n = 0; n < 5; ++n) f[n] = __builtin_bit_cast(s16x8, (u32x4){pk[2 * u][n].x, pk[2 * u][n].y, pk[2 * u + 1][n].x, pk[2 * u + 1][n].y});
        kv[0] = __builtin_amdgcn_mfma_f32_16x16x32_bf16(f[0], f[2], kv[0], 0, 0, 0);
        kv[1] = __builtin_amdgcn_mfma_f32_16x16x32_bf16(f[0], f[3], kv[1], 0, 0, 0);
        kv[2] = __builtin_amdgcn_mfma_f32_16x16x32_bf16(f[1], f[2], kv[2], 0, 0, 0);
        kv[3] = __builtin_amdgcn_mfma_f32_16x16x32_bf16(f[1], f[3], kv[3], 0, 0, 0);
        kv[4] = __builtin_amdgcn_mfma_f32_16x16x32_bf16(f[1], f[4], kv[4], 0, 0, 0);
        kv[5] = __builtin_amdgcn_mfma_f32_16x16x32_bf16(f[2], f[3], kv[5], 0, 0, 0);
        kv[6] = __builtin_amdgcn_mfma_f32_16x16x32_bf16(f[2], f[4], kv[6], 0, 0, 0);
        kv[7] = __builtin_amdgcn_mfma_f32_16x16x32_bf16(f[0], ones, kv[7], 0, 0, 0);
        kv[8] = __builtin_amdgcn_mfma_f32_16x16x32_bf16(f[1], ones, kv[8], 0, 0, 0);
        kv[9] = __builtin_amdgcn_mfma_f32_16x16x32_bf16(f[2], ones, kv[9], 0, 0, 0);
      }
      __builtin_amdgcn_s_barrier();
      f32x4* red = (f32x4*)smem + (wm * 2 + wn) * 640 + lane;
#pragma unroll
      for (int i = 0; i < 10; ++i) red[i * 64] = kv[i];
      __builtin_amdgcn_s_waitcnt(0xC07F);
      __builtin_amdgcn_s_barrier();
    }
  }
  initdone = true;
  if (KV) {
    const float* raw = (const float*)smem;
    float* acc = (float*)(ws + WS_ZERO) + (size_t)ct * 4096 + tid;
#pragma unroll
    for (int it = 0; it < 8; ++it) {
      const int e0 = it * 512 + tid;
      const int hl4 = it >> 1, tile = it & 1, lp = (e0 >> 3) & 63, j = e0 & 7;
      const int row = lp & 15, gk = lp >> 4, gg = row >> 2, r = row & 3;
      const int wn = hl4 >> 1, hl = hl4 & 1;
      const int dd = 8 * gk + j;
      if ((tile == 0 || r < 2) && dd < 20) {
        const int d = dd + 20 * hl, dt = d >> 4;
        const int e = (tile == 0 ? 5 * gg + r : 5 * gg + 3 + r) + 20 * hl + 40, et = e >> 4;
        int t, ln;
        if (tile == 0 && r == 3) { t = 7 + dt; ln = ((d & 15) >> 2) * 16; }
        else { t = dt == 0 ? et - 2 : (dt == 1 ? et : et + 2); ln = ((d & 15) >> 2) * 16 + (e & 15); }
        const int idx = (wn * 640 + t * 64 + ln) * 4 + (d & 3);
        const float v = raw[idx] + raw[2 * 640 * 4 + idx];
        __hip_atomic_fetch_add((GAS float*)(acc + it * 512), v, RLX_AGENT);
      }
    }
    TLS(1);
    publish8(ws, CNT_KV, initdone);
  } else {
    TLS(1);
    publish(ws, CNT_PQ + rt, initdone);
  }
}

DI void aux_block(const u16* __restrict__ u, const u16* __restrict__ wo, char* __restrict__ ws, rsrc_t rs, int ab, char* smem, bool& initdone) {
  const int tid = threadIdx.x, lane = tid & 63, wv = __builtin_amdgcn_readfirstlane(tid >> 6);
  char* reg = smem + wv * KG_REG;
  if (wv < 5) {
    const int tile = ab * 5 + wv; const int rb = tile / 10, cb = tile % 10;
    u16* t = (u16*)reg;
#pragma unroll
    for (int i = 0; i < 8; ++i) {
      const int row = i * 8 + (lane >> 3), ch = lane & 7;
      u32x4 v = *(const u32x4*)(u + (size_t)(rb * 64 + row) * D + cb * 64 + ch * 8);
      u32* p = (u32*)(t + row * 66 + ch * 8);
      p[0] = v.x; p[1] = v.y; p[2] = v.z; p[3] = v.w;
    }
    __builtin_amdgcn_wave_barrier();
#pragma unroll
    for (int i = 0; i < 8; ++i) {
      const int c = i * 8 + (lane >> 3), r8 = lane & 7;
      u32 o[4];
#pragma unroll
      for (int k = 0; k < 4; ++k) o[k] = (u32)t[(r8 * 8 + 2 * k) * 66 + c] | ((u32)t[(r8 * 8 + 2 * k + 1) * 66 + c] << 16);
      st16_wt(rs, WS_UT + ((cb * 64 + c) * S + rb * 64 + r8 * 8) * 2, (u32x4){o[0], o[1], o[2], o[3]});
    }
    __builtin_amdgcn_wave_barrier();
  } else {
#pragma unroll
    for (int it = 0; it < 5; ++it) {
      const int uu = (wv - 5) * 64 + lane + 192 * it;
      if (uu < 800) {
        const int U = ab * 800 + uu; const int img = U / 1280, un = U % 1280; const int ct = img / 10, kt = img % 10;
        const int sb = un >> 4, kb = sb / 10, nb = sb % 10, j = ((un >> 1) & 7) ^ (4 * (kb & 1));
        const int T = 2 * kt + (kb >> 2), G = T / 5, tt = T % 5, ii = 8 * tt + j, hh = ii / 5, r = ii % 5;
        const int k = 160 * G + 20 * hh + 5 * (kb & 3) + r;
        st16_wt(rs, WS_WOP + U * 16, *(const u32x4*)(wo + (size_t)k * D + 160 * ct + nb * 16 + 8 * (un & 1)));
      }
    }
  }
  TLS(1);
  publish8(ws, CNT_T, initdone);
}

constexpr int UC_STRIDE = 4160, UCH = 4 * UC_STRIDE, KP_STRIDE = 1280, KPCH = 4 * KP_STRIDE;
constexpr int UC0 = 4 * KG_REG, PART0 = UC0 + 4 * UCH, KC0 = PART0 + 12 * 2592, RED0 = KC0 + 4 * 1312;
constexpr int B_LDS = RED0 + 128;
constexpr int PSPLIT = 22;
DI void b_block(const KGP& kp, char* __restrict__ ws, rsrc_t rs, const u16* __restrict__ dsk_, u16* __restrict__ out, int cb, char* smem, bool& initdone) {
  const int tid = threadIdx.x, lane = tid & 63, wv = __builtin_amdgcn_readfirstlane(tid >> 6), ch = wv & 3, hf = wv >> 2;
  const int d0 = cb * 4;
  float* red = (float*)(smem + RED0);
  float* part = (float*)(smem + PART0);
  KGL l0, l1;
  kgen_load(kp, d0 + wv / 3, wv % 3, l0);
  kgen_load(kp, d0 + (8 + (wv & 3)) / 3, (8 + (wv & 3)) % 3, l1);
  float dk[4];
#pragma unroll
  for (int c = 0; c < 4; ++c) dk[c] = bf2f(dsk_[d0 + c]);
  u32x4 uv[4];
  unsigned tc = 0;
  gu32* tcp = cnt_ptr(ws, CNT_T + (blockIdx.x & 7));
  if (wv >= 4) asm volatile("global_load_dword %0, %1, off sc1" : "=v"(tc) : "v"(tcp) : "memory");
  {
    float ks = kgen_task(l0, wv % 3, smem + wv * KG_REG, part + wv * 648, [&]() {
      if (wv >= 4) {
        asm volatile("s_waitcnt vmcnt(0)" : "+v"(tc) :: "memory");
        if (tc < 64) { if (lane == 0) wait_cnt_t0(ws, CNT_T + (blockIdx.x & 7), 64); __builtin_amdgcn_wave_barrier(); }
#pragma unroll
        for (int i = 0; i < 4; ++i) uv[i] = ld16_sc(rs, WS_UT + ((d0 + wv - 4) * S + 8 * (lane + 64 * i)) * 2);
      }
    });
    if (lane == 0 && wv % 3 == 0) red[8 + wv / 3] = ks;
  }
  __syncthreads();
  TLS(3);
  if (wv < 4) {
    float ks = kgen_task(l1, (8 + wv) % 3, smem + wv * KG_REG, part + (8 + wv) * 648, []() {});
    if (lane == 0 && wv == 1) red[8 + 3] = ks;
  } else {
    const int c = wv - 4;
    char* ur = smem + UC0 + c * UCH;
    float m = 0.f;
#pragma unroll
    for (int i = 0; i < 4; ++i)
#pragma unroll
      for (int k = 0; k < 4; ++k) m = fmaxf(m, fmaxf(fabsf(lo16(uv[i][k])), fabsf(hi16(uv[i][k]))));
#pragma unroll
    for (int o = 32; o >= 1; o >>= 1) m = fmaxf(m, __shfl_xor(m, o));
    int e = (int)((__float_as_uint(m) >> 23) & 0xff);
    e = e < 30 ? 30 : (e > 220 ? 220 : e);
    const float us = __uint_as_float((u32)(127 + 140 - e) << 23);
    if (lane == 0) red[c] = __uint_as_float((u32)(e - 13) << 23);
#pragma unroll
    for (int i = 0; i < 4; ++i) {
      const int q = lane + 64 * i;
      u32x4 o = {pk_f16(hi16(uv[i].w) * us, lo16(uv[i].w) * us), pk_f16(hi16(uv[i].z) * us, lo16(uv[i].z) * us),
                 pk_f16(hi16(uv[i].y) * us, lo16(uv[i].y) * us), pk_f16(hi16(uv[i].x) * us, lo16(uv[i].x) * us)};
      *(u32x4*)(ur + 2 * (2040 - 8 * q)) = o;
    }
    if (lane < 4) *(u32x4*)(ur + 4096 + 16 * lane) = (u32x4){0, 0, 0, 0};
    __builtin_amdgcn_wave_barrier();
#pragma unroll
    for (int k = 0; k < 5; ++k) {
      const int q = lane + 64 * k;
      if (q < 260) {
        u32x4 a = *(const u32x4*)(ur + 16 * q);
        u32x2 b = *(const u32x2*)(ur + 16 * q + 16);
        u32x4 c1 = {__builtin_amdgcn_alignbit(a.y, a.x, 16), __builtin_amdgcn_alignbit(a.z, a.y, 16), __builtin_amdgcn_alignbit(a.w, a.z, 16), __builtin_amdgcn_alignbit(b.x, a.w, 16)};
        u32x4 c2 = {a.y, a.z, a.w, b.x};
        u32x4 c3 = {c1.y, c1.z, c1.w, __builtin_amdgcn_alignbit(b.y, b.x, 16)};
        *(u32x4*)(ur + 1 * UC_STRIDE + 16 * q) = c1;
        *(u32x4*)(ur + 2 * UC_STRIDE + 16 * q) = c2;
        *(u32x4*)(ur + 3 * UC_STRIDE + 16 * q) = c3;
      }
    }
  }
  __syncthreads();
  TLS(11);
  {
    u16* kc = (u16*)(smem + KC0 + ch * 1312);
    const float* p0 = part + ch * 3 * 648;
#pragma unroll
    for (int i = 0; i < 2; ++i) {
      const int ci = hf * 81 + 64 * i + lane;
      if (lane + 64 * i < 81) {
        f32x4 v = *(const f32x4*)(p0 + ci * 4) + *(const f32x4*)(p0 + 648 + ci * 4) + *(const f32x4*)(p0 + 1296 + ci * 4);
        *(u32x2*)(kc + ci * 4) = (u32x2){pk_f16(v[0] * 128.f, v[1] * 128.f), pk_f16(v[2] * 128.f, v[3] * 128.f)};
      }
    }
  }
  __syncthreads();
  {
    const u16* kc = (const u16*)(smem + KC0 + ch * 1312);
    char* kpl = smem + ch * KPCH;
    if (hf == 1 && lane < 60) *(u32x4*)(kpl + (lane & 3) * KP_STRIDE + (lane >> 2) * 16) = (u32x4){0, 0, 0, 0};
#pragma unroll
    for (int k = 0; k < 2; ++k) {
      const int q = hf + 2 * (lane + 64 * k);
      u32 v[8];
#pragma unroll
      for (int j8 = 0; j8 < 8; ++j8) {
        int j = 8 * q + j8;
        u32 fb = __float_as_uint((float)j); fb += 0x7fffu + ((fb >> 16) & 1u);
        int idx = j < 256 ? j : 256 + (int)((fb >> 16) - 0x4380u);
        v[j8] = kc[idx];
      }
      u32x4 o = {v[0] | (v[1] << 16), v[2] | (v[3] << 16), v[4] | (v[5] << 16), v[6] | (v[7] << 16)};
      *(u32x4*)(kpl + (q & 3) * KP_STRIDE + ((q >> 2) + 15) * 16) = o;
    }
  }
  __syncthreads();
  TLS(12);
  f32x4 acc[4][2];
#pragma unroll
  for (int t = 0; t < 4; ++t) { acc[t][0] = (f32x4){0, 0, 0, 0}; acc[t][1] = (f32x4){0, 0, 0, 0}; }
  const int b = lane & 15, g = lane >> 4;
  char* ureg = smem + UC0 + ch * UCH;
  {
    const int cpy = (3 - b) & 3;
    const char* pa0 = ureg + cpy * UC_STRIDE + 2 * (2044 - 4 * (b >> 2) + 8 * g);
    const char* pb0 = smem + ch * KPCH + g * KP_STRIDE + 16 * (b + 15);
    typedef const volatile LAS u32x2* lp2;
    typedef const volatile LAS u32x4* lp4;
    const u32 abase = (u32)(size_t)(pa0 - 64 * 63 - 32);
    const u32 bbase = (u32)(size_t)(pb0 - 16 * 63);
    constexpr int DP = 2;
    u32x2 Ab[DP + 1][4]; u32x4 Bb[DP + 1][4];
    auto run = [&](auto P0c, auto P1c) {
      constexpr int P0 = decltype(P0c)::value, P1 = decltype(P1c)::value;
      auto issue = [&](int p) {
        const int sl = p % (DP + 1);
        const u32 oa = 64 * (63 - p);
        Ab[sl][0] = *(lp2)(abase + oa + 32); Ab[sl][1] = *(lp2)(abase + oa + 40);
        Ab[sl][2] = *(lp2)(abase + oa);      Ab[sl][3] = *(lp2)(abase + oa + 8);
#pragma unroll
        for (int t = 0; t < 4; ++t) if (t >= (p >> 4)) Bb[sl][t] = *(lp4)(bbase + 16 * (63 - p) + 256 * t);
      };
#pragma unroll
      for (int p = P0; p < P0 + DP; ++p) issue(p);
#pragma unroll
      for (int p = P0; p < P1; ++p) {
        if (p + DP < P1) issue(p + DP);
        __builtin_amdgcn_sched_barrier(0);
        const int sl = p % (DP + 1);
        h8 A0 = __builtin_bit_cast(h8, (u32x4){Ab[sl][0].x, Ab[sl][0].y, Ab[sl][1].x, Ab[sl][1].y});
        h8 A1 = __builtin_bit_cast(h8, (u32x4){Ab[sl][2].x, Ab[sl][2].y, Ab[sl][3].x, Ab[sl][3].y});
#pragma unroll
        for (int t = 0; t < 4; ++t) if (t >= (p >> 4)) {
          h8 B = __builtin_bit_cast(h8, Bb[sl][t]);
          acc[t][0] = __builtin_amdgcn_mfma_f32_16x16x32_f16(A0, B, acc[t][0], 0, 0, 0);
          acc[t][1] = __builtin_amdgcn_mfma_f32_16x16x32_f16(A1, B, acc[t][1], 0, 0, 0);
        }
        __builtin_amdgcn_sched_barrier(0);
      }
    };
    if (hf == 0) run(std::integral_constant<int, 0>{}, std::integral_constant<int, PSPLIT>{});
    else run(std::integral_constant<int, PSPLIT>{}, std::integral_constant<int, 64>{});
  }
  TLS(13);
  f32x4* xch = (f32x4*)(smem + PART0 + ch * 6144) + lane;
  if (hf == 1) { xch[0 * 64] = acc[1][0]; xch[1 * 64] = acc[1][1]; }
  else { xch[2 * 64] = acc[2][0]; xch[3 * 64] = acc[2][1]; xch[4 * 64] = acc[3][0]; xch[5 * 64] = acc[3][1]; }
  __syncthreads();
  {
    const float ysc = red[8 + ch] * red[ch];
    char* yb = smem + ch * 4096;
    f32x4 a[2][2];
    if (hf == 0) { a[0][0] = acc[0][0]; a[0][1] = acc[0][1]; a[1][0] = acc[1][0] + xch[0 * 64]; a[1][1] = acc[1][1] + xch[1 * 64]; }
    else { a[0][0] = acc[2][0] + xch[2 * 64]; a[0][1] = acc[2][1] + xch[3 * 64]; a[1][0] = acc[3][0] + xch[4 * 64]; a[1][1] = acc[3][1] + xch[5 * 64]; }
#pragma unroll
    for (int t = 0; t < 2; ++t)
#pragma unroll
      for (int r = 0; r < 2; ++r) {
        u32x2 o = {pk_bf16(a[t][r][0] * ysc, a[t][r][1] * ysc), pk_bf16(a[t][r][2] * ysc, a[t][r][3] * ysc)};
        *(u32x2*)(yb + 2 * (32 * (16 * (2 * hf + t) + b) + 16 * r + 4 * g)) = o;
      }
  }
  __syncthreads();
  {
    u32x2 yv[4], uu[4]; float iu[4];
#pragma unroll
    for (int c = 0; c < 4; ++c) {
      yv[c] = *(const u32x2*)(smem + c * 4096 + 8 * tid);
      uu[c] = *(const u32x2*)(smem + UC0 + c * UCH + 2 * (2044 - 4 * tid));
      iu[c] = red[c];
    }
#pragma unroll
    for (int i = 0; i < 4; ++i) {
      float r[4];
#pragma unroll
      for (int c = 0; c < 4; ++c) {
        u32 yw = yv[c][i >> 1]; float y = (i & 1) ? hi16(yw) : lo16(yw);
        const u32 uw = (i < 2) ? uu[c].y : uu[c].x; const u16 ub = (i & 1) ? (u16)uw : (u16)(uw >> 16); float uf = (float)__builtin_bit_cast(_Float16, ub) * iu[c];
        r[c] = y + rbf(uf * dk[c]);
      }
      u32x2 o = {pk_bf16(r[0], r[1]), pk_bf16(r[2], r[3])};
      *(u32x2*)(out + (size_t)(4 * tid + i) * 1280 + 640 + d0) = o;
    }
  }
  __builtin_amdgcn_s_waitcnt(0xC07F);
  __builtin_amdgcn_s_barrier();
}

constexpr int OSTAGE = 20480, ONBUF = 4, O_OFF_W = 81920;
DI void o_block(char* __restrict__ ws, rsrc_t rs, u16* __restrict__ out, int rt, int ct, char* smem, bool& initdone, bool late) {
  const int tid = threadIdx.x, lane = tid & 63, wv = __builtin_amdgcn_readfirstlane(tid >> 6);
  const int row0 = rt * 64, fr = lane & 15, g = lane >> 4;
  const int lw = wv - 4;
  u32* rdy = (u32*)(smem + 163824);
  if (tid == 0) {
    wait_init_t0(ws, initdone);
    if (late) {
      const unsigned a = __hip_atomic_load(cnt_ptr(ws, CNT_T + (blockIdx.x & 7)), RLX_AGENT), bq = __hip_atomic_load(cnt_ptr(ws, CNT_PQ + (rt >> 1)), RLX_AGENT), ck = __hip_atomic_load(cnt_ptr(ws, CNT_KV + (blockIdx.x & 7)), RLX_AGENT);
      if (a < 64) wait_cnt_t0(ws, CNT_T + (blockIdx.x & 7), 64);
      rdy[0] = (bq >= 4 ? 1u : 0u) | (ck >= 128 ? 2u : 0u);
    } else {
      wait_cnt_t0(ws, CNT_T + (blockIdx.x & 7), 64);
      rdy[0] = 0u;
    }
  }
  acquire_all();
  const u32 ready = rdy[0];
  const char* wsrc = ws + WS_WOP + (size_t)ct * 10 * OSTAGE + lw * 5120 + lane * 16;
  auto stage = [&](int kt) {
    char* b = smem + O_OFF_W + (kt & (ONBUF - 1)) * OSTAGE + lw * 5120;
#pragma unroll
    for (int i = 0; i < 5; ++i) __builtin_amdgcn_global_load_lds((const u32*)(wsrc + kt * OSTAGE + i * 1024), (LAS u32*)(b + i * 1024), 16, 0, 0);
  };
  if (wv >= 4) {
#pragma unroll 1
    for (int i = 0; i < ONBUF; ++i) stage(i);
  }
  if (!(ready & 1)) {
    if (tid == 0) wait_cnt_t0(ws, CNT_PQ + (rt >> 1), 4);
    asm volatile("s_waitcnt lgkmcnt(0)" ::: "memory");
    __builtin_amdgcn_s_barrier();
  }
  const char* pq = ws + WS_PQ;
  const char* zp = ws + WS_ZERO + CNT_OFF + ZPAD * 128;
  u32x4 kr[4][2][2]; u32x2 pa[4][4], pb[4][4];
#pragma unroll
  for (int hh = 0; hh < 4; ++hh)
#pragma unroll
    for (int st = 0; st < 4; ++st) {
      const char* p = pq + 2 * (((size_t)((rt * 4 + st) * 32 + 4 * wv + hh) * 5) * 64 + fr * 4);
      pa[hh][st] = ld8_sc(g < 3 ? p + 2 * g * 128 : zp);
      pb[hh][st] = ld8_sc(g < 2 ? p + (2 * g + 1) * 128 : zp);
    }
  if (!(ready & 2)) {
    if (tid == 0) wait_cnt_t0(ws, CNT_KV + (blockIdx.x & 7), 128);
    asm volatile("s_waitcnt lgkmcnt(0)" ::: "memory");
    __builtin_amdgcn_s_barrier();
  }
  TLS(6);
  TLS(7);
#pragma unroll
  for (int hh = 0; hh < 4; ++hh)
#pragma unroll
    for (int tl = 0; tl < 2; ++tl) {
      kr[hh][tl][0] = ld16_sc(rs, WS_ZERO + (((4 * wv + hh) * 2 + tl) * 64 + lane) * 32);
      kr[hh][tl][1] = ld16_sc(rs, WS_ZERO + (((4 * wv + hh) * 2 + tl) * 64 + lane) * 32 + 16);
    }
  __builtin_amdgcn_sched_barrier(0);
  {
    s16x8 kf[4][2];
#pragma unroll
    for (int hh = 0; hh < 4; ++hh)
#pragma unroll
      for (int tl = 0; tl < 2; ++tl) {
        f32x4 a = __builtin_bit_cast(f32x4, kr[hh][tl][0]), b = __builtin_bit_cast(f32x4, kr[hh][tl][1]);
        kf[hh][tl] = __builtin_bit_cast(s16x8, (u32x4){pk_bf16(a[0], a[1]), pk_bf16(a[2], a[3]), pk_bf16(b[0], b[1]), pk_bf16(b[2], b[3])});
      }
    asm volatile("" :: "v"(kf[3][1]), "v"(pa[3][3]), "v"(pb[3][3]));
    TLS(10);
    const int G = wv >> 1, half = wv & 1;
#pragma unroll
    for (int st = 0; st < 4; ++st) {
      float vals[20];
#pragma unroll
      for (int hh = 0; hh < 4; ++hh) {
        s16x8 pf = __builtin_bit_cast(s16x8, (u32x4){pa[hh][st].x, pa[hh][st].y, pb[hh][st].x, pb[hh][st].y});
        f32x4 z4 = {0, 0, 0, 0};
        f32x4 t0 = __builtin_amdgcn_mfma_f32_16x16x32_bf16(kf[hh][0], pf, z4, 0, 0, 0);
        f32x4 t1 = __builtin_amdgcn_mfma_f32_16x16x32_bf16(kf[hh][1], pf, z4, 0, 0, 0);
        float den = rbf(rbf(t0[3]) + 1e-6f);
        float rd = __builtin_amdgcn_rcpf(den);
        vals[5 * hh + 0] = t0[0] * rd; vals[5 * hh + 1] = t0[1] * rd; vals[5 * hh + 2] = t0[2] * rd;
        vals[5 * hh + 3] = t1[0] * rd; vals[5 * hh + 4] = t1[1] * rd;
      }
#pragma unroll
      for (int i = 0; i < 5; ++i) {
        const int byte = 40 * half + 8 * i;
        const int T = 5 * G + (byte >> 4);
        *(u32x2*)(smem + (st * 20 + T) * 1024 + lane * 16 + (byte & 8)) = (u32x2){pk_bf16(vals[4 * i], vals[4 * i + 1]), pk_bf16(vals[4 * i + 2], vals[4 * i + 3])};
      }
    }
  }
  if (wv >= 4) {
    asm volatile("s_waitcnt vmcnt(0) lgkmcnt(0)" ::: "memory");
    __builtin_amdgcn_s_barrier();
#pragma unroll 1
    for (int kt = 0; kt < 6; ++kt) {
      asm volatile("s_waitcnt vmcnt(10)" ::: "memory");
      __builtin_amdgcn_s_barrier();
      stage(kt + ONBUF);
    }
    asm volatile("s_waitcnt vmcnt(10)" ::: "memory"); __builtin_amdgcn_s_barrier();
    asm volatile("s_waitcnt vmcnt(5)" ::: "memory");  __builtin_amdgcn_s_barrier();
    asm volatile("s_waitcnt vmcnt(0)" ::: "memory");  __builtin_amdgcn_s_barrier();
    return;
  }
  const int wm = wv >> 1, wn = wv & 1;
  f32x4 acc[2][5];
#pragma unroll
  for (int m = 0; m < 2; ++m)
#pragma unroll
    for (int n = 0; n < 5; ++n) acc[m][n] = (f32x4){0, 0, 0, 0};
  const u32 sbase = (u32)(size_t)smem;
  const u32 boff0 = O_OFF_W + g * 2560 + wn * 1280 + ((0 ^ (4 * (g & 1))) + (fr >> 2)) * 32 + (lane & 3) * 8;
  const u32 boff1 = O_OFF_W + g * 2560 + wn * 1280 + ((4 ^ (4 * (g & 1))) + (fr >> 2)) * 32 + (lane & 3) * 8;
  u32 aad = sbase + wm * 2 * 20 * 1024 + lane * 16;
  u32 b0 = sbase + boff0, b1 = sbase + boff1;
  s16x8 af[2][2]; s16x4 bl[2][5], bh[2][5];
  auto ldop = [&](int ks, int i) {
    if (i < 10) {
      const int n = i >> 1;
      if (i & 1) bh[ks][n] = __builtin_amdgcn_ds_read_tr16_b64_v4i16((LAS s16x4*)(b1 + ks * 10240 + n * 256));
      else       bl[ks][n] = __builtin_amdgcn_ds_read_tr16_b64_v4i16((LAS s16x4*)(b0 + ks * 10240 + n * 256));
    } else af[ks][i - 10] = *(const LAS s16x8*)(aad + ((i - 10) * 20 + ks) * 1024);
  };
  auto mma1 = [&](int sl, int i) {
    const int m = i / 5, n = i % 5;
    s16x8 bb = __builtin_shufflevector(bl[sl][n], bh[sl][n], 0, 1, 2, 3, 4, 5, 6, 7);
    acc[m][n] = __builtin_amdgcn_mfma_f32_16x16x32_bf16(bb, af[sl][m], acc[m][n], 0, 0, 0);
  };
  __builtin_amdgcn_s_waitcnt(0xC07F);
  __builtin_amdgcn_s_barrier();
  TLS(9);
#pragma unroll
  for (int i = 0; i < 12; ++i) ldop(0, i);
  int cur = 0;
#pragma unroll 1
  for (int kt = 0; kt < 10; ++kt) {
    __builtin_amdgcn_sched_barrier(0);
#pragma unroll
    for (int i = 0; i < 10; ++i) {
      mma1(0, i);
      ldop(1, i); if (i < 2) ldop(1, 10 + i);
      __builtin_amdgcn_sched_barrier(0);
    }
    cur = (cur + 1) & (ONBUF - 1);
    b0 = sbase + cur * OSTAGE + boff0; b1 = sbase + cur * OSTAGE + boff1;
    aad += (kt < 9) ? 2048 : 0;
    __builtin_amdgcn_s_waitcnt(0xC07F);
    if (kt < 9) __builtin_amdgcn_s_barrier();
    __builtin_amdgcn_sched_barrier(0);
#pragma unroll
    for (int i = 0; i < 10; ++i) {
      mma1(1, i);
      ldop(0, i); if (i < 2) ldop(0, 10 + i);
      __builtin_amdgcn_sched_barrier(0);
    }
  }
#pragma unroll
  for (int m = 0; m < 2; ++m)
#pragma unroll
    for (int n = 0; n < 5; ++n) {
      u32x2 o = {pk_bf16(acc[m][n][0], acc[m][n][1]), pk_bf16(acc[m][n][2], acc[m][n][3])};
      *(u32x2*)(out + (size_t)(row0 + wm * 32 + m * 16 + fr) * 1280 + 160 * ct + wn * 80 + n * 16 + 4 * g) = o;
    }
}

struct P { const u16 *x, *w, *wo, *u, *lar, *aim, *cr, *ci, *ldt, *dsk; u16* out; char* ws; };
__global__ __launch_bounds__(512, 2) void kmain(P p) {
  extern __shared__ __attribute__((aligned(16))) char smem[];
  const int b = blockIdx.x, tid = threadIdx.x;
  char* ws = p.ws;
  const rsrc_t rs = __builtin_amdgcn_make_buffer_rsrc((void*)ws, 0, WS_SPAN, 0x00020000);
  bool initdone = false;
  TLS(0);
  if (b >= 248) {
    const int part = b - 248;
    for (int i = tid; i < ZERO_BYTES / 128; i += 512) st16_wt(rs, WS_ZERO + part * (ZERO_BYTES / 8) + i * 16, (u32x4){0, 0, 0, 0});
    asm volatile("s_waitcnt vmcnt(0)" ::: "memory");
    __syncthreads();
    if (tid < 256) __hip_atomic_store((gu64*)(ws + WS_FLAG) + tid * 8 + part, MAGIC, RLX_AGENT);
  }
  const KGP kp = {p.lar, p.aim, p.cr, p.ci, p.ldt};
  int bt0 = -1, bt1 = -1, o_task = -1;
  if (b < 192) {
    const int xcd = b & 7, j = b >> 3;
    const int rt = (xcd >> 1) * 4 + (j & 3), c6 = j >> 2;
    if (c6 < 4) { q_block<true>(p.x, p.w, ws, rs, rt, (xcd & 1) * 4 + c6, smem, initdone); if (j < 12) o_task = xcd * 16 + j; else bt0 = xcd * 20 + 16 + (j - 12); }
    else { q_block<false>(p.x, p.w, ws, rs, rt, (xcd & 1) * 2 + (c6 - 4), smem, initdone); bt0 = xcd * 20 + 8 + (j - 16); }
  } else {
    const int ab = b - 192;
    aux_block(p.u, p.wo, ws, rs, ab, smem, initdone);
    { const int xcd = ab & 7, i = ab >> 3; bt0 = xcd * 20 + i; if (i < 4) o_task = xcd * 16 + 12 + i; }
  }
  TLS(2);
  __syncthreads();
#pragma unroll 1
  for (int it = 0; it < 2; ++it) {
    const int t = it == 0 ? bt0 : bt1;
    if (t >= 0) b_block(kp, ws, rs, p.dsk, p.out, t, smem, initdone);
  }
  TLS(5);
  if (o_task >= 0) o_block(ws, rs, p.out, o_task >> 2, o_task & 3, smem, initdone, b >= 192);
  TLS(8);
}

extern "C" void kernel_launch(void* const* d_in, const int* in_sizes, int n_in, void* d_out, int out_size, void* d_ws, size_t ws_size, hipStream_t stream) {
  constexpr int LDS = O_OFF_W + ONBUF * OSTAGE;
  static_assert(QNBUF * QSTAGE <= LDS && 8 * KG_REG <= LDS && B_LDS <= LDS, "LDS");
  (void)hipFuncSetAttribute((const void*)kmain, hipFuncAttributeMaxDynamicSharedMemorySize, LDS);
  P p = {(const u16*)d_in[0], (const u16*)d_in[1], (const u16*)d_in[2], (const u16*)d_in[3], (const u16*)d_in[4], (const u16*)d_in[5], (const u16*)d_in[6], (const u16*)d_in[7], (const u16*)d_in[8], (const u16*)d_in[9], (u16*)d_out, (char*)d_ws};
  kmain<<<256, 512, LDS, stream>>>(p);
}
```

```cpp
#include <hip/hip_runtime.h>
#include <hip/hip_bf16.h>
#include <stdint.h>
#include <type_traits>
typedef unsigned short u16;
typedef unsigned int u32;
typedef float f32x2 __attribute__((ext_vector_type(2)));
typedef float f32x4 __attribute__((ext_vector_type(4)));
typedef float f32x16 __attribute__((ext_vector_type(16)));
typedef short s16x4 __attribute__((ext_vector_type(4)));
typedef short s16x8 __attribute__((ext_vector_type(8)));
typedef _Float16 h8 __attribute__((ext_vector_type(8)));
typedef _Float16 h2 __attribute__((ext_vector_type(2)));
typedef __bf16 bf16x2 __attribute__((ext_vector_type(2)));
typedef u32 u32x2 __attribute__((ext_vector_type(2)));
typedef u32 u32x4 __attribute__((ext_vector_type(4)));
#define DI __device__ __forceinline__
#define LAS __attribute__((address_space(3)))
#define GAS __attribute__((address_space(1)))
typedef GAS unsigned gu32; typedef GAS unsigned long long gu64;
#define RLX_AGENT __ATOMIC_RELAXED, __HIP_MEMORY_SCOPE_AGENT
DI float bf2f(u16 x) { return __uint_as_float(((u32)x) << 16); }
DI u32 pk_bf16(float a, float b) { bf16x2 r = __builtin_convertvector((f32x2){a, b}, bf16x2); return __builtin_bit_cast(u32, r); }
DI u32 pk_f16(float a, float b) { h2 r = __builtin_convertvector((f32x2){a, b}, h2); return __builtin_bit_cast(u32, r); }
DI float lo16(u32 x) { return __uint_as_float(x << 16); }
DI float hi16(u32 x) { return __uint_as_float(x & 0xffff0000u); }
DI float rbf(float f) { return lo16(pk_bf16(f, 0.f)); }
DI u32 phi_pair(float a, float b) {
  u32 t = pk_bf16(a, b);
  float ta = lo16(t), tb = hi16(t);
  float ea = __builtin_amdgcn_fmed3f(__builtin_amdgcn_exp2f(ta * 1.4426950408889634f), 0.f, 1.f);
  float eb = __builtin_amdgcn_fmed3f(__builtin_amdgcn_exp2f(tb * 1.4426950408889634f), 0.f, 1.f);
  return pk_bf16(ea + fmaxf(ta, 1e-6f), eb + fmaxf(tb, 1e-6f));
}
DI f32x2 cmul(f32x2 x, f32x2 y) {
  f32x2 t, z;
  asm("v_pk_mul_f32 %0, %1, %2 op_sel_hi:[0,1]" : "=v"(t) : "v"(x), "v"(y));
  asm("v_pk_fma_f32 %0, %1, %2, %3 op_sel:[1,1,0] op_sel_hi:[1,0,1] neg_lo:[0,1,0]" : "=v"(z) : "v"(x), "v"(y), "v"(t));
  return z;
}
DI f32x2 cexpP(float ar2, float airev, float P) {
  float e = __builtin_amdgcn_exp2f(ar2 * P);
  float t = __builtin_amdgcn_fractf(airev * P);
  float s = __builtin_amdgcn_sinf(t), c = __builtin_amdgcn_cosf(t);
  f32x2 r = {e * c, e * s};
  return r;
}
constexpr int S = 2048, D = 640, NS = 192;
constexpr u32 WS_PQ = 0;
constexpr u32 WS_UT = 2621440;
constexpr u32 WS_WOP = 2 * 2621440;
constexpr u32 WS_FLAG = 2 * 2621440 + 819200;
constexpr u32 WS_ZERO = WS_FLAG + 16384;
constexpr int KVACC_BYTES = 32 * 1024 * 4;
constexpr int CNT_OFF = KVACC_BYTES;
constexpr int CNT_KV = 0, CNT_T = 8, CNT_PQ = 16, CNT_KG = 32, ZPAD = 40;
constexpr int ZERO_BYTES = KVACC_BYTES + 48 * 128;
constexpr u32 WS_SPAN = 7u << 20;
constexpr unsigned long long MAGIC = 0x9e3779b97f4a7c15ull;
constexpr unsigned SPIN_CAP = 1u << 17;
typedef __amdgpu_buffer_rsrc_t rsrc_t;

DI gu32* cnt_ptr(char* ws, int i) { return (gu32*)(ws + WS_ZERO + CNT_OFF + i * 128); }
DI void wait_init_t0(char* ws, bool& done) {
  if (done) return;
  gu64* f = (gu64*)(ws + WS_FLAG) + blockIdx.x * 8;
  for (unsigned sp = 0; sp < SPIN_CAP; ++sp) {
    bool ok = true;
#pragma unroll
    for (int i = 0; i < 8; ++i) ok &= __hip_atomic_load(f + i, RLX_AGENT) == MAGIC;
    if (ok) break;
    __builtin_amdgcn_s_sleep(2);
  }
#pragma unroll
  for (int i = 0; i < 8; ++i) __hip_atomic_store(f + i, 0ull, RLX_AGENT);
  done = true;
}
DI void wait_cnt_t0(char* ws, int i, unsigned want) {
  gu32* c = cnt_ptr(ws, i);
  if (__hip_atomic_load(c, RLX_AGENT) >= want) return;
  unsigned v0, v1, v2, v3;
#define PLD(v) asm volatile("global_load_dword %0, %1, off sc1" : "=v"(v) : "v"(c) : "memory")
  PLD(v0); __builtin_amdgcn_s_sleep(2); PLD(v1); __builtin_amdgcn_s_sleep(2); PLD(v2); __builtin_amdgcn_s_sleep(2);
  for (unsigned sp = 0; sp < SPIN_CAP; ++sp) {
    PLD(v3); asm volatile("s_waitcnt vmcnt(3)" : "+v"(v0) :: "memory"); if (v0 >= want) break; __builtin_amdgcn_s_sleep(2);
    PLD(v0); asm volatile("s_waitcnt vmcnt(3)" : "+v"(v1) :: "memory"); if (v1 >= want) break; __builtin_amdgcn_s_sleep(2);
    PLD(v1); asm volatile("s_waitcnt vmcnt(3)" : "+v"(v2) :: "memory"); if (v2 >= want) break; __builtin_amdgcn_s_sleep(2);
    PLD(v2); asm volatile("s_waitcnt vmcnt(3)" : "+v"(v3) :: "memory"); if (v3 >= want) break; __builtin_amdgcn_s_sleep(2);
  }
  asm volatile("s_waitcnt vmcnt(0)" : "+v"(v0), "+v"(v1), "+v"(v2), "+v"(v3) :: "memory");
#undef PLD
}
DI void publish(char* ws, int i, bool& initdone) {
  asm volatile("s_waitcnt vmcnt(0)" ::: "memory");
  __syncthreads();
  if (threadIdx.x == 0) { wait_init_t0(ws, initdone); __hip_atomic_fetch_add(cnt_ptr(ws, i), 1u, RLX_AGENT); }
}
DI void publish8(char* ws, int i, bool& initdone) {
  if (threadIdx.x == 0) wait_init_t0(ws, initdone);
  asm volatile("s_waitcnt vmcnt(0)" ::: "memory");
  __syncthreads();
  if (threadIdx.x < 8) __hip_atomic_fetch_add(cnt_ptr(ws, i + threadIdx.x), 1u, RLX_AGENT);
}
DI u32x4 ld16_sc(rsrc_t r, u32 off) { return __builtin_amdgcn_raw_buffer_load_b128(r, off, 0, 16); }
DI u32x2 ld8_sc(const void* p) { unsigned long long v = __hip_atomic_load((gu64*)p, RLX_AGENT); return (u32x2){(u32)v, (u32)(v >> 32)}; }
DI float ld4_sc(const void* p) { return __uint_as_float(__hip_atomic_load((gu32*)p, RLX_AGENT)); }
DI void acquire_all() {
  if (threadIdx.x == 0) { __builtin_amdgcn_fence(__ATOMIC_ACQUIRE, "agent"); asm volatile("s_waitcnt vmcnt(0)" ::: "memory"); }
  __syncthreads();
}
DI void st16_wt(rsrc_t r, u32 off, u32x4 v) { __builtin_amdgcn_raw_buffer_store_b128(v, r, off, 0, 16); }
DI void st8_wt(void* p, u32x2 v) { __hip_atomic_store((gu64*)p, ((unsigned long long)v.y << 32) | v.x, RLX_AGENT); }

constexpr int QSTAGE = 36864, QNBUF = 4;
constexpr int TROW = 272, KG_REG = 14720;

DI f32x2 cm(f32x2 x, f32x2 y) {
  f32x2 xx = {x.x, x.x}, xy = {x.y, x.y}, ys = {-y.y, y.x};
  return __builtin_elementwise_fma(xy, ys, xx * y);
}
DI f32x2 cmc(f32x2 x, f32x2 y) {
  f32x2 xx = {x.x, x.x}, xy = {x.y, x.y}, yc = {y.x, -y.y}, ys = {y.y, y.x};
  return __builtin_elementwise_fma(xy, ys, xx * yc);
}
DI void kgen_chunk(float a2, float av, f32x2 Ein, char* reg, f32x16& kacc) {
  const int lane = threadIdx.x & 63;
  u32* tE = (u32*)(reg) + lane;
  u32* tF = (u32*)(reg + 21 * TROW) + lane;
  constexpr int R = TROW / 4;
  auto stE = [&](int r, f32x2 v) { tE[r * R] = pk_f16(v.x, v.y); };
  auto stF = [&](int r, f32x2 v) { tF[r * R] = pk_f16(v.x, v.y); };
  f32x2 w1 = cexpP(a2, av, 1.f), v1 = cexpP(a2, av, 8.f), B256 = cexpP(a2, av, 256.f);
  f32x2 w2 = cm(w1, w1), w4 = cm(w2, w2);
  f32x2 v2 = cm(v1, v1), v4 = cm(v2, v2), v8 = cm(v4, v4), v16 = cm(v8, v8);
  f32x2 B512 = cm(B256, B256), B1024 = cm(B512, B512), B768 = cm(B512, B256);
  f32x2 E0 = {Ein.x, -Ein.y};
  f32x2 E1 = cmc(E0, w1), E2 = cmc(E0, w2), E3 = cmc(E1, w2), E4 = cmc(E0, w4), E5 = cmc(E1, w4), E6 = cmc(E2, w4), E7 = cmc(E3, w4);
  stE(0, E0); stE(1, E1); stE(2, E2); stE(3, E3); stE(4, E4); stE(5, E5); stE(6, E6); stE(7, E7);
  stE(8, cmc(E0, B256)); stE(9, cmc(E2, B256)); stE(10, cmc(E4, B256)); stE(11, cmc(E6, B256));
  stE(12, cmc(E0, B512)); stE(13, cmc(E4, B512)); stE(14, cmc(E0, B768)); stE(15, cmc(E4, B768));
  f32x2 G0 = cmc(E0, B1024);
  stE(16, G0); stE(17, cmc(G0, B256)); stE(18, cmc(G0, B512)); stE(19, cmc(G0, B768)); stE(20, cmc(G0, B1024));
  f32x2 F[8];
  F[0].x = 1.f; F[0].y = 0.f; F[1] = v1; F[2] = v2; F[3] = cm(v2, v1); F[4] = v4; F[5] = cm(v4, v1); F[6] = cm(v4, v2); F[7] = cm(v4, F[3]);
  f32x2 v24 = cm(v16, v8);
#pragma unroll
  for (int i = 0; i < 8; ++i) {
    stF(i, F[i]);
    stF(8 + i, i == 0 ? v8 : cm(F[i], v8));
    stF(16 + i, i == 0 ? v16 : cm(F[i], v16));
    stF(24 + i, i == 0 ? v24 : cm(F[i], v24));
  }
  __builtin_amdgcn_wave_barrier();
  const int r = lane & 31, hh = lane >> 5;
  const char* pa = reg + (r < 21 ? r : 20) * TROW + 16 * hh;
  const char* pb = reg + (21 + r) * TROW + 16 * hh;
#pragma unroll
  for (int s = 0; s < 8; ++s) {
    h8 a = *(const h8*)(pa + 32 * s);
    h8 b = *(const h8*)(pb + 32 * s);
    kacc = __builtin_amdgcn_mfma_f32_32x32x16_f16(a, b, kacc, 0, 0, 0);
  }
  __builtin_amdgcn_wave_barrier();
}

struct KGP { const u16 *lar, *aim, *cr, *ci, *ldt; };
struct KGL { u16 pcr[3], pci[3], plar, paim, pldt; };
DI void kgen_load(const KGP& p, int d, int c, KGL& l) {
  const int lane = threadIdx.x & 63;
#pragma unroll
  for (int q = 0; q < 3; ++q) { int n = d * NS + 64 * q + lane; l.pcr[q] = p.cr[n]; l.pci[q] = p.ci[n]; }
  l.plar = p.lar[d * NS + 64 * c + lane]; l.paim = p.aim[d * NS + 64 * c + lane];
  l.pldt = p.ldt[d];
}
template <class HOOK> DI float kgen_task(const KGL& l, int c, char* reg, float* dst, HOOK hook) {
  const int lane = threadIdx.x & 63;
  const float dt = rbf(expf(bf2f(l.pldt)));
  float cmax = 0.f;
#pragma unroll
  for (int q = 0; q < 3; ++q) cmax = fmaxf(cmax, fmaxf(fabsf(bf2f(l.pcr[q])), fabsf(bf2f(l.pci[q]))));
  cmax *= dt * 2.f;
#pragma unroll
  for (int o = 32; o >= 1; o >>= 1) cmax = fmaxf(cmax, __shfl_xor(cmax, o));
  int ce = (int)((__float_as_uint(cmax) >> 23) & 0xff);
  ce = ce < 30 ? 30 : (ce > 220 ? 220 : ce);
  const float sc = __uint_as_float((u32)(127 + 125 - ce) << 23);
  const float isc = __uint_as_float((u32)(ce + 2) << 23);
  const float Are = -rbf(expf(bf2f(l.plar)));
  const float ar2 = Are * dt * 1.4426950408889634f, airev = bf2f(l.paim) * dt * 0.15915494309189535f;
  const u16 mcr = c == 0 ? l.pcr[0] : (c == 1 ? l.pcr[1] : l.pcr[2]), mci = c == 0 ? l.pci[0] : (c == 1 ? l.pci[1] : l.pci[2]);
  f32x2 E = {bf2f(mcr) * dt * 2.f * sc, bf2f(mci) * dt * 2.f * sc};
  f32x16 kacc;
#pragma unroll
  for (int i = 0; i < 16; ++i) kacc[i] = 0.f;
  hook();
  kgen_chunk(ar2, airev, E, reg, kacc);
  const int i = lane & 31, hh = lane >> 5;
#pragma unroll
  for (int rg = 0; rg < 12; ++rg) {
    const int q = rg >> 2, sub = rg & 3;
    int idx; bool ok = true;
    if (q == 0) idx = 8 * i + 4 * hh + sub;
    else if (q == 1) idx = hh == 0 ? 256 + 4 * i + sub : 384 + 64 * (sub >> 1) + 2 * i + (sub & 1);
    else { idx = hh == 0 ? 512 + 32 * sub + i : 640; ok = (hh == 0) || (sub == 0 && i == 0); }
    if (ok) dst[idx] = kacc[rg];
  }
  return isc * (1.f / 128.f);
}

template <bool KV>
DI void q_block(const u16* __restrict__ x, const u16* __restrict__ w, char* __restrict__ ws, rsrc_t rs, int rt, int ct, char* smem, bool& initdone) {
  const int tid = threadIdx.x, lane = tid & 63, wv = __builtin_amdgcn_readfirstlane(tid >> 6);
  const int row0 = rt * 128;
  const int fr = lane & 15, g = lane >> 4;
  if (wv >= 4) {
    const int lw = wv - 4;
    const u16* xs[4]; const u16* ws_[5];
#pragma unroll
    for (int i = 0; i < 4; ++i) { int pc = lw * 4 + i; int row = 8 * pc + (lane >> 3); int ch = (lane & 7) ^ ((row >> 1) & 7); xs[i] = x + (size_t)(row0 + row) * D + 8 * ch; }
#pragma unroll
    for (int i = 0; i < 5; ++i) {
      int pc = lw * 5 + i; int sb = 4 * pc + (lane >> 4); int kb = sb / 10, nb = sb % 10; int row = ((lane >> 1) & 7) ^ (4 * (kb & 1)); int c = nb * 16 + 8 * (lane & 1);
      int gc;
      if (KV) { int wn2 = c / 80, cc = c % 80; gc = cc < 40 ? 640 + 80 * ct + 40 * wn2 + cc : 1280 + 80 * ct + 40 * wn2 + (cc - 40); }
      else gc = 160 * ct + c;
      ws_[i] = w + (size_t)(8 * kb + row) * 1920 + gc;
    }
    auto stage = [&](int kt) {
      char* b = smem + (kt & (QNBUF - 1)) * QSTAGE;
#pragma unroll
      for (int i = 0; i < 4; ++i) __builtin_amdgcn_global_load_lds((const u32*)(xs[i] + kt * 64), (LAS u32*)(b + (lw * 4 + i) * 1024), 16, 0, 0);
#pragma unroll
      for (int i = 0; i < 5; ++i) __builtin_amdgcn_global_load_lds((const u32*)(ws_[i] + (size_t)kt * 64 * 1920), (LAS u32*)(b + 16384 + (lw * 5 + i) * 1024), 16, 0, 0);
    };
#pragma unroll 1
    for (int i = 0; i < QNBUF; ++i) stage(i);
    asm volatile("s_waitcnt vmcnt(%0)" :: "n"((QNBUF - 1) * 9) : "memory");
    __builtin_amdgcn_s_barrier();
#pragma unroll 1
    for (int kt = 0; kt < 6; ++kt) {
      asm volatile("s_waitcnt vmcnt(18)" ::: "memory");
      __builtin_amdgcn_s_barrier();
      stage(kt + QNBUF);
    }
    asm volatile("s_waitcnt vmcnt(18)" ::: "memory"); __builtin_amdgcn_s_barrier();
    asm volatile("s_waitcnt vmcnt(9)" ::: "memory");  __builtin_amdgcn_s_barrier();
    asm volatile("s_waitcnt vmcnt(0)" ::: "memory");  __builtin_amdgcn_s_barrier();
    if (KV) { __builtin_amdgcn_s_barrier(); __builtin_amdgcn_s_barrier(); }
  } else {
    const int wm = wv >> 1, wn = wv & 1;
    f32x4 acc[4][5];
#pragma unroll
    for (int m = 0; m < 4; ++m)
#pragma unroll
      for (int n = 0; n < 5; ++n) acc[m][n] = (f32x4){0, 0, 0, 0};
    const u32 aoff0 = (wm * 64 + fr) * 128 + (((0 + g) ^ (fr >> 1)) * 16);
    const u32 aoff1 = (wm * 64 + fr) * 128 + (((4 + g) ^ (fr >> 1)) * 16);
    const u32 boff0 = 16384 + g * 2560 + wn * 1280 + ((0 ^ (4 * (g & 1))) + (fr >> 2)) * 32 + (lane & 3) * 8;
    const u32 boff1 = 16384 + g * 2560 + wn * 1280 + ((4 ^ (4 * (g & 1))) + (fr >> 2)) * 32 + (lane & 3) * 8;
    const u32 sbase = (u32)(size_t)smem;
    s16x8 af[2][4]; s16x4 bl[2][5], bh[2][5];
    u32 a0 = sbase + aoff0, a1 = sbase + aoff1, b0 = sbase + boff0, b1 = sbase + boff1;
    auto ldop = [&](int ks, int i) {
      if (i < 10) {
        const int n = i >> 1;
        if (i & 1) bh[ks][n] = __builtin_amdgcn_ds_read_tr16_b64_v4i16((LAS s16x4*)(b1 + ks * 10240 + n * 256));
        else       bl[ks][n] = __builtin_amdgcn_ds_read_tr16_b64_v4i16((LAS s16x4*)(b0 + ks * 10240 + n * 256));
      } else af[ks][i - 10] = *(const LAS s16x8*)((ks ? a1 : a0) + (i - 10) * 2048);
    };
    auto mma1 = [&](int sl, int i) {
      const int m = i / 5, n = i % 5;
      s16x8 bb = __builtin_shufflevector(bl[sl][n], bh[sl][n], 0, 1, 2, 3, 4, 5, 6, 7);
      acc[m][n] = KV ? __builtin_amdgcn_mfma_f32_16x16x32_bf16(af[sl][m], bb, acc[m][n], 0, 0, 0)
                     : __builtin_amdgcn_mfma_f32_16x16x32_bf16(bb, af[sl][m], acc[m][n], 0, 0, 0);
    };
    __builtin_amdgcn_s_barrier();
#pragma unroll
    for (int i = 0; i < 14; ++i) ldop(0, i);
    int cur = 0;
    unsigned long long fl = MAGIC;
    gu64* fp = (gu64*)(ws + WS_FLAG) + blockIdx.x * 8 + (lane & 7);
#pragma unroll 1
    for (int kt = 0; kt < 10; ++kt) {
      if (kt == 6 && wv == 0) asm volatile("global_load_dwordx2 %0, %1, off sc1" : "=v"(fl) : "v"(fp) : "memory");
      __builtin_amdgcn_sched_barrier(0);
#pragma unroll
      for (int i = 0; i < 20; ++i) {
        mma1(0, i);
        if (i < 14) ldop(1, i);
        __builtin_amdgcn_sched_barrier(0);
      }
      cur = (cur + 1) & (QNBUF - 1);
      const u32 nb = sbase + cur * QSTAGE;
      a0 = nb + aoff0; a1 = nb + aoff1; b0 = nb + boff0; b1 = nb + boff1;
      __builtin_amdgcn_s_waitcnt(0xC07F);
      if (kt < 9) __builtin_amdgcn_s_barrier();
      __builtin_amdgcn_sched_barrier(0);
#pragma unroll
      for (int i = 0; i < 20; ++i) {
        mma1(1, i);
        if (i < 14) ldop(0, i);
        __builtin_amdgcn_sched_barrier(0);
      }
    }
    if (wv == 0) {
      asm volatile("s_waitcnt vmcnt(0)" : "+v"(fl) :: "memory");
      if (__all(fl == MAGIC)) { if (lane < 8) __hip_atomic_store(fp, 0ull, RLX_AGENT); }
      else if (lane == 0) { bool d = false; wait_init_t0(ws, d); }
    }
    if (!KV) {
      char* pq = ws + WS_PQ;
#pragma unroll
      for (int m = 0; m < 4; ++m)
#pragma unroll
        for (int n = 0; n < 5; ++n) {
          u32x2 o = {phi_pair(acc[m][n][0], acc[m][n][1]), phi_pair(acc[m][n][2], acc[m][n][3])};
          const int col = 160 * ct + wn * 80 + n * 16 + 4 * g; const int h = col / 20, q = (col % 20) >> 2;
          st8_wt(pq + 2 * (((size_t)((rt * 8 + wm * 4 + m) * 32 + h) * 5 + q) * 64 + fr * 4), o);
        }
    } else {
      u32x2 pk[4][5];
      const bool isk = fr < 8;
#pragma unroll
      for (int m = 0; m < 4; ++m)
#pragma unroll
        for (int n = 0; n < 5; ++n) {
          if (n <= 2) { pk[m][n].x = phi_pair(acc[m][n][0], acc[m][n][1]); pk[m][n].y = phi_pair(acc[m][n][2], acc[m][n][3]); }
          if (n >= 2) {
            u32 v0 = pk_bf16(acc[m][n][0], acc[m][n][1]), v1 = pk_bf16(acc[m][n][2], acc[m][n][3]);
            if (n > 2) { pk[m][n].x = v0; pk[m][n].y = v1; } else { pk[m][n].x = isk ? pk[m][n].x : v0; pk[m][n].y = isk ? pk[m][n].y : v1; }
          }
        }
      f32x4 kv[10];
#pragma unroll
      for (int i = 0; i < 10; ++i) kv[i] = (f32x4){0, 0, 0, 0};
      const s16x8 ones = {0x3f80, 0x3f80, 0x3f80, 0x3f80, 0x3f80, 0x3f80, 0x3f80, 0x3f80};
#pragma unroll
      for (int u = 0; u < 2; ++u) {
        s16x8 f[5];
#pragma unroll
        for (int n = 0; n < 5; ++n) f[n] = __builtin_bit_cast(s16x8, (u32x4){pk[2 * u][n].x, pk[2 * u][n].y, pk[2 * u + 1][n].x, pk[2 * u + 1][n].y});
        kv[0] = __builtin_amdgcn_mfma_f32_16x16x32_bf16(f[0], f[2], kv[0], 0, 0, 0);
        kv[1] = __builtin_amdgcn_mfma_f32_16x16x32_bf16(f[0], f[3], kv[1], 0, 0, 0);
        kv[2] = __builtin_amdgcn_mfma_f32_16x16x32_bf16(f[1], f[2], kv[2], 0, 0, 0);
        kv[3] = __builtin_amdgcn_mfma_f32_16x16x32_bf16(f[1], f[3], kv[3], 0, 0, 0);
        kv[4] = __builtin_amdgcn_mfma_f32_16x16x32_bf16(f[1], f[4], kv[4], 0, 0, 0);
        kv[5] = __builtin_amdgcn_mfma_f32_16x16x32_bf16(f[2], f[3], kv[5], 0, 0, 0);
        kv[6] = __builtin_amdgcn_mfma_f32_16x16x32_bf16(f[2], f[4], kv[6], 0, 0, 0);
        kv[7] = __builtin_amdgcn_mfma_f32_16x16x32_bf16(f[0], ones, kv[7], 0, 0, 0);
        kv[8] = __builtin_amdgcn_mfma_f32_16x16x32_bf16(f[1], ones, kv[8], 0, 0, 0);
        kv[9] = __builtin_amdgcn_mfma_f32_16x16x32_bf16(f[2], ones, kv[9], 0, 0, 0);
      }
      __builtin_amdgcn_s_barrier();
      f32x4* red = (f32x4*)smem + (wm * 2 + wn) * 640 + lane;
#pragma unroll
      for (int i = 0; i < 10; ++i) red[i * 64] = kv[i];
      __builtin_amdgcn_s_waitcnt(0xC07F);
      __builtin_amdgcn_s_barrier();
    }
  }
  initdone = true;
  if (KV) {
    const float* raw = (const float*)smem;
    float* acc = (float*)(ws + WS_ZERO) + (size_t)ct * 4096 + tid;
#pragma unroll
    for (int it = 0; it < 8; ++it) {
      const int e0 = it * 512 + tid;
      const int hl4 = it >> 1, tile = it & 1, lp = (e0 >> 3) & 63, j = e0 & 7;
      const int row = lp & 15, gk = lp >> 4, gg = row >> 2, r = row & 3;
      const int wn = hl4 >> 1, hl = hl4 & 1;
      const int dd = 8 * gk + j;
      if ((tile == 0 || r < 2) && dd < 20) {
        const int d = dd + 20 * hl, dt = d >> 4;
        const int e = (tile == 0 ? 5 * gg + r : 5 * gg + 3 + r) + 20 * hl + 40, et = e >> 4;
        int t, ln;
        if (tile == 0 && r == 3) { t = 7 + dt; ln = ((d & 15) >> 2) * 16; }
        else { t = dt == 0 ? et - 2 : (dt == 1 ? et : et + 2); ln = ((d & 15) >> 2) * 16 + (e & 15); }
        const int idx = (wn * 640 + t * 64 + ln) * 4 + (d & 3);
        const float v = raw[idx] + raw[2 * 640 * 4 + idx];
        __hip_atomic_fetch_add((GAS float*)(acc + it * 512), v, RLX_AGENT);
      }
    }
    publish8(ws, CNT_KV, initdone);
  } else {
    publish(ws, CNT_PQ + rt, initdone);
  }
}

DI void aux_block(const u16* __restrict__ u, const u16* __restrict__ wo, char* __restrict__ ws, rsrc_t rs, int ab, char* smem, bool& initdone) {
  const int tid = threadIdx.x, lane = tid & 63, wv = __builtin_amdgcn_readfirstlane(tid >> 6);
  char* reg = smem + wv * KG_REG;
  if (wv < 5) {
    const int tile = ab * 5 + wv; const int rb = tile / 10, cb = tile % 10;
    u16* t = (u16*)reg;
#pragma unroll
    for (int i = 0; i < 8; ++i) {
      const int row = i * 8 + (lane >> 3), ch = lane & 7;
      u32x4 v = *(const u32x4*)(u + (size_t)(rb * 64 + row) * D + cb * 64 + ch * 8);
      u32* p = (u32*)(t + row * 66 + ch * 8);
      p[0] = v.x; p[1] = v.y; p[2] = v.z; p[3] = v.w;
    }
    __builtin_amdgcn_wave_barrier();
#pragma unroll
    for (int i = 0; i < 8; ++i) {
      const int c = i * 8 + (lane >> 3), r8 = lane & 7;
      u32 o[4];
#pragma unroll
      for (int k = 0; k < 4; ++k) o[k] = (u32)t[(r8 * 8 + 2 * k) * 66 + c] | ((u32)t[(r8 * 8 + 2 * k + 1) * 66 + c] << 16);
      st16_wt(rs, WS_UT + ((cb * 64 + c) * S + rb * 64 + r8 * 8) * 2, (u32x4){o[0], o[1], o[2], o[3]});
    }
    __builtin_amdgcn_wave_barrier();
  } else {
#pragma unroll
    for (int it = 0; it < 5; ++it) {
      const int uu = (wv - 5) * 64 + lane + 192 * it;
      if (uu < 800) {
        const int U = ab * 800 + uu; const int img = U / 1280, un = U % 1280; const int ct = img / 10, kt = img % 10;
        const int sb = un >> 4, kb = sb / 10, nb = sb % 10, j = ((un >> 1) & 7) ^ (4 * (kb & 1));
        const int T = 2 * kt + (kb >> 2), G = T / 5, tt = T % 5, ii = 8 * tt + j, hh = ii / 5, r = ii % 5;
        const int k = 160 * G + 20 * hh + 5 * (kb & 3) + r;
        st16_wt(rs, WS_WOP + U * 16, *(const u32x4*)(wo + (size_t)k * D + 160 * ct + nb * 16 + 8 * (un & 1)));
      }
    }
  }
  publish8(ws, CNT_T, initdone);
}

constexpr int UC_STRIDE = 4160, UCH = 4 * UC_STRIDE, KP_STRIDE = 1280, KPCH = 4 * KP_STRIDE;
constexpr int UC0 = 4 * KG_REG, PART0 = UC0 + 4 * UCH, KC0 = PART0 + 12 * 2592, RED0 = KC0 + 4 * 1312;
constexpr int B_LDS = RED0 + 128;
constexpr int PSPLIT = 22;
DI void b_block(const KGP& kp, char* __restrict__ ws, rsrc_t rs, const u16* __restrict__ dsk_, u16* __restrict__ out, int cb, char* smem, bool& initdone) {
  const int tid = threadIdx.x, lane = tid & 63, wv = __builtin_amdgcn_readfirstlane(tid >> 6), ch = wv & 3, hf = wv >> 2;
  const int d0 = cb * 4;
  float* red = (float*)(smem + RED0);
  float* part = (float*)(smem + PART0);
  KGL l0, l1;
  kgen_load(kp, d0 + wv / 3, wv % 3, l0);
  kgen_load(kp, d0 + (8 + (wv & 3)) / 3, (8 + (wv & 3)) % 3, l1);
  float dk[4];
#pragma unroll
  for (int c = 0; c < 4; ++c) dk[c] = bf2f(dsk_[d0 + c]);
  u32x4 uv[4];
  unsigned tc = 0;
  gu32* tcp = cnt_ptr(ws, CNT_T + (blockIdx.x & 7));
  if (wv >= 4) asm volatile("global_load_dword %0, %1, off sc1" : "=v"(tc) : "v"(tcp) : "memory");
  {
    float ks = kgen_task(l0, wv % 3, smem + wv * KG_REG, part + wv * 648, [&]() {
      if (wv >= 4) {
        asm volatile("s_waitcnt vmcnt(0)" : "+v"(tc) :: "memory");
        if (tc < 64) { if (lane == 0) wait_cnt_t0(ws, CNT_T + (blockIdx.x & 7), 64); __builtin_amdgcn_wave_barrier(); }
#pragma unroll
        for (int i = 0; i < 4; ++i) uv[i] = ld16_sc(rs, WS_UT + ((d0 + wv - 4) * S + 8 * (lane + 64 * i)) * 2);
      }
    });
    if (lane == 0 && wv % 3 == 0) red[8 + wv / 3] = ks;
  }
  __syncthreads();
  if (wv < 4) {
    float ks = kgen_task(l1, (8 + wv) % 3, smem + wv * KG_REG, part + (8 + wv) * 648, []() {});
    if (lane == 0 && wv == 1) red[8 + 3] = ks;
  } else {
    const int c = wv - 4;
    char* ur = smem + UC0 + c * UCH;
    float m = 0.f;
#pragma unroll
    for (int i = 0; i < 4; ++i)
#pragma unroll
      for (int k = 0; k < 4; ++k) m = fmaxf(m, fmaxf(fabsf(lo16(uv[i][k])), fabsf(hi16(uv[i][k]))));
#pragma unroll
    for (int o = 32; o >= 1; o >>= 1) m = fmaxf(m, __shfl_xor(m, o));
    int e = (int)((__float_as_uint(m) >> 23) & 0xff);
    e = e < 30 ? 30 : (e > 220 ? 220 : e);
    const float us = __uint_as_float((u32)(127 + 140 - e) << 23);
    if (lane == 0) red[c] = __uint_as_float((u32)(e - 13) << 23);
#pragma unroll
    for (int i = 0; i < 4; ++i) {
      const int q = lane + 64 * i;
      u32x4 o = {pk_f16(hi16(uv[i].w) * us, lo16(uv[i].w) * us), pk_f16(hi16(uv[i].z) * us, lo16(uv[i].z) * us),
                 pk_f16(hi16(uv[i].y) * us, lo16(uv[i].y) * us), pk_f16(hi16(uv[i].x) * us, lo16(uv[i].x) * us)};
      *(u32x4*)(ur + 2 * (2040 - 8 * q)) = o;
    }
    if (lane < 4) *(u32x4*)(ur + 4096 + 16 * lane) = (u32x4){0, 0, 0, 0};
    __builtin_amdgcn_wave_barrier();
#pragma unroll
    for (int k = 0; k < 5; ++k) {
      const int q = lane + 64 * k;
      if (q < 260) {
        u32x4 a = *(const u32x4*)(ur + 16 * q);
        u32x2 b = *(const u32x2*)(ur + 16 * q + 16);
        u32x4 c1 = {__builtin_amdgcn_alignbit(a.y, a.x, 16), __builtin_amdgcn_alignbit(a.z, a.y, 16), __builtin_amdgcn_alignbit(a.w, a.z, 16), __builtin_amdgcn_alignbit(b.x, a.w, 16)};
        u32x4 c2 = {a.y, a.z, a.w, b.x};
        u32x4 c3 = {c1.y, c1.z, c1.w, __builtin_amdgcn_alignbit(b.y, b.x, 16)};
        *(u32x4*)(ur + 1 * UC_STRIDE + 16 * q) = c1;
        *(u32x4*)(ur + 2 * UC_STRIDE + 16 * q) = c2;
        *(u32x4*)(ur + 3 * UC_STRIDE + 16 * q) = c3;
      }
    }
  }
  __syncthreads();
  {
    u16* kc = (u16*)(smem + KC0 + ch * 1312);
    const float* p0 = part + ch * 3 * 648;
#pragma unroll
    for (int i = 0; i < 2; ++i) {
      const int ci = hf * 81 + 64 * i + lane;
      if (lane + 64 * i < 81) {
        f32x4 v = *(const f32x4*)(p0 + ci * 4) + *(const f32x4*)(p0 + 648 + ci * 4) + *(const f32x4*)(p0 + 1296 + ci * 4);
        *(u32x2*)(kc + ci * 4) = (u32x2){pk_f16(v[0] * 128.f, v[1] * 128.f), pk_f16(v[2] * 128.f, v[3] * 128.f)};
      }
    }
  }
  __syncthreads();
  {
    const u16* kc = (const u16*)(smem + KC0 + ch * 1312);
    char* kpl = smem + ch * KPCH;
    if (hf == 1 && lane < 60) *(u32x4*)(kpl + (lane & 3) * KP_STRIDE + (lane >> 2) * 16) = (u32x4){0, 0, 0, 0};
#pragma unroll
    for (int k = 0; k < 2; ++k) {
      const int q = hf + 2 * (lane + 64 * k);
      u32 v[8];
#pragma unroll
      for (int j8 = 0; j8 < 8; ++j8) {
        int j = 8 * q + j8;
        u32 fb = __float_as_uint((float)j); fb += 0x7fffu + ((fb >> 16) & 1u);
        int idx = j < 256 ? j : 256 + (int)((fb >> 16) - 0x4380u);
        v[j8] = kc[idx];
      }
      u32x4 o = {v[0] | (v[1] << 16), v[2] | (v[3] << 16), v[4] | (v[5] << 16), v[6] | (v[7] << 16)};
      *(u32x4*)(kpl + (q & 3) * KP_STRIDE + ((q >> 2) + 15) * 16) = o;
    }
  }
  __syncthreads();
  f32x4 acc[4][2];
#pragma unroll
  for (int t = 0; t < 4; ++t) { acc[t][0] = (f32x4){0, 0, 0, 0}; acc[t][1] = (f32x4){0, 0, 0, 0}; }
  const int b = lane & 15, g = lane >> 4;
  char* ureg = smem + UC0 + ch * UCH;
  {
    const int cpy = (3 - b) & 3;
    const char* pa0 = ureg + cpy * UC_STRIDE + 2 * (2044 - 4 * (b >> 2) + 8 * g);
    const char* pb0 = smem + ch * KPCH + g * KP_STRIDE + 16 * (b + 15);
    typedef const volatile LAS u32x2* lp2;
    typedef const volatile LAS u32x4* lp4;
    const u32 abase = (u32)(size_t)(pa0 - 64 * 63 - 32);
    const u32 bbase = (u32)(size_t)(pb0 - 16 * 63);
    constexpr int DP = 2;
    u32x2 Ab[DP + 1][4]; u32x4 Bb[DP + 1][4];
    auto run = [&](auto P0c, auto P1c) {
      constexpr int P0 = decltype(P0c)::value, P1 = decltype(P1c)::value;
      auto issue = [&](int p) {
        const int sl = p % (DP + 1);
        const u32 oa = 64 * (63 - p);
        Ab[sl][0] = *(lp2)(abase + oa + 32); Ab[sl][1] = *(lp2)(abase + oa + 40);
        Ab[sl][2] = *(lp2)(abase + oa);      Ab[sl][3] = *(lp2)(abase + oa + 8);
#pragma unroll
        for (int t = 0; t < 4; ++t) if (t >= (p >> 4)) Bb[sl][t] = *(lp4)(bbase + 16 * (63 - p) + 256 * t);
      };
#pragma unroll
      for (int p = P0; p < P0 + DP; ++p) issue(p);
#pragma unroll
      for (int p = P0; p < P1; ++p) {
        if (p + DP < P1) issue(p + DP);
        __builtin_amdgcn_sched_barrier(0);
        const int sl = p % (DP + 1);
        h8 A0 = __builtin_bit_cast(h8, (u32x4){Ab[sl][0].x, Ab[sl][0].y, Ab[sl][1].x, Ab[sl][1].y});
        h8 A1 = __builtin_bit_cast(h8, (u32x4){Ab[sl][2].x, Ab[sl][2].y, Ab[sl][3].x, Ab[sl][3].y});
#pragma unroll
        for (int t = 0; t < 4; ++t) if (t >= (p >> 4)) {
          h8 B = __builtin_bit_cast(h8, Bb[sl][t]);
          acc[t][0] = __builtin_amdgcn_mfma_f32_16x16x32_f16(A0, B, acc[t][0], 0, 0, 0);
          acc[t][1] = __builtin_amdgcn_mfma_f32_16x16x32_f16(A1, B, acc[t][1], 0, 0, 0);
        }
        __builtin_amdgcn_sched_barrier(0);
      }
    };
    if (hf == 0) run(std::integral_constant<int, 0>{}, std::integral_constant<int, PSPLIT>{});
    else run(std::integral_constant<int, PSPLIT>{}, std::integral_constant<int, 64>{});
  }
  f32x4* xch = (f32x4*)(smem + PART0 + ch * 6144) + lane;
  if (hf == 1) { xch[0 * 64] = acc[1][0]; xch[1 * 64] = acc[1][1]; }
  else { xch[2 * 64] = acc[2][0]; xch[3 * 64] = acc[2][1]; xch[4 * 64] = acc[3][0]; xch[5 * 64] = acc[3][1]; }
  __syncthreads();
  {
    const float ysc = red[8 + ch] * red[ch];
    char* yb = smem + ch * 4096;
    f32x4 a[2][2];
    if (hf == 0) { a[0][0] = acc[0][0]; a[0][1] = acc[0][1]; a[1][0] = acc[1][0] + xch[0 * 64]; a[1][1] = acc[1][1] + xch[1 * 64]; }
    else { a[0][0] = acc[2][0] + xch[2 * 64]; a[0][1] = acc[2][1] + xch[3 * 64]; a[1][0] = acc[3][0] + xch[4 * 64]; a[1][1] = acc[3][1] + xch[5 * 64]; }
#pragma unroll
    for (int t = 0; t < 2; ++t)
#pragma unroll
      for (int r = 0; r < 2; ++r) {
        u32x2 o = {pk_bf16(a[t][r][0] * ysc, a[t][r][1] * ysc), pk_bf16(a[t][r][2] * ysc, a[t][r][3] * ysc)};
        *(u32x2*)(yb + 2 * (32 * (16 * (2 * hf + t) + b) + 16 * r + 4 * g)) = o;
      }
  }
  __syncthreads();
  {
    u32x2 yv[4], uu[4]; float iu[4];
#pragma unroll
    for (int c = 0; c < 4; ++c) {
      yv[c] = *(const u32x2*)(smem + c * 4096 + 8 * tid);
      uu[c] = *(const u32x2*)(smem + UC0 + c * UCH + 2 * (2044 - 4 * tid));
      iu[c] = red[c];
    }
#pragma unroll
    for (int i = 0; i < 4; ++i) {
      float r[4];
#pragma unroll
      for (int c = 0; c < 4; ++c) {
        u32 yw = yv[c][i >> 1]; float y = (i & 1) ? hi16(yw) : lo16(yw);
        const u32 uw = (i < 2) ? uu[c].y : uu[c].x; const u16 ub = (i & 1) ? (u16)uw : (u16)(uw >> 16); float uf = (float)__builtin_bit_cast(_Float16, ub) * iu[c];
        r[c] = y + rbf(uf * dk[c]);
      }
      u32x2 o = {pk_bf16(r[0], r[1]), pk_bf16(r[2], r[3])};
      *(u32x2*)(out + (size_t)(4 * tid + i) * 1280 + 640 + d0) = o;
    }
  }
  __builtin_amdgcn_s_waitcnt(0xC07F);
  __builtin_amdgcn_s_barrier();
}

constexpr int OSTAGE = 20480, ONBUF = 4, O_OFF_W = 81920;
DI void o_block(char* __restrict__ ws, rsrc_t rs, u16* __restrict__ out, int rt, int ct, char* smem, bool& initdone, bool late) {
  const int tid = threadIdx.x, lane = tid & 63, wv = __builtin_amdgcn_readfirstlane(tid >> 6);
  const int row0 = rt * 64, fr = lane & 15, g = lane >> 4;
  const int lw = wv - 4;
  u32* rdy = (u32*)(smem + 163824);
  if (tid == 0) {
    wait_init_t0(ws, initdone);
    if (late) {
      const unsigned a = __hip_atomic_load(cnt_ptr(ws, CNT_T + (blockIdx.x & 7)), RLX_AGENT), bq = __hip_atomic_load(cnt_ptr(ws, CNT_PQ + (rt >> 1)), RLX_AGENT), ck = __hip_atomic_load(cnt_ptr(ws, CNT_KV + (blockIdx.x & 7)), RLX_AGENT);
      if (a < 64) wait_cnt_t0(ws, CNT_T + (blockIdx.x & 7), 64);
      rdy[0] = (bq >= 4 ? 1u : 0u) | (ck >= 128 ? 2u : 0u);
    } else {
      wait_cnt_t0(ws, CNT_T + (blockIdx.x & 7), 64);
      rdy[0] = 0u;
    }
  }
  acquire_all();
  const u32 ready = __builtin_amdgcn_readfirstlane(rdy[0]);
  __builtin_amdgcn_s_waitcnt(0xC07F);
  __builtin_amdgcn_s_barrier();
  const char* wsrc = ws + WS_WOP + (size_t)ct * 10 * OSTAGE + lw * 5120 + lane * 16;
  auto stage = [&](int kt) {
    char* b = smem + O_OFF_W + (kt & (ONBUF - 1)) * OSTAGE + lw * 5120;
#pragma unroll
    for (int i = 0; i < 5; ++i) __builtin_amdgcn_global_load_lds((const u32*)(wsrc + kt * OSTAGE + i * 1024), (LAS u32*)(b + i * 1024), 16, 0, 0);
  };
  if (wv >= 4) {
#pragma unroll 1
    for (int i = 0; i < ONBUF; ++i) stage(i);
  }
  if (!(ready & 1)) {
    if (tid == 0) wait_cnt_t0(ws, CNT_PQ + (rt >> 1), 4);
    asm volatile("s_waitcnt lgkmcnt(0)" ::: "memory");
    __builtin_amdgcn_s_barrier();
  }
  const char* pq = ws + WS_PQ;
  const char* zp = ws + WS_ZERO + CNT_OFF + ZPAD * 128;
  u32x4 kr[4][2][2]; u32x2 pa[4][4], pb[4][4];
#pragma unroll
  for (int hh = 0; hh < 4; ++hh)
#pragma unroll
    for (int st = 0; st < 4; ++st) {
      const char* p = pq + 2 * (((size_t)((rt * 4 + st) * 32 + 4 * wv + hh) * 5) * 64 + fr * 4);
      pa[hh][st] = ld8_sc(g < 3 ? p + 2 * g * 128 : zp);
      pb[hh][st] = ld8_sc(g < 2 ? p + (2 * g + 1) * 128 : zp);
    }
  if (!(ready & 2)) {
    if (tid == 0) wait_cnt_t0(ws, CNT_KV + (blockIdx.x & 7), 128);
    asm volatile("s_waitcnt lgkmcnt(0)" ::: "memory");
    __builtin_amdgcn_s_barrier();
  }
#pragma unroll
  for (int hh = 0; hh < 4; ++hh)
#pragma unroll
    for (int tl = 0; tl < 2; ++tl) {
      kr[hh][tl][0] = ld16_sc(rs, WS_ZERO + (((4 * wv + hh) * 2 + tl) * 64 + lane) * 32);
      kr[hh][tl][1] = ld16_sc(rs, WS_ZERO + (((4 * wv + hh) * 2 + tl) * 64 + lane) * 32 + 16);
    }
  __builtin_amdgcn_sched_barrier(0);
  {
    s16x8 kf[4][2];
#pragma unroll
    for (int hh = 0; hh < 4; ++hh)
#pragma unroll
      for (int tl = 0; tl < 2; ++tl) {
        f32x4 a = __builtin_bit_cast(f32x4, kr[hh][tl][0]), b = __builtin_bit_cast(f32x4, kr[hh][tl][1]);
        kf[hh][tl] = __builtin_bit_cast(s16x8, (u32x4){pk_bf16(a[0], a[1]), pk_bf16(a[2], a[3]), pk_bf16(b[0], b[1]), pk_bf16(b[2], b[3])});
      }
    asm volatile("" :: "v"(kf[3][1]), "v"(pa[3][3]), "v"(pb[3][3]));
    const int G = wv >> 1, half = wv & 1;
#pragma unroll
    for (int st = 0; st < 4; ++st) {
      float vals[20];
#pragma unroll
      for (int hh = 0; hh < 4; ++hh) {
        s16x8 pf = __builtin_bit_cast(s16x8, (u32x4){pa[hh][st].x, pa[hh][st].y, pb[hh][st].x, pb[hh][st].y});
        f32x4 z4 = {0, 0, 0, 0};
        f32x4 t0 = __builtin_amdgcn_mfma_f32_16x16x32_bf16(kf[hh][0], pf, z4, 0, 0, 0);
        f32x4 t1 = __builtin_amdgcn_mfma_f32_16x16x32_bf16(kf[hh][1], pf, z4, 0, 0, 0);
        float den = rbf(rbf(t0[3]) + 1e-6f);
        float rd = __builtin_amdgcn_rcpf(den);
        vals[5 * hh + 0] = t0[0] * rd; vals[5 * hh + 1] = t0[1] * rd; vals[5 * hh + 2] = t0[2] * rd;
        vals[5 * hh + 3] = t1[0] * rd; vals[5 * hh + 4] = t1[1] * rd;
      }
#pragma unroll
      for (int i = 0; i < 5; ++i) {
        const int byte = 40 * half + 8 * i;
        const int T = 5 * G + (byte >> 4);
        *(u32x2*)(smem + (st * 20 + T) * 1024 + lane * 16 + (byte & 8)) = (u32x2){pk_bf16(vals[4 * i], vals[4 * i + 1]), pk_bf16(vals[4 * i + 2], vals[4 * i + 3])};
      }
    }
  }
  if (wv >= 4) {
    asm volatile("s_waitcnt vmcnt(0) lgkmcnt(0)" ::: "memory");
    __builtin_amdgcn_s_barrier();
#pragma unroll 1
    for (int kt = 0; kt < 6; ++kt) {
      asm volatile("s_waitcnt vmcnt(10)" ::: "memory");
      __builtin_amdgcn_s_barrier();
      stage(kt + ONBUF);
    }
    asm volatile("s_waitcnt vmcnt(10)" ::: "memory"); __builtin_amdgcn_s_barrier();
    asm volatile("s_waitcnt vmcnt(5)" ::: "memory");  __builtin_amdgcn_s_barrier();
    asm volatile("s_waitcnt vmcnt(0)" ::: "memory");  __builtin_amdgcn_s_barrier();
    return;
  }
  const int wm = wv >> 1, wn = wv & 1;
  f32x4 acc[2][5];
#pragma unroll
  for (int m = 0; m < 2; ++m)
#pragma unroll
    for (int n = 0; n < 5; ++n) acc[m][n] = (f32x4){0, 0, 0, 0};
  const u32 sbase = (u32)(size_t)smem;
  const u32 boff0 = O_OFF_W + g * 2560 + wn * 1280 + ((0 ^ (4 * (g & 1))) + (fr >> 2)) * 32 + (lane & 3) * 8;
  const u32 boff1 = O_OFF_W + g * 2560 + wn * 1280 + ((4 ^ (4 * (g & 1))) + (fr >> 2)) * 32 + (lane & 3) * 8;
  u32 aad = sbase + wm * 2 * 20 * 1024 + lane * 16;
  u32 b0 = sbase + boff0, b1 = sbase + boff1;
  s16x8 af[2][2]; s16x4 bl[2][5], bh[2][5];
  auto ldop = [&](int ks, int i) {
    if (i < 10) {
      const int n = i >> 1;
      if (i & 1) bh[ks][n] = __builtin_amdgcn_ds_read_tr16_b64_v4i16((LAS s16x4*)(b1 + ks * 10240 + n * 256));
      else       bl[ks][n] = __builtin_amdgcn_ds_read_tr16_b64_v4i16((LAS s16x4*)(b0 + ks * 10240 + n * 256));
    } else af[ks][i - 10] = *(const LAS s16x8*)(aad + ((i - 10) * 20 + ks) * 1024);
  };
  auto mma1 = [&](int sl, int i) {
    const int m = i / 5, n = i % 5;
    s16x8 bb = __builtin_shufflevector(bl[sl][n], bh[sl][n], 0, 1, 2, 3, 4, 5, 6, 7);
    acc[m][n] = __builtin_amdgcn_mfma_f32_16x16x32_bf16(bb, af[sl][m], acc[m][n], 0, 0, 0);
  };
  __builtin_amdgcn_s_waitcnt(0xC07F);
  __builtin_amdgcn_s_barrier();
#pragma unroll
  for (int i = 0; i < 12; ++i) ldop(0, i);
  int cur = 0;
#pragma unroll 1
  for (int kt = 0; kt < 10; ++kt) {
    __builtin_amdgcn_sched_barrier(0);
#pragma unroll
    for (int i = 0; i < 10; ++i) {
      mma1(0, i);
      ldop(1, i); if (i < 2) ldop(1, 10 + i);
      __builtin_amdgcn_sched_barrier(0);
    }
    cur = (cur + 1) & (ONBUF - 1);
    b0 = sbase + cur * OSTAGE + boff0; b1 = sbase + cur * OSTAGE + boff1;
    aad += (kt < 9) ? 2048 : 0;
    __builtin_amdgcn_s_waitcnt(0xC07F);
    if (kt < 9) __builtin_amdgcn_s_barrier();
    __builtin_amdgcn_sched_barrier(0);
#pragma unroll
    for (int i = 0; i < 10; ++i) {
      mma1(1, i);
      ldop(0, i); if (i < 2) ldop(0, 10 + i);
      __builtin_amdgcn_sched_barrier(0);
    }
  }
#pragma unroll
  for (int m = 0; m < 2; ++m)
#pragma unroll
    for (int n = 0; n < 5; ++n) {
      u32x2 o = {pk_bf16(acc[m][n][0], acc[m][n][1]), pk_bf16(acc[m][n][2], acc[m][n][3])};
      *(u32x2*)(out + (size_t)(row0 + wm * 32 + m * 16 + fr) * 1280 + 160 * ct + wn * 80 + n * 16 + 4 * g) = o;
    }
}

struct P { const u16 *x, *w, *wo, *u, *lar, *aim, *cr, *ci, *ldt, *dsk; u16* out; char* ws; };
__global__ __launch_bounds__(512, 2) void kmain(P p) {
  extern __shared__ __attribute__((aligned(16))) char smem[];
  const int b = blockIdx.x, tid = threadIdx.x;
  char* ws = p.ws;
  const rsrc_t rs = __builtin_amdgcn_make_buffer_rsrc((void*)ws, 0, WS_SPAN, 0x00020000);
  bool initdone = false;
  if (b >= 248) {
    const int part = b - 248;
    for (int i = tid; i < ZERO_BYTES / 128; i += 512) st16_wt(rs, WS_ZERO + part * (ZERO_BYTES / 8) + i * 16, (u32x4){0, 0, 0, 0});
    asm volatile("s_waitcnt vmcnt(0)" ::: "memory");
    __syncthreads();
    if (tid < 256) __hip_atomic_store((gu64*)(ws + WS_FLAG) + tid * 8 + part, MAGIC, RLX_AGENT);
  }
  const KGP kp = {p.lar, p.aim, p.cr, p.ci, p.ldt};
  int bt0 = -1, bt1 = -1, o_task = -1;
  if (b < 192) {
    const int xcd = b & 7, j = b >> 3;
    const int rt = (xcd >> 1) * 4 + (j & 3), c6 = j >> 2;
    if (c6 < 4) { q_block<true>(p.x, p.w, ws, rs, rt, (xcd & 1) * 4 + c6, smem, initdone); if (j < 12) o_task = xcd * 16 + j; else bt0 = xcd * 20 + 16 + (j - 12); }
    else { q_block<false>(p.x, p.w, ws, rs, rt, (xcd & 1) * 2 + (c6 - 4), smem, initdone); bt0 = xcd * 20 + 8 + (j - 16); }
  } else {
    const int ab = b - 192;
    aux_block(p.u, p.wo, ws, rs, ab, smem, initdone);
    { const int xcd = ab & 7, i = ab >> 3; bt0 = xcd * 20 + i; if (i < 4) o_task = xcd * 16 + 12 + i; }
  }
  __syncthreads();
#pragma unroll 1
  for (int it = 0; it < 2; ++it) {
    const int t = it == 0 ? bt0 : bt1;
    if (t >= 0) b_block(kp, ws, rs, p.dsk, p.out, t, smem, initdone);
  }
  if (o_task >= 0) o_block(ws, rs, p.out, o_task >> 2, o_task & 3, smem, initdone, b >= 192);
}

extern "C" void kernel_launch(void* const* d_in, const int* in_sizes, int n_in, void* d_out, int out_size, void* d_ws, size_t ws_size, hipStream_t stream) {
  constexpr int LDS = O_OFF_W + ONBUF * OSTAGE;
  static_assert(QNBUF * QSTAGE <= LDS && 8 * KG_REG <= LDS && B_LDS <= LDS, "LDS");
  (void)hipFuncSetAttribute((const void*)kmain, hipFuncAttributeMaxDynamicSharedMemorySize, LDS);
  P p = {(const u16*)d_in[0], (const u16*)d_in[1], (const u16*)d_in[2], (const u16*)d_in[3], (const u16*)d_in[4], (const u16*)d_in[5], (const u16*)d_in[6], (const u16*)d_in[7], (const u16*)d_in[8], (const u16*)d_in[9], (u16*)d_out, (char*)d_ws};
  kmain<<<256, 512, LDS, stream>>>(p);
}
```
